```python
import torch

def _softmax(x, axis=-1):
    return torch.softmax(x, dim=axis)
def _gelu(x):
    return torch.nn.functional.gelu(x, approximate="tanh")
def _silu(x):
    return torch.nn.functional.silu(x)
def _sigmoid(x):
    return torch.sigmoid(x)
def _layernorm(x, axis=-1, eps=1e-5):
    m = x.mean(dim=axis, keepdim=True)
    v = x.var(dim=axis, keepdim=True, unbiased=False)
    return (x - m) / torch.sqrt(v + eps)
def _scatter_add(tgt, idx, src, axis=0):
    return tgt.clone().index_add_(axis, idx, src)
def _segment_sum(src, seg, n):
    out = torch.zeros((n,) + src.shape[1:], dtype=src.dtype)
    return out.index_add_(0, seg, src)
def _pad(x, pw):
    flat = tuple(v for pair in reversed(pw) for v in pair)
    return torch.nn.functional.pad(x, flat)

kernel_name = 'recurrent_scan__sliding_block_cached__swiglu_ffn__decode'

def setup_inputs():

    _w = (lambda a: (lambda t: t.to(torch.bfloat16) if t.is_floating_point() else t)(torch.as_tensor(a)))
    def _A_setup():
        B, Sq, D = 1, 1, 448
        Hq, Hkv, Sk, Dh = 16, 8, 65536, 256
        assert Hq % Hkv == 0, (Hq, Hkv)
        return {
            "x": _w((lambda *s: torch.randn(*s, dtype=torch.bfloat16) * 0.05)(B, Sq, D)),
            "k_cache": _w((lambda *s: torch.randn(*s, dtype=torch.bfloat16) * 1.6)(B, Hkv, Sk, Dh)),
            "v_cache": _w((lambda *s: torch.randn(*s, dtype=torch.bfloat16) * 0.2)(B, Hkv, Sk, Dh)),
            "wq": _w((lambda *s: torch.randn(*s, dtype=torch.bfloat16) * 1.6)(D, Hq * Dh) * (D ** -0.5)),
            "wo": _w((lambda *s: torch.randn(*s, dtype=torch.bfloat16) * 0.2)(Hq * Dh, D) * ((Hq * Dh) ** -0.5)),
        }
    _A_in = _A_setup()

    def _B_setup():
        B, S, D, F = 1, 1, 448, 1792
        return {
            "x": _w((lambda *s: torch.randn(*s, dtype=torch.bfloat16) * 0.05)(B, S, D)),
            "w_gate": _w((lambda *s: torch.randn(*s, dtype=torch.bfloat16) * 0.1)(D, F)),
            "w_up": _w((lambda *s: torch.randn(*s, dtype=torch.bfloat16) * 0.1)(D, F)),
            "w_down": _w((lambda *s: torch.randn(*s, dtype=torch.bfloat16) * 0.1)(F, D)),
        }
    _B_in = _B_setup()

    return {
        "A_x": _A_in["x"],
        "A_k_cache": _A_in["k_cache"],
        "A_v_cache": _A_in["v_cache"],
        "A_wq": _A_in["wq"],
        "A_wo": _A_in["wo"],
        "B_x": _B_in["x"],
        "B_w_gate": _B_in["w_gate"],
        "B_w_up": _B_in["w_up"],
        "B_w_down": _B_in["w_down"],
    }

def reference(A_x, A_k_cache, A_v_cache, A_wq, A_wo, B_x, B_w_gate, B_w_up, B_w_down):
    def _A_ref(x, k_cache, v_cache, wq, wo):
        B, Sq, D = x.shape
        Hq, Dh = 16, 256
        Hkv, Sk = k_cache.shape[1], k_cache.shape[2]
        W = min(4096, max(256, Sk // 4))
        rep = Hq // Hkv
        q = (lambda x, *d: x.permute(*d))((x @ wq).reshape(B, Sq, Hq, Dh), 0, 2, 1, 3)
        k = (lambda x, n, axis: torch.repeat_interleave(x, n, dim=axis))(k_cache[:, :, Sk - W:, :], rep, axis=1)
        v = (lambda x, n, axis: torch.repeat_interleave(x, n, dim=axis))(v_cache[:, :, Sk - W:, :], rep, axis=1)
        scores = torch.einsum("bhqd,bhkd->bhqk", q, k) * (Dh ** -0.5)
        p = _softmax(scores, axis=-1)
        o = torch.einsum("bhqk,bhkd->bhqd", p, v)
        return (lambda x, *d: x.permute(*d))(o, 0, 2, 1, 3).reshape(B, Sq, Hq * Dh) @ wo

    def _B_ref(x, w_gate, w_up, w_down):
        g = _silu(x @ w_gate)
        u = x @ w_up
        return (g * u) @ w_down

    x = A_x
    xn = _layernorm(x, axis=-1)
    s = _A_ref(xn, A_k_cache, A_v_cache, A_wq, A_wo)
    g = _silu(_B_ref(xn, B_w_gate, B_w_up, B_w_down))
    return x + s * g
```

```cpp
#include <hip/hip_runtime.h>
#include <hip/hip_bf16.h>
#include <stdint.h>
#define MIMIC 1
typedef unsigned short u16;
typedef unsigned v4u __attribute__((ext_vector_type(4)));
constexpr int D = 448, HQ = 16, HKV = 8, SK = 65536, DH = 256, W = 4096, F = 1792;
__device__ __forceinline__ float bf2f(unsigned b) { return __builtin_bit_cast(float, b << 16); }
__device__ __forceinline__ float rbf(float f) {
  return __bfloat162float(__float2bfloat16(f));
}
__device__ __forceinline__ float wsum(float v) { for (int o = 32; o > 0; o >>= 1) v += __shfl_xor(v, o); return v; }
__device__ __forceinline__ float wmax(float v) { for (int o = 32; o > 0; o >>= 1) v = fmaxf(v, __shfl_xor(v, o)); return v; }

__device__ void layernorm(const u16* x, float* xn  ) {
  const int tid = threadIdx.x;
  if (tid < 64) {
    float v[7]; float s = 0;
    for (int i = 0; i < 7; ++i) { v[i] = bf2f(x[tid + 64 * i]); s += v[i]; }
    const float mean = wsum(s) * (1.f / D);
    float q = 0; for (int i = 0; i < 7; ++i) { float d = v[i] - mean; q += d * d; }
    const float var = wsum(q) * (1.f / D);
    const float mb = rbf(mean), vb = rbf(var);
    const float den = rbf(sqrtf(rbf(vb + 1e-5f)));
    for (int i = 0; i < 7; ++i) xn[tid + 64 * i] = rbf(rbf(v[i] - mb) / den);
  }
  __syncthreads();
}
__device__ void gemv64(const u16* Wm, int N, int col0, const float* xn, float* red, float* res) {
  const int tid = threadIdx.x, c = tid & 7, r = tid >> 3;
  float acc[8] = {0};
  for (int i = 0; i < 14; ++i) {
    const int d = r + 32 * i;
    const v4u w = *(const v4u*)(Wm + (size_t)d * N + col0 + 8 * c);
    const float xv = xn[d];
    for (int k = 0; k < 4; ++k) { acc[2 * k] += xv * bf2f(w[k] & 0xffffu); acc[2 * k + 1] += xv * bf2f(w[k] >> 16); }
  }
  for (int k = 0; k < 8; ++k) red[r * 64 + 8 * c + k] = acc[k];
  __syncthreads();
  if (tid < 64) { float s = 0; for (int i = 0; i < 32; ++i) s += red[i * 64 + tid]; res[tid] = s; }
  __syncthreads();
}
__global__ void __launch_bounds__(256) k1(const u16* x, const u16* wq, const u16* wg, const u16* wu, float* q, float* h) {
  __shared__ float xn[D], red[32 * 64], ra[64], rb[64];
  layernorm(x, xn);
  const int b = blockIdx.x, tid = threadIdx.x;
  if (b < 64) { gemv64(wq, HQ * DH, 64 * b, xn, red, ra); if (tid < 64) q[64 * b + tid] = rbf(ra[tid]); }
  else { const int c0 = 64 * (b - 64); gemv64(wg, F, c0, xn, red, ra); gemv64(wu, F, c0, xn, red, rb);
    if (tid < 64) { const float g = rbf(ra[tid]), u = rbf(rb[tid]); const float sg = rbf(g / (1.f + __expf(-g))); h[c0 + tid] = rbf(sg * u); } }
}
__global__ void __launch_bounds__(256) k2(const u16* kc, const u16* vc, const float* q, float* po, float* pml) {
  __shared__ float qs[512], sc[2][128], ml[2][2];
  const int b = blockIdx.x, h = b & 7, j = b >> 3, tid = threadIdx.x, lane = tid & 63, w = tid >> 6;
  qs[tid] = q[512 * h + tid]; qs[tid + 256] = q[512 * h + 256 + tid];
  __syncthreads();
  const u16* K = kc + ((size_t)h * SK + (SK - W) + 128 * j) * DH;
  const u16* V = vc + ((size_t)h * SK + (SK - W) + 128 * j) * DH;
  for (int i = 0; i < 16; ++i) {
    const int key = 32 * w + 2 * i + (lane >> 5), c = lane & 31;
    const v4u kv = *(const v4u*)(K + (size_t)key * DH + 8 * c);
    float a0 = 0, a1 = 0;
    for (int k = 0; k < 4; ++k) { const float lo = bf2f(kv[k] & 0xffffu), hi = bf2f(kv[k] >> 16);
      a0 += lo * qs[8 * c + 2 * k] + hi * qs[8 * c + 2 * k + 1]; a1 += lo * qs[256 + 8 * c + 2 * k] + hi * qs[256 + 8 * c + 2 * k + 1]; }
    for (int o = 16; o > 0; o >>= 1) { a0 += __shfl_xor(a0, o); a1 += __shfl_xor(a1, o); }
    if (c == 0) { sc[0][key] = rbf(a0) * 0.0625f; sc[1][key] = rbf(a1) * 0.0625f; }
  }
  __syncthreads();
  if (w < 2) { const float s0 = sc[w][lane], s1 = sc[w][lane + 64]; const float m = wmax(fmaxf(s0, s1));
    const float p0 = __expf(s0 - m), p1 = __expf(s1 - m); sc[w][lane] = p0; sc[w][lane + 64] = p1; const float l = wsum(p0 + p1);
    if (lane == 0) { pml[(b * 2 + w) * 2] = m; pml[(b * 2 + w) * 2 + 1] = l; } }
  __syncthreads();
  float o0 = 0, o1 = 0;
  for (int key = 0; key < 128; ++key) { const float v = bf2f(V[(size_t)key * DH + tid]); o0 += sc[0][key] * v; o1 += sc[1][key] * v; }
  po[(size_t)b * 512 + tid] = o0; po[(size_t)b * 512 + 256 + tid] = o1;
}
__global__ void __launch_bounds__(448) k3(const float* po, const float* pml, const u16* wo, const float* hh, const u16* wd, float* ps, float* pf) {
  __shared__ float o[16];
  const int b = blockIdx.x, h = b & 7, j = b >> 3, tid = threadIdx.x, a = j >> 4;
  if (tid < 16) {
    float M = -INFINITY; for (int s = 0; s < 32; ++s) M = fmaxf(M, pml[((s * 8 + h) * 2 + a) * 2]);
    float L = 0, acc = 0;
    for (int s = 0; s < 32; ++s) { const float wt = __expf(pml[((s * 8 + h) * 2 + a) * 2] - M); L += wt * pml[((s * 8 + h) * 2 + a) * 2 + 1]; acc += wt * po[(size_t)(s * 8 + h) * 512 + 16 * j + tid]; }
    o[tid] = rbf(acc / L);
  }
  __syncthreads();
  float s = 0; for (int r = 0; r < 16; ++r) s += o[r] * bf2f(wo[(size_t)(512 * h + 16 * j + r) * D + tid]);
  ps[(size_t)b * D + tid] = s;
  float f = 0; for (int r = 0; r < 7; ++r) f += hh[7 * b + r] * bf2f(wd[(size_t)(7 * b + r) * D + tid]);
  pf[(size_t)b * D + tid] = f;
}
__global__ void __launch_bounds__(256) k4(const u16* x, const float* ps, const float* pf, __hip_bfloat16* out) {
  __shared__ float rs[4][16], rf[4][16];
  const int c0 = 16 * blockIdx.x, tid = threadIdx.x, c = tid & 15, r = tid >> 4;
  float s = 0, f = 0;
  for (int i = 0; i < 16; ++i) { s += ps[(size_t)(r + 16 * i) * D + c0 + c]; f += pf[(size_t)(r + 16 * i) * D + c0 + c]; }
  s += __shfl_xor(s, 16); s += __shfl_xor(s, 32); f += __shfl_xor(f, 16); f += __shfl_xor(f, 32);
  if ((tid & 63) < 16) { rs[tid >> 6][c] = s; rf[tid >> 6][c] = f; }
  __syncthreads();
  if (tid < 16) { s = rbf(rs[0][c] + rs[1][c] + rs[2][c] + rs[3][c]); f = rbf(rf[0][c] + rf[1][c] + rf[2][c] + rf[3][c]);
    const float g = rbf(f / (1.f + __expf(-f))); out[c0 + c] = __float2bfloat16(bf2f(x[c0 + c]) + rbf(s * g)); }
}
extern "C" void kernel_launch(void* const* d_in, const int* in_sizes, int n_in, void* d_out, int out_size, void* d_ws, size_t ws_size, hipStream_t stream) {
  const u16* x = (const u16*)d_in[0]; const u16* kc = (const u16*)d_in[1]; const u16* vc = (const u16*)d_in[2];
  const u16* wq = (const u16*)d_in[3]; const u16* wo = (const u16*)d_in[4];
  const u16* wg = (const u16*)d_in[6]; const u16* wu = (const u16*)d_in[7]; const u16* wd = (const u16*)d_in[8];
  float* ws = (float*)d_ws;
  float* q = ws; float* h = ws + 4096; float* pml = ws + 8192; float* po = ws + 16384; float* ps = po + 256 * 512; float* pf = ps + 256 * D;
  k1<<<64 + 28, 256, 0, stream>>>(x, wq, wg, wu, q, h);
  k2<<<256, 256, 0, stream>>>(kc, vc, q, po, pml);
  k3<<<256, 448, 0, stream>>>(po, pml, wo, h, wd, ps, pf);
  k4<<<28, 256, 0, stream>>>(x, ps, pf, (__hip_bfloat16*)d_out);
}
```

```cpp
#include <hip/hip_runtime.h>
#include <stdint.h>

#define GAS __attribute__((address_space(1)))
#define LAS __attribute__((address_space(3)))
typedef unsigned v4u __attribute__((ext_vector_type(4)));
typedef unsigned v2u __attribute__((ext_vector_type(2)));
typedef float f32x4 __attribute__((ext_vector_type(4)));
typedef __bf16 bf16x8 __attribute__((ext_vector_type(8)));
typedef __bf16 bf16x2 __attribute__((ext_vector_type(2)));
#define RLX_AGENT __ATOMIC_RELAXED, __HIP_MEMORY_SCOPE_AGENT

constexpr int DM = 448, FF = 1792, SK = 65536, WIN = 4096, DH = 256;
constexpr int NWG = 256, NTHR = 576;
constexpr int WS_Q = 4096;
constexpr int WS_GU = WS_Q + 8 * 8192;
constexpr int WS_ML = WS_GU + 28 * 4096;
constexpr int O_PITCH = 4352;
constexpr int WS_O = WS_ML + 8192;
constexpr int S_PITCH = 3584;
constexpr int WS_S = WS_O + 256 * O_PITCH;
constexpr int WS_F = WS_S + 128 * S_PITCH;
constexpr int WS_END = WS_F + 112 * S_PITCH;
constexpr int L_XS = 0;
constexpr int L_Q = 256;
constexpr int L_P = 512;
constexpr int L_O = 768;
constexpr int L_W = 800;
constexpr int L_WM = 832;
constexpr int L_WL = 840;
constexpr int L_X = 848;
constexpr int L_RED = 864;
constexpr int L_FLAG = 928;
constexpr int L_TOTAL = 21504;
constexpr unsigned SPIN_CAP = 1u << 16;

#define STAMP(k)
#define STAMP_DECL
#define STAMP_FLUSH()

__device__ __forceinline__ float asf(unsigned u) { return __builtin_bit_cast(float, u); }
__device__ __forceinline__ unsigned asu(float f) { return __builtin_bit_cast(unsigned, f); }
__device__ __forceinline__ float rbf(float f) { return (float)(__bf16)f; }
__device__ __forceinline__ unsigned pk2(float lo, float hi) { bf16x2 t = {(__bf16)lo, (__bf16)hi}; return __builtin_bit_cast(unsigned, t); }
__device__ __forceinline__ float dot2(unsigned a, unsigned b, float c) {
  return __builtin_amdgcn_fdot2_f32_bf16(__builtin_bit_cast(bf16x2, a), __builtin_bit_cast(bf16x2, b), c, false);
}
__device__ __forceinline__ f32x4 mfma(v4u a, v4u b, f32x4 c) {
  return __builtin_amdgcn_mfma_f32_16x16x32_bf16(__builtin_bit_cast(bf16x8, a), __builtin_bit_cast(bf16x8, b), c, 0, 0, 0);
}
template <int CTRL> __device__ __forceinline__ float dpp(float x) {
  return asf((unsigned)__builtin_amdgcn_mov_dpp((int)asu(x), CTRL, 0xf, 0xf, true));
}
constexpr int XOR1 = 0xB1, XOR2 = 0x4E, XOR7 = 0x141, XOR8 = 0x128;
__device__ __forceinline__ float x16_sum(float x) { auto s = __builtin_amdgcn_permlane16_swap(asu(x), asu(x), false, false); return asf(s[0]) + asf(s[1]); }
__device__ __forceinline__ float x32_sum(float x) { auto s = __builtin_amdgcn_permlane32_swap(asu(x), asu(x), false, false); return asf(s[0]) + asf(s[1]); }
__device__ __forceinline__ float x16_max(float x) { auto s = __builtin_amdgcn_permlane16_swap(asu(x), asu(x), false, false); return fmaxf(asf(s[0]), asf(s[1])); }
__device__ __forceinline__ float x32_max(float x) { auto s = __builtin_amdgcn_permlane32_swap(asu(x), asu(x), false, false); return fmaxf(asf(s[0]), asf(s[1])); }
__device__ __forceinline__ float wave_sum(float x) { x += dpp<XOR1>(x); x += dpp<XOR2>(x); x += dpp<XOR7>(x); x += dpp<XOR8>(x); x = x16_sum(x); return x32_sum(x); }
__device__ __forceinline__ __amdgpu_buffer_rsrc_t mkrs(const GAS char* p, int bytes) { return __builtin_amdgcn_make_buffer_rsrc((void*)p, 0, bytes, 0x00020000); }
__device__ __forceinline__ v4u ldnt(__amdgpu_buffer_rsrc_t rs, int voff, int soff) {
  const v4u r = __builtin_amdgcn_raw_buffer_load_b128(rs, voff, soff, 2);
  __builtin_amdgcn_sched_barrier(0);
  return r;
}
__device__ __forceinline__ v4u ldsc(__amdgpu_buffer_rsrc_t rs, int off) { return __builtin_amdgcn_raw_buffer_load_b128(rs, off, 0, 16); }
__device__ __forceinline__ void stsc(__amdgpu_buffer_rsrc_t rs, int off, float v0, float v1, unsigned tag) {
  v4u g = {asu(v0), tag, asu(v1), tag};
  __builtin_amdgcn_raw_buffer_store_b128(g, rs, off, 0, 16);
}
#define BARRIER() asm volatile("s_waitcnt lgkmcnt(0)\n\ts_barrier" ::: "memory")
#define LGKM0() asm volatile("s_waitcnt lgkmcnt(0)" ::: "memory")
#define CFENCE() asm volatile("" ::: "memory")
__device__ __forceinline__ v4u amask(unsigned x, const v4u& m) { return v4u{x & m.x, x & m.y, x & m.z, x & m.w}; }
__device__ __forceinline__ void fixup(const f32x4& A, const f32x4& B, bool nlo, float& v0, float& v1) {
  float s[4];
#pragma unroll
  for (int r = 0; r < 4; ++r) {
    auto t = __builtin_amdgcn_permlane32_swap(asu(A[r]), asu(B[r]), false, false);
    const float a = asf(t[0]), b = asf(t[1]);
    s[r] = r < 2 ? a + dpp<XOR8>(b) : dpp<XOR8>(a) + b;
  }
  v0 = nlo ? s[0] : s[2];
  v1 = nlo ? s[1] : s[3];
}
template <int N> __device__ __forceinline__ void poll(__amdgpu_buffer_rsrc_t rs, int off, int stride, unsigned tag, v4u (&v)[N]) {
#pragma unroll 1
  for (unsigned spins = 0; spins < SPIN_CAP; ++spins) {
    CFENCE();
    bool ok = true;
#pragma unroll
    for (int k = 0; k < N; ++k) v[k] = __builtin_amdgcn_raw_buffer_load_b128(rs, off, k * stride, 16);
#pragma unroll
    for (int k = 0; k < N; ++k) ok &= (v[k].y == tag) & (v[k].w == tag);
    if (__all(ok)) break;
    __builtin_amdgcn_s_sleep(1);
  }
}
__device__ __forceinline__ void lds_wait(volatile LAS unsigned* w, unsigned want) {
#pragma unroll 1
  for (unsigned spins = 0; spins < (SPIN_CAP << 6); ++spins) {
    if (__builtin_amdgcn_readfirstlane(*w) >= want) break;
    __builtin_amdgcn_s_sleep(1);
  }
  CFENCE();
}
__device__ __forceinline__ float silu(float x) { return x / (1.f + __expf(-x)); }

__global__ void __launch_bounds__(NTHR) fused_decode(const GAS char* __restrict__ x, const GAS char* __restrict__ kc, const GAS char* __restrict__ vc,
                                                     const GAS char* __restrict__ wq, const GAS char* __restrict__ wo, const GAS char* __restrict__ wg,
                                                     const GAS char* __restrict__ wu, const GAS char* __restrict__ wd, GAS char* out, GAS char* ws) {
  __shared__ __attribute__((aligned(16))) unsigned lds_[L_TOTAL];
  LAS unsigned* L = (LAS unsigned*)lds_;
  volatile LAS unsigned* FL = (volatile LAS unsigned*)(L + L_FLAG);
  const int b = blockIdx.x, h = b & 7, j = b >> 3;
  const int tid = threadIdx.x, lane = tid & 63, w = __builtin_amdgcn_readfirstlane(tid >> 6);
  const bool isE = (j & 1) == 0;
  const int ei = j >> 1;
  const int fi = h + 8 * (j >> 1);
  const bool ffn = !isE && fi < 112;
  const bool fin = !isE && fi >= 100;
  const int cg = fi >> 2, tq = fi & 3, fc = fi - 100;
  const int rs = lane >> 3, c8 = lane & 7, g4 = lane >> 4, n16 = lane & 15;
  const bool nlo = n16 < 8;
  const int gidx = 4 * c8 + 2 * (g4 & 1) + (n16 >> 3);
  const __amdgpu_buffer_rsrc_t rw = __builtin_amdgcn_make_buffer_rsrc((void*)ws, 0, WS_END, 0x00020000);
  const unsigned hm = (c8 & 1) ? 0xffff0000u : 0x0000ffffu;
  const v4u msk = {(c8 >> 1) == 0 ? hm : 0u, (c8 >> 1) == 1 ? hm : 0u, (c8 >> 1) == 2 ? hm : 0u, (c8 >> 1) == 3 ? hm : 0u};
  constexpr float C2 = 0.0625f * 1.4426950408889634f;
  STAMP_DECL;

  if (w == 0) {
    STAMP(0);
    __builtin_amdgcn_s_setprio(3);
    const unsigned ep = __builtin_amdgcn_raw_buffer_load_b32(rw, 0, 0, 16);
    const v4u xr = __builtin_amdgcn_raw_buffer_load_b128(mkrs(x, 896), lane * 16, 0, 0);
    __builtin_amdgcn_sched_barrier(0);
    const bool ph1 = isE || ffn;
    v4u fr[28];
    const int r0 = isE ? 224 * (ei & 1) : 112 * tq;
    {
      const int pitch = isE ? 8192 : 3584;
      const int sb = r0 * pitch + (isE ? (512 * h + 64 * (ei >> 1)) * 2 : cg * 128);
      const int nb = ph1 ? DM * pitch : 0;
      const __amdgpu_buffer_rsrc_t ra = mkrs(isE ? wq : wg, nb), rb = mkrs(isE ? wq : wu, nb);
      const int sb1 = isE ? sb + 112 * 8192 : sb;
      const int vo = rs * pitch + c8 * 16;
#pragma unroll
      for (int i = 0; i < 14; ++i) fr[i] = ldnt(ra, vo, sb + i * 8 * pitch);
#pragma unroll
      for (int i = 0; i < 14; ++i) fr[14 + i] = ldnt(rb, vo, sb1 + i * 8 * pitch);
    }
    if (lane < 4) FL[lane] = 0u;
    __builtin_amdgcn_sched_barrier(0);
    BARRIER();
    __builtin_amdgcn_sched_barrier(0);
    const unsigned tag = ep + 1u;
    float xv[8];
#pragma unroll
    for (int k = 0; k < 4; ++k) { xv[2 * k] = asf(xr[k] << 16); xv[2 * k + 1] = asf(xr[k] & 0xffff0000u); }
    float s1 = ((xv[0] + xv[1]) + (xv[2] + xv[3])) + ((xv[4] + xv[5]) + (xv[6] + xv[7]));
    const float mean = wave_sum(s1) * (1.f / DM);
    float s2 = 0.f;
#pragma unroll
    for (int k = 0; k < 8; ++k) { const float d = xv[k] - mean; s2 += d * d; }
    s2 = lane < 56 ? s2 : 0.f;
    const float var = wave_sum(s2) * (1.f / DM);
    const float mb = rbf(mean), den = rbf(sqrtf(rbf(rbf(var) + 1e-5f)));
    if (lane == 0) FL[3] = tag;
    if (fin && (lane >> 1) == fc) *(LAS v4u*)(L + L_X + 4 * (lane & 1)) = xr;
    STAMP(1);
    if (ph1) {
      const int li = lane - (r0 >> 3);
      if (li >= 0 && li < (isE ? 28 : 14)) {
        const int hf = li >= 14 ? 1 : 0, ii = li - 14 * hf;
#pragma unroll
        for (int k = 0; k < 8; ++k) { const float xn = rbf(xv[k] - mb) / den; L[L_XS + hf * 128 + k * 16 + ii] = pk2(xn, xn); }
      }
      LGKM0();
      f32x4 a0 = {0.f, 0.f, 0.f, 0.f};
      {
        v4u xs[4];
#pragma unroll
        for (int k = 0; k < 4; ++k) xs[k] = *(const LAS v4u*)(L + L_XS + rs * 16 + 4 * k);
#pragma unroll
        for (int i = 0; i < 14; ++i) a0 = mfma(amask(xs[i >> 2][i & 3], msk), fr[i], a0);
      }
      f32x4 a1 = isE ? a0 : f32x4{0.f, 0.f, 0.f, 0.f};
      {
        v4u xs[4];
#pragma unroll
        for (int k = 0; k < 4; ++k) xs[k] = *(const LAS v4u*)(L + L_XS + (isE ? 128 : 0) + rs * 16 + 4 * k);
#pragma unroll
        for (int i = 0; i < 14; ++i) a1 = mfma(amask(xs[i >> 2][i & 3], msk), fr[14 + i], a1);
      }
      if (isE) a0 = a1;
      float v0, v1;
      fixup(a0, a1, nlo, v0, v1);
      if (isE) { if (lane < 32) stsc(rw, WS_Q + h * 8192 + (ei & 1) * 4096 + (ei >> 1) * 512 + gidx * 16, v0, v1, tag); }
      else stsc(rw, WS_GU + cg * 4096 + tq * 1024 + (lane >> 5) * 512 + gidx * 16, v0, v1, tag);
    }
    STAMP(2);
    {
      v4u qv[8];
      poll<8>(rw, WS_Q + h * 8192 + lane * 16, 1024, tag, qv);
#pragma unroll
      for (int k = 0; k < 4; ++k) L[L_Q + 64 * k + lane] = pk2(asf(qv[k].x) + asf(qv[k + 4].x), asf(qv[k].z) + asf(qv[k + 4].z));
      LGKM0();
      if (lane == 0) FL[0] = 1u;
    }
    STAMP(3);
    if (ffn) {
      v4u gu[1];
      poll<1>(rw, WS_GU + cg * 4096 + rs * 512 + tq * 128 + c8 * 16, 0, tag, gu);
      float y0 = x32_sum(x16_sum(asf(gu[0].x))), y1 = x32_sum(x16_sum(asf(gu[0].z)));
      y0 = rbf(y0); y1 = rbf(y1);
      const float u0 = dpp<XOR8>(y0), u1 = dpp<XOR8>(y1);
      const float h0 = rbf(silu(y0)) * u0, h1 = rbf(silu(y1)) * u1;
      if (lane < 8) {
        const int r = 2 * c8;
        L[L_O + (r & 7) * 4 + (r >> 3)] = pk2(h0, h0);
        L[L_O + ((r + 1) & 7) * 4 + (r >> 3)] = pk2(h1, h1);
      }
    }
    lds_wait(FL + 2, 4u);
    if (lane < 2) {
      const float M = fmaxf(fmaxf(asf(L[L_WM + lane]), asf(L[L_WM + 2 + lane])), fmaxf(asf(L[L_WM + 4 + lane]), asf(L[L_WM + 6 + lane])));
      const float Ls = (asf(L[L_WL + lane]) + asf(L[L_WL + 2 + lane])) + (asf(L[L_WL + 4 + lane]) + asf(L[L_WL + 6 + lane]));
      stsc(rw, WS_ML + (h * 2 + lane) * 512 + j * 16, M, Ls, tag);
    }
    STAMP(4);
    BARRIER();
    STAMP(5);
    if (isE) {
      v4u ml[1];
      poll<1>(rw, WS_ML + (h * 2 + (ei >> 3)) * 512 + (lane & 31) * 16, 0, tag, ml);
      const float m = asf(ml[0].x);
      float M = m; M = fmaxf(M, dpp<XOR1>(M)); M = fmaxf(M, dpp<XOR2>(M)); M = fmaxf(M, dpp<XOR7>(M)); M = fmaxf(M, dpp<XOR8>(M)); M = x16_max(M);
      const float e = __builtin_amdgcn_exp2f((m - M) * C2);
      float Ls = e * asf(ml[0].z); Ls += dpp<XOR1>(Ls); Ls += dpp<XOR2>(Ls); Ls += dpp<XOR7>(Ls); Ls += dpp<XOR8>(Ls); Ls = x16_sum(Ls);
      if (lane < 32) L[L_W + (lane & 3) * 8 + (lane >> 2)] = asu(e / Ls);
      LGKM0();
      const v4u w0 = *(const LAS v4u*)(L + L_W + g4 * 8), w1 = *(const LAS v4u*)(L + L_W + g4 * 8 + 4);
      STAMP(6);
      v4u ov[8];
      poll<8>(rw, WS_O + (h + 8 * g4) * O_PITCH + ei * 256 + n16 * 16, 32 * O_PITCH, tag, ov);
      STAMP(7);
      float o0 = 0.f, o1 = 0.f;
#pragma unroll
      for (int k = 0; k < 8; ++k) { const float wk = asf(k < 4 ? w0[k & 3] : w1[k & 3]); o0 = fmaf(wk, asf(ov[k].x), o0); o1 = fmaf(wk, asf(ov[k].z), o1); }
      o0 = x32_sum(x16_sum(o0)); o1 = x32_sum(x16_sum(o1));
      if (lane < 16) { const int r = 2 * lane;
        L[L_O + (r & 7) * 4 + (r >> 3)] = pk2(o0, o0);
        L[L_O + ((r + 1) & 7) * 4 + (r >> 3)] = pk2(o1, o1); }
      BARRIER();
    } else if (fin) {
      v4u fv[14];
      poll<14>(rw, WS_F + rs * S_PITCH + fc * 128 + c8 * 16, 8 * S_PITCH, tag, fv);
      float f0 = 0.f, f1 = 0.f;
#pragma unroll
      for (int k = 0; k < 14; ++k) { f0 += asf(fv[k].x); f1 += asf(fv[k].z); }
      f0 += dpp<XOR8>(f0); f1 += dpp<XOR8>(f1);
      f0 = x32_sum(x16_sum(f0)); f1 = x32_sum(x16_sum(f1));
      const float g0 = rbf(silu(rbf(f0))), g1 = rbf(silu(rbf(f1)));
      const unsigned xw = L[L_X + c8];
      STAMP(6);
      BARRIER();
      const v2u r0v = *(const LAS v2u*)(L + L_RED + 2 * c8), r1v = *(const LAS v2u*)(L + L_RED + 16 + 2 * c8),
                r2v = *(const LAS v2u*)(L + L_RED + 32 + 2 * c8), r3v = *(const LAS v2u*)(L + L_RED + 48 + 2 * c8);
      const float s0 = rbf((asf(r0v.x) + asf(r1v.x)) + (asf(r2v.x) + asf(r3v.x))), s1v = rbf((asf(r0v.y) + asf(r1v.y)) + (asf(r2v.y) + asf(r3v.y)));
      const unsigned res = pk2(asf(xw << 16) + rbf(s0 * g0), asf(xw & 0xffff0000u) + rbf(s1v * g1));
      if (lane < 8) *(GAS unsigned*)(out + fc * 32 + lane * 4) = res;
      if (fc == 0 && lane == 0) __hip_atomic_store((GAS unsigned*)ws, tag, RLX_AGENT);
      STAMP(8);
    }
    STAMP_FLUSH();
  } else if (w <= 4) {
    const int k = w - 1;
    __builtin_amdgcn_s_setprio(2);
    BARRIER();
    v4u kf[16];
    {
      const __amdgpu_buffer_rsrc_t rk = mkrs(kc + ((size_t)h * SK + (SK - WIN) + 128 * j + 32 * k) * 512, 32 * 512);
      const int vo = g4 * 512 + n16 * 16;
#pragma unroll
      for (int i = 0; i < 16; ++i) kf[i] = ldnt(rk, vo + (i & 1) * 256, (i >> 1) * 2048);
    }
    __builtin_amdgcn_sched_barrier(0);
    lds_wait(FL + 0, 1u);
    v4u q[4];
#pragma unroll
    for (int u = 0; u < 4; ++u) q[u] = *(const LAS v4u*)(L + L_Q + u * 64 + 4 * n16);
    float xs[16];
#pragma unroll
    for (int i = 0; i < 8; ++i)
#pragma unroll
      for (int a = 0; a < 2; ++a) {
        float d = 0.f;
#pragma unroll
        for (int hf = 0; hf < 2; ++hf)
#pragma unroll
          for (int e = 0; e < 4; ++e) d = dot2(kf[2 * i + hf][e], q[2 * a + hf][e], d);
        xs[8 * a + i] = d;
      }
#define TR_STEP(HALF, CTRL, BIT) _Pragma("unroll") for (int u = 0; u < (HALF); ++u) { \
      const float lo_ = xs[u], hi_ = xs[u + (HALF)]; const float t1_ = lo_ + dpp<CTRL>(lo_), t2_ = hi_ + dpp<CTRL>(hi_); xs[u] = (n16 & (BIT)) ? t2_ : t1_; }
    TR_STEP(8, XOR8, 8) TR_STEP(4, XOR7, 4) TR_STEP(2, XOR2, 2) TR_STEP(1, XOR1, 1)
#undef TR_STEP
    const float sc = rbf(xs[0]);
    float m = sc; m = fmaxf(m, dpp<XOR1>(m)); m = fmaxf(m, dpp<XOR2>(m)); m = fmaxf(m, dpp<XOR7>(m)); m = x32_max(x16_max(m));
    const int a = n16 >> 3;
    if ((lane & 0x37) == 0) L[L_WM + 2 * k + a] = asu(m);
    LGKM0();
    if (lane == 0) __hip_atomic_fetch_add((LAS unsigned*)(L + L_FLAG + 1), 1u, __ATOMIC_RELAXED, __HIP_MEMORY_SCOPE_WORKGROUP);
    lds_wait(FL + 1, 4u);
    const float M = fmaxf(fmaxf(asf(L[L_WM + a]), asf(L[L_WM + 2 + a])), fmaxf(asf(L[L_WM + 4 + a]), asf(L[L_WM + 6 + a])));
    const float p = __builtin_amdgcn_exp2f((sc - M) * C2);
    L[L_P + a * 128 + (4 * (c8 & 1) + g4) * 16 + 4 * k + (c8 >> 1)] = pk2(p, p);
    float l = p; l += dpp<XOR1>(l); l += dpp<XOR2>(l); l += dpp<XOR7>(l); l = x32_sum(x16_sum(l));
    if ((lane & 0x37) == 0) L[L_WL + 2 * k + a] = asu(l);
    LGKM0();
    if (lane == 0) __hip_atomic_fetch_add((LAS unsigned*)(L + L_FLAG + 2), 1u, __ATOMIC_RELAXED, __HIP_MEMORY_SCOPE_WORKGROUP);
    if (k == 0) STAMP(9);
    BARRIER();
    const unsigned tag = FL[3];
    const int cA = 2 * k, sA = cA * 128, sB = k < 3 ? sA + 128 : (1 << 20);
    if (isE) {
      v4u wf[8];
      const __amdgpu_buffer_rsrc_t ro = mkrs(wo + (size_t)(512 * h + 32 * ei) * 896, 32 * 896);
      const int vo = rs * 896 + c8 * 16;
#pragma unroll
      for (int i = 0; i < 4; ++i) { wf[i] = ldnt(ro, vo, i * 8 * 896 + sA); wf[4 + i] = ldnt(ro, vo, i * 8 * 896 + sB); }
      __builtin_amdgcn_sched_barrier(0);
      BARRIER();
      const v4u ov = *(const LAS v4u*)(L + L_O + rs * 4);
      f32x4 a0 = {0.f, 0.f, 0.f, 0.f}, a1 = {0.f, 0.f, 0.f, 0.f};
#pragma unroll
      for (int i = 0; i < 4; ++i) { const v4u am = amask(ov[i], msk); a0 = mfma(am, wf[i], a0); a1 = mfma(am, wf[4 + i], a1); }
      float v0, v1;
      fixup(a0, a1, nlo, v0, v1);
      if (k < 3 || lane < 32) stsc(rw, WS_S + (h * 16 + ei) * S_PITCH + (cA + (lane >> 5)) * 512 + gidx * 16, v0, v1, tag);
      if (k == 0) STAMP(10);
    } else {
      if (ffn) {
        v4u wf[4];
        const __amdgpu_buffer_rsrc_t rd = mkrs(wd + (size_t)(64 * cg + 16 * tq) * 896, 16 * 896);
        const int vo = rs * 896 + c8 * 16;
#pragma unroll
        for (int i = 0; i < 2; ++i) { wf[i] = ldnt(rd, vo, i * 8 * 896 + sA); wf[2 + i] = ldnt(rd, vo, i * 8 * 896 + sB); }
        const v4u ov = *(const LAS v4u*)(L + L_O + rs * 4);
        f32x4 a0 = {0.f, 0.f, 0.f, 0.f}, a1 = {0.f, 0.f, 0.f, 0.f};
#pragma unroll
        for (int i = 0; i < 2; ++i) { const v4u am = amask(ov[i], msk); a0 = mfma(am, wf[i], a0); a1 = mfma(am, wf[2 + i], a1); }
        float v0, v1;
        fixup(a0, a1, nlo, v0, v1);
        if (k < 3 || lane < 32) stsc(rw, WS_F + fi * S_PITCH + (cA + (lane >> 5)) * 512 + gidx * 16, v0, v1, tag);
        if (k == 0) STAMP(11);
      }
      if (fin) {
        v4u sv[4];
        poll<4>(rw, WS_S + (32 * k + rs) * S_PITCH + fc * 128 + c8 * 16, 8 * S_PITCH, tag, sv);
        float s0 = (asf(sv[0].x) + asf(sv[1].x)) + (asf(sv[2].x) + asf(sv[3].x)), s1 = (asf(sv[0].z) + asf(sv[1].z)) + (asf(sv[2].z) + asf(sv[3].z));
        s0 += dpp<XOR8>(s0); s1 += dpp<XOR8>(s1);
        s0 = x32_sum(x16_sum(s0)); s1 = x32_sum(x16_sum(s1));
        if (lane < 8) *(LAS v2u*)(L + L_RED + 16 * k + 2 * c8) = v2u{asu(s0), asu(s1)};
        if (k == 0) STAMP(12);
        BARRIER();
      }
    }
    STAMP_FLUSH();
  } else {
    const int v = w - 5;
    BARRIER();
    v4u vf[16];
    {
      const __amdgpu_buffer_rsrc_t rv = mkrs(vc + ((size_t)h * SK + (SK - WIN) + 128 * j) * 512, 128 * 512);
      const int vo = rs * 512 + c8 * 16 + v * 128;
#pragma unroll
      for (int i = 0; i < 16; ++i) vf[i] = ldnt(rv, vo, i * 4096);
    }
    __builtin_amdgcn_sched_barrier(0);
    if (v == 3) STAMP(13);
    BARRIER();
    const unsigned tag = FL[3];
    v4u pa[4], pb[4];
#pragma unroll
    for (int u = 0; u < 4; ++u) { pa[u] = *(const LAS v4u*)(L + L_P + rs * 16 + 4 * u); pb[u] = *(const LAS v4u*)(L + L_P + 128 + rs * 16 + 4 * u); }
    f32x4 a0 = {0.f, 0.f, 0.f, 0.f}, a1 = {0.f, 0.f, 0.f, 0.f};
#pragma unroll
    for (int i = 0; i < 16; ++i) { a0 = mfma(amask(pa[i >> 2][i & 3], msk), vf[i], a0); a1 = mfma(amask(pb[i >> 2][i & 3], msk), vf[i], a1); }
    float v0, v1;
    fixup(a0, a1, nlo, v0, v1);
    stsc(rw, WS_O + b * O_PITCH + (lane >> 5) * 2048 + v * 512 + gidx * 16, v0, v1, tag);
    if (v == 3) STAMP(14);
    if (v == 0) STAMP(15);
    STAMP_FLUSH();
  }
}

extern "C" void kernel_launch(void* const* d_in, const int* in_sizes, int n_in, void* d_out, int out_size, void* d_ws, size_t ws_size,
                              hipStream_t stream) {
  fused_decode<<<NWG, NTHR, 0, stream>>>((const GAS char*)d_in[0], (const GAS char*)d_in[1], (const GAS char*)d_in[2], (const GAS char*)d_in[3],
                                         (const GAS char*)d_in[4], (const GAS char*)d_in[6], (const GAS char*)d_in[7], (const GAS char*)d_in[8],
                                         (GAS char*)d_out, (GAS char*)d_ws);
}
```

```cpp
#define PROBE 0
#include <hip/hip_runtime.h>
#include <stdint.h>

#define GAS __attribute__((address_space(1)))
#define LAS __attribute__((address_space(3)))
typedef unsigned v4u __attribute__((ext_vector_type(4)));
typedef unsigned v2u __attribute__((ext_vector_type(2)));
typedef float f32x4 __attribute__((ext_vector_type(4)));
typedef __bf16 bf16x8 __attribute__((ext_vector_type(8)));
typedef __bf16 bf16x2 __attribute__((ext_vector_type(2)));
#define RLX_AGENT __ATOMIC_RELAXED, __HIP_MEMORY_SCOPE_AGENT

constexpr int DM = 448, FF = 1792, SK = 65536, WIN = 4096, DH = 256;
constexpr int NWG = 256, NTHR = 576;
constexpr int WS_Q = 4096;
constexpr int WS_GU = WS_Q + 8 * 8192;
constexpr int WS_ML = WS_GU + 28 * 4096;
constexpr int O_PITCH = 4352;
constexpr int WS_O = WS_ML + 8192;
constexpr int S_PITCH = 3584;
constexpr int WS_S = WS_O + 256 * O_PITCH;
constexpr int WS_F = WS_S + 128 * S_PITCH;
constexpr int WS_END = WS_F + 112 * S_PITCH;
constexpr int L_XS = 0;
constexpr int L_Q = 256;
constexpr int L_P = 512;
constexpr int L_O = 768;
constexpr int L_W = 800;
constexpr int L_WM = 832;
constexpr int L_WL = 840;
constexpr int L_X = 848;
constexpr int L_RED = 864;
constexpr int L_FLAG = 928;
constexpr int L_TOTAL = 1024;
constexpr unsigned SPIN_CAP = 1u << 16;

#define STAMP(k) do { if ((k) == PROBE) stp = st0; } while (0)
#define STAMP_DECL const unsigned long long st0 = __builtin_amdgcn_s_memrealtime(); unsigned long long stp = 0
#define STAMP_FLUSH() do { if (stp) { const unsigned long long tend = st0 + 4000 + 4 * (stp - st0); while (__builtin_amdgcn_s_memrealtime() < tend) __builtin_amdgcn_s_sleep(2); } } while (0)

__device__ __forceinline__ float asf(unsigned u) { return __builtin_bit_cast(float, u); }
__device__ __forceinline__ unsigned asu(float f) { return __builtin_bit_cast(unsigned, f); }
__device__ __forceinline__ float rbf(float f) { return (float)(__bf16)f; }
__device__ __forceinline__ unsigned pk2(float lo, float hi) { bf16x2 t = {(__bf16)lo, (__bf16)hi}; return __builtin_bit_cast(unsigned, t); }
__device__ __forceinline__ float dot2(unsigned a, unsigned b, float c) {
  return __builtin_amdgcn_fdot2_f32_bf16(__builtin_bit_cast(bf16x2, a), __builtin_bit_cast(bf16x2, b), c, false);
}
__device__ __forceinline__ f32x4 mfma(v4u a, v4u b, f32x4 c) {
  return __builtin_amdgcn_mfma_f32_16x16x32_bf16(__builtin_bit_cast(bf16x8, a), __builtin_bit_cast(bf16x8, b), c, 0, 0, 0);
}
template <int CTRL> __device__ __forceinline__ float dpp(float x) {
  return asf((unsigned)__builtin_amdgcn_mov_dpp((int)asu(x), CTRL, 0xf, 0xf, true));
}
constexpr int XOR1 = 0xB1, XOR2 = 0x4E, XOR7 = 0x141, XOR8 = 0x128;
__device__ __forceinline__ float x16_sum(float x) { auto s = __builtin_amdgcn_permlane16_swap(asu(x), asu(x), false, false); return asf(s[0]) + asf(s[1]); }
__device__ __forceinline__ float x32_sum(float x) { auto s = __builtin_amdgcn_permlane32_swap(asu(x), asu(x), false, false); return asf(s[0]) + asf(s[1]); }
__device__ __forceinline__ float x16_max(float x) { auto s = __builtin_amdgcn_permlane16_swap(asu(x), asu(x), false, false); return fmaxf(asf(s[0]), asf(s[1])); }
__device__ __forceinline__ float x32_max(float x) { auto s = __builtin_amdgcn_permlane32_swap(asu(x), asu(x), false, false); return fmaxf(asf(s[0]), asf(s[1])); }
__device__ __forceinline__ float wave_sum(float x) { x += dpp<XOR1>(x); x += dpp<XOR2>(x); x += dpp<XOR7>(x); x += dpp<XOR8>(x); x = x16_sum(x); return x32_sum(x); }
__device__ __forceinline__ __amdgpu_buffer_rsrc_t mkrs(const GAS char* p, int bytes) { return __builtin_amdgcn_make_buffer_rsrc((void*)p, 0, bytes, 0x00020000); }
__device__ __forceinline__ v4u ldnt(__amdgpu_buffer_rsrc_t rs, int voff, int soff) {
  const v4u r = __builtin_amdgcn_raw_buffer_load_b128(rs, voff, soff, 2);
  __builtin_amdgcn_sched_barrier(0);
  return r;
}
__device__ __forceinline__ v4u ldsc(__amdgpu_buffer_rsrc_t rs, int off) { return __builtin_amdgcn_raw_buffer_load_b128(rs, off, 0, 16); }
__device__ __forceinline__ void stsc(__amdgpu_buffer_rsrc_t rs, int off, float v0, float v1, unsigned tag) {
  v4u g = {asu(v0), tag, asu(v1), tag};
  __builtin_amdgcn_raw_buffer_store_b128(g, rs, off, 0, 16);
}
#define BARRIER() asm volatile("s_waitcnt lgkmcnt(0)\n\ts_barrier" ::: "memory")
#define LGKM0() asm volatile("s_waitcnt lgkmcnt(0)" ::: "memory")
#define CFENCE() asm volatile("" ::: "memory")
__device__ __forceinline__ v4u amask(unsigned x, const v4u& m) { return v4u{x & m.x, x & m.y, x & m.z, x & m.w}; }
__device__ __forceinline__ void fixup(const f32x4& A, const f32x4& B, bool nlo, float& v0, float& v1) {
  float s[4];
#pragma unroll
  for (int r = 0; r < 4; ++r) {
    auto t = __builtin_amdgcn_permlane32_swap(asu(A[r]), asu(B[r]), false, false);
    const float a = asf(t[0]), b = asf(t[1]);
    s[r] = r < 2 ? a + dpp<XOR8>(b) : dpp<XOR8>(a) + b;
  }
  v0 = nlo ? s[0] : s[2];
  v1 = nlo ? s[1] : s[3];
}
template <int N> __device__ __forceinline__ void poll(__amdgpu_buffer_rsrc_t rs, int off, int stride, unsigned tag, v4u (&v)[N]) {
#pragma unroll 1
  for (unsigned spins = 0; spins < SPIN_CAP; ++spins) {
    CFENCE();
    bool ok = true;
#pragma unroll
    for (int k = 0; k < N; ++k) v[k] = __builtin_amdgcn_raw_buffer_load_b128(rs, off, k * stride, 16);
#pragma unroll
    for (int k = 0; k < N; ++k) ok &= (v[k].y == tag) & (v[k].w == tag);
    if (__all(ok)) break;
    __builtin_amdgcn_s_sleep(1);
  }
}
__device__ __forceinline__ void lds_wait(volatile LAS unsigned* w, unsigned want) {
#pragma unroll 1
  for (unsigned spins = 0; spins < (SPIN_CAP << 6); ++spins) {
    if (__builtin_amdgcn_readfirstlane(*w) >= want) break;
    __builtin_amdgcn_s_sleep(1);
  }
  CFENCE();
}
__device__ __forceinline__ float silu(float x) { return x / (1.f + __expf(-x)); }

__global__ void __launch_bounds__(NTHR) fused_decode(const GAS char* __restrict__ x, const GAS char* __restrict__ kc, const GAS char* __restrict__ vc,
                                                     const GAS char* __restrict__ wq, const GAS char* __restrict__ wo, const GAS char* __restrict__ wg,
                                                     const GAS char* __restrict__ wu, const GAS char* __restrict__ wd, GAS char* out, GAS char* ws) {
  __shared__ __attribute__((aligned(16))) unsigned lds_[L_TOTAL];
  LAS unsigned* L = (LAS unsigned*)lds_;
  volatile LAS unsigned* FL = (volatile LAS unsigned*)(L + L_FLAG);
  const int b = blockIdx.x, h = b & 7, j = b >> 3;
  const int tid = threadIdx.x, lane = tid & 63, w = __builtin_amdgcn_readfirstlane(tid >> 6);
  const bool isE = (j & 1) == 0;
  const int ei = j >> 1;
  const int fi = h + 8 * (j >> 1);
  const bool ffn = !isE && fi < 112;
  const bool fin = !isE && fi >= 100;
  const int cg = fi >> 2, tq = fi & 3, fc = fi - 100;
  const int rs = lane >> 3, c8 = lane & 7, g4 = lane >> 4, n16 = lane & 15;
  const bool nlo = n16 < 8;
  const int gidx = 4 * c8 + 2 * (g4 & 1) + (n16 >> 3);
  const __amdgpu_buffer_rsrc_t rw = __builtin_amdgcn_make_buffer_rsrc((void*)ws, 0, WS_END, 0x00020000);
  const unsigned hm = (c8 & 1) ? 0xffff0000u : 0x0000ffffu;
  const v4u msk = {(c8 >> 1) == 0 ? hm : 0u, (c8 >> 1) == 1 ? hm : 0u, (c8 >> 1) == 2 ? hm : 0u, (c8 >> 1) == 3 ? hm : 0u};
  constexpr float C2 = 0.0625f * 1.4426950408889634f;
  STAMP_DECL;

  if (w == 0) {
    STAMP(0);
    __builtin_amdgcn_s_setprio(3);
    const unsigned ep = __builtin_amdgcn_raw_buffer_load_b32(rw, 0, 0, 16);
    const v4u xr = __builtin_amdgcn_raw_buffer_load_b128(mkrs(x, 896), lane * 16, 0, 0);
    __builtin_amdgcn_sched_barrier(0);
    const bool ph1 = isE || ffn;
    v4u fr[28];
    const int r0 = isE ? 224 * (ei & 1) : 112 * tq;
    {
      const int pitch = isE ? 8192 : 3584;
      const int sb = r0 * pitch + (isE ? (512 * h + 64 * (ei >> 1)) * 2 : cg * 128);
      const int nb = ph1 ? DM * pitch : 0;
      const __amdgpu_buffer_rsrc_t ra = mkrs(isE ? wq : wg, nb), rb = mkrs(isE ? wq : wu, nb);
      const int sb1 = isE ? sb + 112 * 8192 : sb;
      const int vo = rs * pitch + c8 * 16;
#pragma unroll
      for (int i = 0; i < 14; ++i) fr[i] = ldnt(ra, vo, sb + i * 8 * pitch);
#pragma unroll
      for (int i = 0; i < 14; ++i) fr[14 + i] = ldnt(rb, vo, sb1 + i * 8 * pitch);
    }
    if (lane < 4) FL[lane] = 0u;
    __builtin_amdgcn_sched_barrier(0);
    BARRIER();
    __builtin_amdgcn_sched_barrier(0);
    const unsigned tag = ep + 1u;
    float xv[8];
#pragma unroll
    for (int k = 0; k < 4; ++k) { xv[2 * k] = asf(xr[k] << 16); xv[2 * k + 1] = asf(xr[k] & 0xffff0000u); }
    float s1 = ((xv[0] + xv[1]) + (xv[2] + xv[3])) + ((xv[4] + xv[5]) + (xv[6] + xv[7]));
    const float mean = wave_sum(s1) * (1.f / DM);
    float s2 = 0.f;
#pragma unroll
    for (int k = 0; k < 8; ++k) { const float d = xv[k] - mean; s2 += d * d; }
    s2 = lane < 56 ? s2 : 0.f;
    const float var = wave_sum(s2) * (1.f / DM);
    const float mb = rbf(mean), den = rbf(sqrtf(rbf(rbf(var) + 1e-5f)));
    if (lane == 0) FL[3] = tag;
    if (fin && (lane >> 1) == fc) *(LAS v4u*)(L + L_X + 4 * (lane & 1)) = xr;
    STAMP(1);
    if (ph1) {
      const int li = lane - (r0 >> 3);
      if (li >= 0 && li < (isE ? 28 : 14)) {
        const int hf = li >= 14 ? 1 : 0, ii = li - 14 * hf;
#pragma unroll
        for (int k = 0; k < 8; ++k) { const float xn = rbf(xv[k] - mb) / den; L[L_XS + hf * 128 + k * 16 + ii] = pk2(xn, xn); }
      }
      LGKM0();
      f32x4 a0 = {0.f, 0.f, 0.f, 0.f};
      {
        v4u xs[4];
#pragma unroll
        for (int k = 0; k < 4; ++k) xs[k] = *(const LAS v4u*)(L + L_XS + rs * 16 + 4 * k);
#pragma unroll
        for (int i = 0; i < 14; ++i) a0 = mfma(amask(xs[i >> 2][i & 3], msk), fr[i], a0);
      }
      f32x4 a1 = isE ? a0 : f32x4{0.f, 0.f, 0.f, 0.f};
      {
        v4u xs[4];
#pragma unroll
        for (int k = 0; k < 4; ++k) xs[k] = *(const LAS v4u*)(L + L_XS + (isE ? 128 : 0) + rs * 16 + 4 * k);
#pragma unroll
        for (int i = 0; i < 14; ++i) a1 = mfma(amask(xs[i >> 2][i & 3], msk), fr[14 + i], a1);
      }
      if (isE) a0 = a1;
      float v0, v1;
      fixup(a0, a1, nlo, v0, v1);
      if (isE) { if (lane < 32) stsc(rw, WS_Q + h * 8192 + (ei & 1) * 4096 + (ei >> 1) * 512 + gidx * 16, v0, v1, tag); }
      else stsc(rw, WS_GU + cg * 4096 + tq * 1024 + (lane >> 5) * 512 + gidx * 16, v0, v1, tag);
    }
    STAMP(2);
    {
      v4u qv[8];
      poll<8>(rw, WS_Q + h * 8192 + lane * 16, 1024, tag, qv);
#pragma unroll
      for (int k = 0; k < 4; ++k) L[L_Q + 64 * k + lane] = pk2(asf(qv[k].x) + asf(qv[k + 4].x), asf(qv[k].z) + asf(qv[k + 4].z));
      LGKM0();
      if (lane == 0) FL[0] = 1u;
    }
    STAMP(3);
    if (ffn) {
      v4u gu[1];
      poll<1>(rw, WS_GU + cg * 4096 + rs * 512 + tq * 128 + c8 * 16, 0, tag, gu);
      float y0 = x32_sum(x16_sum(asf(gu[0].x))), y1 = x32_sum(x16_sum(asf(gu[0].z)));
      y0 = rbf(y0); y1 = rbf(y1);
      const float u0 = dpp<XOR8>(y0), u1 = dpp<XOR8>(y1);
      const float h0 = rbf(silu(y0)) * u0, h1 = rbf(silu(y1)) * u1;
      if (lane < 8) {
        const int r = 2 * c8;
        L[L_O + (r & 7) * 4 + (r >> 3)] = pk2(h0, h0);
        L[L_O + ((r + 1) & 7) * 4 + (r >> 3)] = pk2(h1, h1);
      }
    }
    lds_wait(FL + 2, 4u);
    if (lane < 2) {
      const float M = fmaxf(fmaxf(asf(L[L_WM + lane]), asf(L[L_WM + 2 + lane])), fmaxf(asf(L[L_WM + 4 + lane]), asf(L[L_WM + 6 + lane])));
      const float Ls = (asf(L[L_WL + lane]) + asf(L[L_WL + 2 + lane])) + (asf(L[L_WL + 4 + lane]) + asf(L[L_WL + 6 + lane]));
      stsc(rw, WS_ML + (h * 2 + lane) * 512 + j * 16, M, Ls, tag);
    }
    STAMP(4);
    BARRIER();
    STAMP(5);
    if (isE) {
      v4u ml[1];
      poll<1>(rw, WS_ML + (h * 2 + (ei >> 3)) * 512 + (lane & 31) * 16, 0, tag, ml);
      const float m = asf(ml[0].x);
      float M = m; M = fmaxf(M, dpp<XOR1>(M)); M = fmaxf(M, dpp<XOR2>(M)); M = fmaxf(M, dpp<XOR7>(M)); M = fmaxf(M, dpp<XOR8>(M)); M = x16_max(M);
      const float e = __builtin_amdgcn_exp2f((m - M) * C2);
      float Ls = e * asf(ml[0].z); Ls += dpp<XOR1>(Ls); Ls += dpp<XOR2>(Ls); Ls += dpp<XOR7>(Ls); Ls += dpp<XOR8>(Ls); Ls = x16_sum(Ls);
      if (lane < 32) L[L_W + (lane & 3) * 8 + (lane >> 2)] = asu(e / Ls);
      LGKM0();
      const v4u w0 = *(const LAS v4u*)(L + L_W + g4 * 8), w1 = *(const LAS v4u*)(L + L_W + g4 * 8 + 4);
      STAMP(6);
      v4u ov[8];
      poll<8>(rw, WS_O + (h + 8 * g4) * O_PITCH + ei * 256 + n16 * 16, 32 * O_PITCH, tag, ov);
      STAMP(7);
      float o0 = 0.f, o1 = 0.f;
#pragma unroll
      for (int k = 0; k < 8; ++k) { const float wk = asf(k < 4 ? w0[k & 3] : w1[k & 3]); o0 = fmaf(wk, asf(ov[k].x), o0); o1 = fmaf(wk, asf(ov[k].z), o1); }
      o0 = x32_sum(x16_sum(o0)); o1 = x32_sum(x16_sum(o1));
      if (lane < 16) { const int r = 2 * lane;
        L[L_O + (r & 7) * 4 + (r >> 3)] = pk2(o0, o0);
        L[L_O + ((r + 1) & 7) * 4 + (r >> 3)] = pk2(o1, o1); }
      BARRIER();
    } else if (fin) {
      v4u fv[14];
      poll<14>(rw, WS_F + rs * S_PITCH + fc * 128 + c8 * 16, 8 * S_PITCH, tag, fv);
      float f0 = 0.f, f1 = 0.f;
#pragma unroll
      for (int k = 0; k < 14; ++k) { f0 += asf(fv[k].x); f1 += asf(fv[k].z); }
      f0 += dpp<XOR8>(f0); f1 += dpp<XOR8>(f1);
      f0 = x32_sum(x16_sum(f0)); f1 = x32_sum(x16_sum(f1));
      const float g0 = rbf(silu(rbf(f0))), g1 = rbf(silu(rbf(f1)));
      const unsigned xw = L[L_X + c8];
      STAMP(6);
      BARRIER();
      const v2u r0v = *(const LAS v2u*)(L + L_RED + 2 * c8), r1v = *(const LAS v2u*)(L + L_RED + 16 + 2 * c8),
                r2v = *(const LAS v2u*)(L + L_RED + 32 + 2 * c8), r3v = *(const LAS v2u*)(L + L_RED + 48 + 2 * c8);
      const float s0 = rbf((asf(r0v.x) + asf(r1v.x)) + (asf(r2v.x) + asf(r3v.x))), s1v = rbf((asf(r0v.y) + asf(r1v.y)) + (asf(r2v.y) + asf(r3v.y)));
      const unsigned res = pk2(asf(xw << 16) + rbf(s0 * g0), asf(xw & 0xffff0000u) + rbf(s1v * g1));
      if (lane < 8) *(GAS unsigned*)(out + fc * 32 + lane * 4) = res;
      if (fc == 0 && lane == 0) __hip_atomic_store((GAS unsigned*)ws, tag, RLX_AGENT);
      STAMP(8);
    }
    STAMP_FLUSH();
  } else if (w <= 4) {
    const int k = w - 1;
    __builtin_amdgcn_s_setprio(2);
    BARRIER();
    v4u kf[16];
    {
      const __amdgpu_buffer_rsrc_t rk = mkrs(kc + ((size_t)h * SK + (SK - WIN) + 128 * j + 32 * k) * 512, 32 * 512);
      const int vo = g4 * 512 + n16 * 16;
#pragma unroll
      for (int i = 0; i < 16; ++i) kf[i] = ldnt(rk, vo + (i & 1) * 256, (i >> 1) * 2048);
    }
    __builtin_amdgcn_sched_barrier(0);
    lds_wait(FL + 0, 1u);
    v4u q[4];
#pragma unroll
    for (int u = 0; u < 4; ++u) q[u] = *(const LAS v4u*)(L + L_Q + u * 64 + 4 * n16);
    float xs[16];
#pragma unroll
    for (int i = 0; i < 8; ++i)
#pragma unroll
      for (int a = 0; a < 2; ++a) {
        float d = 0.f;
#pragma unroll
        for (int hf = 0; hf < 2; ++hf)
#pragma unroll
          for (int e = 0; e < 4; ++e) d = dot2(kf[2 * i + hf][e], q[2 * a + hf][e], d);
        xs[8 * a + i] = d;
      }
#define TR_STEP(HALF, CTRL, BIT) _Pragma("unroll") for (int u = 0; u < (HALF); ++u) { \
      const float lo_ = xs[u], hi_ = xs[u + (HALF)]; const float t1_ = lo_ + dpp<CTRL>(lo_), t2_ = hi_ + dpp<CTRL>(hi_); xs[u] = (n16 & (BIT)) ? t2_ : t1_; }
    TR_STEP(8, XOR8, 8) TR_STEP(4, XOR7, 4) TR_STEP(2, XOR2, 2) TR_STEP(1, XOR1, 1)
#undef TR_STEP
    const float sc = rbf(xs[0]);
    float m = sc; m = fmaxf(m, dpp<XOR1>(m)); m = fmaxf(m, dpp<XOR2>(m)); m = fmaxf(m, dpp<XOR7>(m)); m = x32_max(x16_max(m));
    const int a = n16 >> 3;
    if ((lane & 0x37) == 0) L[L_WM + 2 * k + a] = asu(m);
    LGKM0();
    if (lane == 0) __hip_atomic_fetch_add((LAS unsigned*)(L + L_FLAG + 1), 1u, __ATOMIC_RELAXED, __HIP_MEMORY_SCOPE_WORKGROUP);
    lds_wait(FL + 1, 4u);
    const float M = fmaxf(fmaxf(asf(L[L_WM + a]), asf(L[L_WM + 2 + a])), fmaxf(asf(L[L_WM + 4 + a]), asf(L[L_WM + 6 + a])));
    const float p = __builtin_amdgcn_exp2f((sc - M) * C2);
    L[L_P + a * 128 + (4 * (c8 & 1) + g4) * 16 + 4 * k + (c8 >> 1)] = pk2(p, p);
    float l = p; l += dpp<XOR1>(l); l += dpp<XOR2>(l); l += dpp<XOR7>(l); l = x32_sum(x16_sum(l));
    if ((lane & 0x37) == 0) L[L_WL + 2 * k + a] = asu(l);
    LGKM0();
    if (lane == 0) __hip_atomic_fetch_add((LAS unsigned*)(L + L_FLAG + 2), 1u, __ATOMIC_RELAXED, __HIP_MEMORY_SCOPE_WORKGROUP);
    if (k == 0) STAMP(9);
    BARRIER();
    const unsigned tag = FL[3];
    const int cA = 2 * k, sA = cA * 128, sB = k < 3 ? sA + 128 : (1 << 20);
    if (isE) {
      v4u wf[8];
      const __amdgpu_buffer_rsrc_t ro = mkrs(wo + (size_t)(512 * h + 32 * ei) * 896, 32 * 896);
      const int vo = rs * 896 + c8 * 16;
#pragma unroll
      for (int i = 0; i < 4; ++i) { wf[i] = ldnt(ro, vo, i * 8 * 896 + sA); wf[4 + i] = ldnt(ro, vo, i * 8 * 896 + sB); }
      __builtin_amdgcn_sched_barrier(0);
      BARRIER();
      const v4u ov = *(const LAS v4u*)(L + L_O + rs * 4);
      f32x4 a0 = {0.f, 0.f, 0.f, 0.f}, a1 = {0.f, 0.f, 0.f, 0.f};
#pragma unroll
      for (int i = 0; i < 4; ++i) { const v4u am = amask(ov[i], msk); a0 = mfma(am, wf[i], a0); a1 = mfma(am, wf[4 + i], a1); }
      float v0, v1;
      fixup(a0, a1, nlo, v0, v1);
      if (k < 3 || lane < 32) stsc(rw, WS_S + (h * 16 + ei) * S_PITCH + (cA + (lane >> 5)) * 512 + gidx * 16, v0, v1, tag);
      if (k == 0) STAMP(10);
    } else {
      if (ffn) {
        v4u wf[4];
        const __amdgpu_buffer_rsrc_t rd = mkrs(wd + (size_t)(64 * cg + 16 * tq) * 896, 16 * 896);
        const int vo = rs * 896 + c8 * 16;
#pragma unroll
        for (int i = 0; i < 2; ++i) { wf[i] = ldnt(rd, vo, i * 8 * 896 + sA); wf[2 + i] = ldnt(rd, vo, i * 8 * 896 + sB); }
        const v4u ov = *(const LAS v4u*)(L + L_O + rs * 4);
        f32x4 a0 = {0.f, 0.f, 0.f, 0.f}, a1 = {0.f, 0.f, 0.f, 0.f};
#pragma unroll
        for (int i = 0; i < 2; ++i) { const v4u am = amask(ov[i], msk); a0 = mfma(am, wf[i], a0); a1 = mfma(am, wf[2 + i], a1); }
        float v0, v1;
        fixup(a0, a1, nlo, v0, v1);
        if (k < 3 || lane < 32) stsc(rw, WS_F + fi * S_PITCH + (cA + (lane >> 5)) * 512 + gidx * 16, v0, v1, tag);
        if (k == 0) STAMP(11);
      }
      if (fin) {
        v4u sv[4];
        poll<4>(rw, WS_S + (32 * k + rs) * S_PITCH + fc * 128 + c8 * 16, 8 * S_PITCH, tag, sv);
        float s0 = (asf(sv[0].x) + asf(sv[1].x)) + (asf(sv[2].x) + asf(sv[3].x)), s1 = (asf(sv[0].z) + asf(sv[1].z)) + (asf(sv[2].z) + asf(sv[3].z));
        s0 += dpp<XOR8>(s0); s1 += dpp<XOR8>(s1);
        s0 = x32_sum(x16_sum(s0)); s1 = x32_sum(x16_sum(s1));
        if (lane < 8) *(LAS v2u*)(L + L_RED + 16 * k + 2 * c8) = v2u{asu(s0), asu(s1)};
        if (k == 0) STAMP(12);
        BARRIER();
      }
    }
    STAMP_FLUSH();
  } else {
    const int v = w - 5;
    BARRIER();
    v4u vf[16];
    {
      const __amdgpu_buffer_rsrc_t rv = mkrs(vc + ((size_t)h * SK + (SK - WIN) + 128 * j) * 512, 128 * 512);
      const int vo = rs * 512 + c8 * 16 + v * 128;
#pragma unroll
      for (int i = 0; i < 16; ++i) vf[i] = ldnt(rv, vo, i * 4096);
    }
    __builtin_amdgcn_sched_barrier(0);
    if (v == 3) STAMP(13);
    BARRIER();
    const unsigned tag = FL[3];
    v4u pa[4], pb[4];
#pragma unroll
    for (int u = 0; u < 4; ++u) { pa[u] = *(const LAS v4u*)(L + L_P + rs * 16 + 4 * u); pb[u] = *(const LAS v4u*)(L + L_P + 128 + rs * 16 + 4 * u); }
    f32x4 a0 = {0.f, 0.f, 0.f, 0.f}, a1 = {0.f, 0.f, 0.f, 0.f};
#pragma unroll
    for (int i = 0; i < 16; ++i) { a0 = mfma(amask(pa[i >> 2][i & 3], msk), vf[i], a0); a1 = mfma(amask(pb[i >> 2][i & 3], msk), vf[i], a1); }
    float v0, v1;
    fixup(a0, a1, nlo, v0, v1);
    stsc(rw, WS_O + b * O_PITCH + (lane >> 5) * 2048 + v * 512 + gidx * 16, v0, v1, tag);
    if (v == 3) STAMP(14);
    if (v == 0) STAMP(15);
    STAMP_FLUSH();
  }
}

extern "C" void kernel_launch(void* const* d_in, const int* in_sizes, int n_in, void* d_out, int out_size, void* d_ws, size_t ws_size,
                              hipStream_t stream) {
  fused_decode<<<NWG, NTHR, 0, stream>>>((const GAS char*)d_in[0], (const GAS char*)d_in[1], (const GAS char*)d_in[2], (const GAS char*)d_in[3],
                                         (const GAS char*)d_in[4], (const GAS char*)d_in[6], (const GAS char*)d_in[7], (const GAS char*)d_in[8],
                                         (GAS char*)d_out, (GAS char*)d_ws);
}
```

```cpp
#define PRETOUCH 0
#include <hip/hip_runtime.h>
#include <stdint.h>

#define GAS __attribute__((address_space(1)))
#define LAS __attribute__((address_space(3)))
typedef unsigned v4u __attribute__((ext_vector_type(4)));
typedef unsigned v2u __attribute__((ext_vector_type(2)));
typedef float f32x4 __attribute__((ext_vector_type(4)));
typedef __bf16 bf16x8 __attribute__((ext_vector_type(8)));
typedef __bf16 bf16x2 __attribute__((ext_vector_type(2)));
typedef short s16x4 __attribute__((ext_vector_type(4)));
#define RLX_AGENT __ATOMIC_RELAXED, __HIP_MEMORY_SCOPE_AGENT

constexpr int DM = 448, FF = 1792, SK = 65536, WIN = 4096, DH = 256;
constexpr int NWG = 256, NTHR = 320;
constexpr int WS_Q = 4096;
constexpr int WS_GU = WS_Q + 8 * 8192;
constexpr int WS_ML = WS_GU + 28 * 2048;
constexpr int O_PITCH = 4352;
constexpr int WS_O = WS_ML + 8192;
constexpr int S_PITCH = 3584;
constexpr int WS_S = WS_O + 256 * O_PITCH;
constexpr int WS_F = WS_S + 128 * S_PITCH;
constexpr int WS_END = WS_F + 112 * S_PITCH;
constexpr int L_R1 = 0;
constexpr int L_Q = 256;
constexpr int L_P = 512;
constexpr int L_O = 768;
constexpr int L_W = 800;
constexpr int L_WM = 832;
constexpr int L_WL = 840;
constexpr int L_RED = 864;
constexpr int L_OP = 960;
constexpr int L_Z = 944;
constexpr int L_FLAG = 928;
constexpr int L_TOTAL = 21504;
constexpr unsigned SPIN_CAP = 1u << 16;
#define HMAP 1
#define WINL 6
#define ABL 0
#define WOSPLIT 1
#define ODLY 100
#define VB1 10
#define VB2 12
#define VB3 14
#define WOPRE 8
#define OPRE 0
#define QPOS 0
#define WOWIN 6
#define VSTART 6
#define CODE_LINES 64

#define RAW(k, v)
#define STAMP(k)
#define STAMP_DECL
#define STAMP_FLUSH()

__device__ __forceinline__ float asf(unsigned u) { return __builtin_bit_cast(float, u); }
__device__ __forceinline__ unsigned asu(float f) { return __builtin_bit_cast(unsigned, f); }
__device__ __forceinline__ float rbf(float f) { return (float)(__bf16)f; }
__device__ __forceinline__ unsigned pk2(float lo, float hi) { bf16x2 t = {(__bf16)lo, (__bf16)hi}; return __builtin_bit_cast(unsigned, t); }
__device__ __forceinline__ f32x4 mfma32(v4u a, v4u b, f32x4 c) {
  return __builtin_amdgcn_mfma_f32_16x16x32_bf16(__builtin_bit_cast(bf16x8, a), __builtin_bit_cast(bf16x8, b), c, 0, 0, 0);
}
__device__ __forceinline__ f32x4 mfma16(v2u a, v2u b, f32x4 c) {
  return __builtin_amdgcn_mfma_f32_16x16x16bf16_1k(__builtin_bit_cast(s16x4, a), __builtin_bit_cast(s16x4, b), c, 0, 0, 0);
}
template <int CTRL> __device__ __forceinline__ float dpp(float x) {
  return asf((unsigned)__builtin_amdgcn_mov_dpp((int)asu(x), CTRL, 0xf, 0xf, true));
}
constexpr int XOR1 = 0xB1, XOR2 = 0x4E, XOR7 = 0x141, XOR8 = 0x128;
__device__ __forceinline__ float x16_sum(float x) { auto s = __builtin_amdgcn_permlane16_swap(asu(x), asu(x), false, false); return asf(s[0]) + asf(s[1]); }
__device__ __forceinline__ float x32_sum(float x) { auto s = __builtin_amdgcn_permlane32_swap(asu(x), asu(x), false, false); return asf(s[0]) + asf(s[1]); }
__device__ __forceinline__ float x16_max(float x) { auto s = __builtin_amdgcn_permlane16_swap(asu(x), asu(x), false, false); return fmaxf(asf(s[0]), asf(s[1])); }
__device__ __forceinline__ float x32_max(float x) { auto s = __builtin_amdgcn_permlane32_swap(asu(x), asu(x), false, false); return fmaxf(asf(s[0]), asf(s[1])); }
__device__ __forceinline__ float wave_sum(float x) { x += dpp<XOR1>(x); x += dpp<XOR2>(x); x += dpp<XOR7>(x); x += dpp<XOR8>(x); x = x16_sum(x); return x32_sum(x); }
__device__ __forceinline__ __amdgpu_buffer_rsrc_t mkrs(const GAS char* p, int bytes) { return __builtin_amdgcn_make_buffer_rsrc((void*)p, 0, bytes, 0x00020000); }
__device__ __forceinline__ v4u ldnt(__amdgpu_buffer_rsrc_t rs, int voff, int soff) {
  const v4u r = __builtin_amdgcn_raw_buffer_load_b128(rs, voff, soff, 2);
  __builtin_amdgcn_sched_barrier(0);
  return r;
}
__device__ __forceinline__ void stsc(__amdgpu_buffer_rsrc_t rs, int off, float v0, float v1, unsigned tag) {
  v4u g = {asu(v0), tag, asu(v1), tag};
  __builtin_amdgcn_raw_buffer_store_b128(g, rs, off, 0, 16);
}
#define BARRIER() asm volatile("s_waitcnt lgkmcnt(0)\n\ts_barrier" ::: "memory")
#define LGKM0() asm volatile("s_waitcnt lgkmcnt(0)" ::: "memory")
#define VMWIN(i) do { if ((i) >= WINL) asm volatile("s_waitcnt vmcnt(%0)" ::"i"(WINL - 1) : "memory"); } while (0)
#define CFENCE() asm volatile("" ::: "memory")
__device__ __forceinline__ v4u amask(unsigned x, const v4u& m) { return v4u{x & m.x, x & m.y, x & m.z, x & m.w}; }
template <bool R16> __device__ __forceinline__ void fixup(const f32x4& A, const f32x4& B, bool nlo, float& v0, float& v1) {
  float s[4];
#pragma unroll
  for (int r = 0; r < 4; ++r) {
    auto t = R16 ? __builtin_amdgcn_permlane16_swap(asu(A[r]), asu(B[r]), false, false) : __builtin_amdgcn_permlane32_swap(asu(A[r]), asu(B[r]), false, false);
    const float a = asf(t[0]), b = asf(t[1]);
    s[r] = r < 2 ? a + dpp<XOR8>(b) : dpp<XOR8>(a) + b;
  }
  v0 = nlo ? s[0] : s[2];
  v1 = nlo ? s[1] : s[3];
}
template <int N> __device__ __forceinline__ unsigned poll(__amdgpu_buffer_rsrc_t rs, int off, int stride, unsigned tag, v4u (&v)[N]) {
  unsigned spins = 0;
#pragma unroll 1
  for (; spins < SPIN_CAP; ++spins) {
    CFENCE();
    bool ok = true;
#pragma unroll
    for (int k = 0; k < N; ++k) v[k] = __builtin_amdgcn_raw_buffer_load_b128(rs, off, k * stride, 16);
#pragma unroll
    for (int k = 0; k < N; ++k) ok &= (v[k].y == tag) & (v[k].w == tag);
    if (__all(ok)) break;
  }
  return spins + 1;
}
__device__ __forceinline__ void lds_wait(volatile LAS unsigned* w, unsigned want) {
#pragma unroll 1
  for (unsigned spins = 0; spins < (SPIN_CAP << 5); ++spins) {
    if (__builtin_amdgcn_readfirstlane(*w) >= want) break;
    __builtin_amdgcn_s_sleep(1);
  }
  CFENCE();
}
__device__ __forceinline__ void lds_spin(volatile LAS unsigned* w, unsigned want) {
#pragma unroll 1
  for (unsigned spins = 0; spins < (SPIN_CAP << 6); ++spins) {
    if (__builtin_amdgcn_readfirstlane(*w) >= want) break;
  }
  CFENCE();
}
__device__ __forceinline__ void lds_arrive(LAS unsigned* w, int lane) {
  LGKM0();
  if (lane == 0) __hip_atomic_fetch_add(w, 1u, __ATOMIC_RELAXED, __HIP_MEMORY_SCOPE_WORKGROUP);
}
__device__ __forceinline__ float silu(float x) { return x * __builtin_amdgcn_rcpf(1.f + __expf(-x)); }

#define VMW(n) asm volatile("s_waitcnt vmcnt(%0)" ::"i"(n) : "memory")
__global__ void __launch_bounds__(NTHR) fused_decode(const GAS char* __restrict__ x, const GAS char* __restrict__ kc, const GAS char* __restrict__ vc,
                                                     const GAS char* __restrict__ wq, const GAS char* __restrict__ wo, const GAS char* __restrict__ wg,
                                                     const GAS char* __restrict__ wu, const GAS char* __restrict__ wd, GAS char* out, GAS char* ws) {
  __shared__ __attribute__((aligned(16))) unsigned lds_[L_TOTAL];
  LAS unsigned* L = (LAS unsigned*)lds_;
  volatile LAS unsigned* FL = (volatile LAS unsigned*)(L + L_FLAG);
  const int b = blockIdx.x;
  const int h = (b >> 3) & 7, j = (b & 7) * 4 + (b >> 6);
  const int pb = h + 8 * j;
  const int tid = threadIdx.x, lane = tid & 63, w = __builtin_amdgcn_readfirstlane(tid >> 6);
  STAMP_DECL;
  unsigned cpf = 0, cpf2 = 0;
  if (w == 4 && b >= 240) {
    const unsigned long long pc = __builtin_amdgcn_s_getpc() & ~255ull;
    cpf = *(const GAS unsigned*)(pc + 1024 + lane * 128);
    if (lane + 64 < CODE_LINES) cpf2 = *(const GAS unsigned*)(pc + 1024 + 8192 + lane * 128);
  }
  asm volatile("" ::"s"(x), "s"(kc), "s"(vc), "s"(wq), "s"(wo), "s"(wg), "s"(wu), "s"(wd), "s"(out), "s"(ws));
  const unsigned ep = *(const __attribute__((address_space(4))) unsigned*)(unsigned long long)ws;
  const bool isE = (j & 1) == 0;
  const int ei = j >> 1;
  const int fi = (b & 63) + 64 * (b >> 7);
  const bool ffn = !isE && fi < 112;
  const bool fin = !isE && fi >= 100;
  const int cg = fi >> 2, tq = fi & 3, fc = fi - 100;
  const int rs = lane >> 3, c8 = lane & 7, g4 = lane >> 4, n16 = lane & 15;
  const bool nlo = n16 < 8;
  const int gidx = 4 * c8 + 2 * (g4 & 1) + (n16 >> 3);
  const __amdgpu_buffer_rsrc_t rw = mkrs(ws, WS_END);
  constexpr float C2 = 0.0625f * 1.4426950408889634f;

  if (w == 4) {
    STAMP(0);
    __builtin_amdgcn_s_setprio(3);
    if (lane < 20) FL[lane] = 0u;
    unsigned pt = 0;
    asm volatile("s_waitcnt lgkmcnt(0)\n\ts_barrier" ::"s"(ep) : "memory");
    const unsigned tag = ep + 1u;
    if (isE || ffn) {
      const v2u r0 = *(const LAS v2u*)(L + L_R1 + 2 * (lane & 31)), r1 = *(const LAS v2u*)(L + L_R1 + 64 + 2 * (lane & 31)),
                r2 = *(const LAS v2u*)(L + L_R1 + 128 + 2 * (lane & 31)), r3 = *(const LAS v2u*)(L + L_R1 + 192 + 2 * (lane & 31));
      const float v0 = (asf(r0.x) + asf(r1.x)) + (asf(r2.x) + asf(r3.x)), v1 = (asf(r0.y) + asf(r1.y)) + (asf(r2.y) + asf(r3.y));
      const int dst = isE ? WS_Q + h * 8192 + (ei & 1) * 4096 + (ei >> 1) * 512 : WS_GU + cg * 2048 + tq * 512;
      if (lane < 32) stsc(rw, dst + lane * 16, v0, v1, tag);
    }
    STAMP(2);
    const unsigned xw = __builtin_amdgcn_raw_buffer_load_b32(mkrs(x, 896), c8 * 4, fin ? fc * 32 : 0, 0);
    asm volatile("" ::"v"(cpf), "v"(cpf2), "v"(pt));
    BARRIER();
    BARRIER();
    BARRIER();
    if (lane < 2) {
      const float M = fmaxf(fmaxf(asf(L[L_WM + lane]), asf(L[L_WM + 2 + lane])), fmaxf(asf(L[L_WM + 4 + lane]), asf(L[L_WM + 6 + lane])));
      const float Ls = (asf(L[L_WL + lane]) + asf(L[L_WL + 2 + lane])) + (asf(L[L_WL + 4 + lane]) + asf(L[L_WL + 6 + lane]));
      stsc(rw, WS_ML + (h * 2 + lane) * 512 + j * 16, M, Ls, tag);
    }
    STAMP(4);
    if (isE) {
      v4u ml[1];
      const unsigned np1 = poll<1>(rw, WS_ML + (h * 2 + (ei >> 3)) * 512 + (lane & 31) * 16, 0, tag, ml); RAW(29, np1); (void)np1;
      const float m = asf(ml[0].x);
      float M = m; M = fmaxf(M, dpp<XOR1>(M)); M = fmaxf(M, dpp<XOR2>(M)); M = fmaxf(M, dpp<XOR7>(M)); M = fmaxf(M, dpp<XOR8>(M)); M = x16_max(M);
      const float e = __builtin_amdgcn_exp2f((m - M) * C2);
      float Ls = e * asf(ml[0].z); Ls += dpp<XOR1>(Ls); Ls += dpp<XOR2>(Ls); Ls += dpp<XOR7>(Ls); Ls += dpp<XOR8>(Ls); Ls = x16_sum(Ls);
      if (lane < 32) L[L_W + lane] = asu(e * __builtin_amdgcn_rcpf(Ls));
      if (lane == 0) FL[7] = 1u;
      STAMP(6);
    } else {
      if (ffn) {
        v4u gu[1];
        poll<1>(rw, WS_GU + cg * 2048 + g4 * 512 + tq * 128 + c8 * 16, 0, tag, gu);
        const float y0 = rbf(x16_sum(asf(gu[0].x))), y1 = rbf(x16_sum(asf(gu[0].z)));
        auto t0 = __builtin_amdgcn_permlane32_swap(asu(y0), asu(y0), false, false);
        auto t1 = __builtin_amdgcn_permlane32_swap(asu(y1), asu(y1), false, false);
        const float h0 = rbf(silu(asf(t0[0]))) * asf(t0[1]), h1 = rbf(silu(asf(t1[0]))) * asf(t1[1]);
        if (lane < 8) {
          const int r = 2 * c8;
          L[L_O + (r & 7) * 4 + (r >> 3)] = pk2(h0, h0);
          L[L_O + ((r + 1) & 7) * 4 + (r >> 3)] = pk2(h1, h1);
        }
        STAMP(6);
      }
      BARRIER();
      if (fin) {
        if (ABL & 32) { const unsigned long long t_ = __builtin_amdgcn_s_memrealtime(); while (__builtin_amdgcn_s_memrealtime() < t_ + 150) __builtin_amdgcn_s_sleep(1); }
        v4u fv[14];
        poll<14>(rw, WS_F + rs * S_PITCH + fc * 128 + c8 * 16, 8 * S_PITCH, tag, fv);
        float f0 = 0.f, f1 = 0.f;
#pragma unroll
        for (int k = 0; k < 14; ++k) { f0 += asf(fv[k].x); f1 += asf(fv[k].z); }
        f0 += dpp<XOR8>(f0); f1 += dpp<XOR8>(f1);
        f0 = x32_sum(x16_sum(f0)); f1 = x32_sum(x16_sum(f1));
        const float g0 = rbf(silu(rbf(f0))), g1 = rbf(silu(rbf(f1)));
        STAMP(16);
        BARRIER();
        const v2u r0v = *(const LAS v2u*)(L + L_RED + 2 * c8), r1v = *(const LAS v2u*)(L + L_RED + 16 + 2 * c8),
                  r2v = *(const LAS v2u*)(L + L_RED + 32 + 2 * c8), r3v = *(const LAS v2u*)(L + L_RED + 48 + 2 * c8);
        const float s0 = rbf((asf(r0v.x) + asf(r1v.x)) + (asf(r2v.x) + asf(r3v.x))), s1v = rbf((asf(r0v.y) + asf(r1v.y)) + (asf(r2v.y) + asf(r3v.y)));
        const unsigned res = pk2(asf(xw << 16) + rbf(s0 * g0), asf(xw & 0xffff0000u) + rbf(s1v * g1));
        if (lane < 8) *(GAS unsigned*)(out + fc * 32 + lane * 4) = res;
        if (fc == 0 && lane == 0) __hip_atomic_store((GAS unsigned*)ws, tag, RLX_AGENT);
        STAMP(8);
      }
    }
    STAMP_FLUSH();
  } else {
    const int k = w;
    const unsigned hm = (lane & 1) ? 0xffff0000u : 0x0000ffffu;
    const int sa = 2 * g4 + ((lane >> 2) & 1);
    const v2u msk2 = {(lane & 2) ? 0u : hm, (lane & 2) ? hm : 0u};
    v4u kf[16], vf[16];
    const __amdgpu_buffer_rsrc_t rk = mkrs(kc + ((size_t)h * SK + (SK - WIN) + 128 * j) * 512, 128 * 512);
    const __amdgpu_buffer_rsrc_t rv = mkrs(vc + ((size_t)h * SK + (SK - WIN) + 128 * j) * 512, 128 * 512);
    const int ko = k * 2048 + (n16 >> 2) * 512 + g4 * 64 + (lane & 3) * 16;
    const int vo = rs * 512 + c8 * 16 + k * 128;
#define LDK(i) kf[i] = ldnt(rk, ko + ((i) & 1) * 256, ((i) >> 1) * 8192)
#define LDV(i) vf[i] = ldnt(rv, vo, (i) * 4096)
    const int half = isE ? (ei & 1) : (tq & 1);
    const int r0 = 256 * half + 64 * k;
    const __amdgpu_buffer_rsrc_t rx = mkrs(x, 896);
    const v4u xr = __builtin_amdgcn_raw_buffer_load_b128(rx, lane * 16, 0, 0);
    const v4u xs = __builtin_amdgcn_raw_buffer_load_b128(rx, sa * 16, r0 * 2, 0);
    __builtin_amdgcn_sched_barrier(0);
    v4u fr[8];
    {
      const int pitch = isE ? 8192 : 3584;
      const int sb = r0 * pitch + (isE ? (512 * h + 64 * (ei >> 1)) * 2 : cg * 128);
      const __amdgpu_buffer_rsrc_t ra = mkrs(isE ? wq : (tq & 2) ? wu : wg, (isE || ffn) ? DM * pitch : 0);
      const int fo = rs * 8 * pitch + c8 * 16;
#pragma unroll
      for (int i = 0; i < 8; ++i) fr[i] = ldnt(ra, fo, sb + i * pitch);
    }
    if (k == 0) STAMP(18);
    {
      float xv[8];
#pragma unroll
      for (int e = 0; e < 4; ++e) { xv[2 * e] = asf(xr[e] << 16); xv[2 * e + 1] = asf(xr[e] & 0xffff0000u); }
      const float s1 = ((xv[0] + xv[1]) + (xv[2] + xv[3])) + ((xv[4] + xv[5]) + (xv[6] + xv[7]));
      const float mean = wave_sum(s1) * (1.f / DM);
      float s2 = 0.f;
#pragma unroll
      for (int e = 0; e < 8; ++e) { const float d = xv[e] - mean; s2 += d * d; }
      s2 = lane < 56 ? s2 : 0.f;
      const float var = wave_sum(s2) * (1.f / DM);
      const float mb = rbf(mean), rden = __builtin_amdgcn_rcpf(rbf(__builtin_amdgcn_sqrtf(rbf(rbf(var) + 1e-5f))));
      unsigned xc[8];
#pragma unroll
      for (int i = 0; i < 8; ++i) {
        const float xe = asf((i & 1) ? xs[i >> 1] & 0xffff0000u : xs[i >> 1] << 16);
        const float xn = rbf(xe - mb) * rden;
        xc[i] = pk2(xn, xn);
      }
      if (k == 0) STAMP(19);
      __builtin_amdgcn_sched_barrier(0);
      f32x4 a0 = {0.f, 0.f, 0.f, 0.f}, a1 = {0.f, 0.f, 0.f, 0.f};
#pragma unroll
      for (int i = 0; i < 8; ++i) {
        const v2u am = {xc[i] & msk2.x, xc[i] & msk2.y};
        a0 = mfma16(am, v2u{fr[i].x, fr[i].y}, a0);
        a1 = mfma16(am, v2u{fr[i].z, fr[i].w}, a1);
        __builtin_amdgcn_sched_barrier(0);
        if (i >= 8 - WINL) LDK(i - (8 - WINL));
      }
      float v0, v1;
      fixup<true>(a0, a1, nlo, v0, v1);
      if (lane < 32) *(LAS v2u*)(L + L_R1 + 64 * k + 2 * gidx) = v2u{asu(v0), asu(v1)};
      asm volatile("s_waitcnt lgkmcnt(0)\n\ts_barrier" ::"s"(ep) : "memory");
      if (k == 0) STAMP(12);
    }
    const unsigned tag = ep + 1u;
#pragma unroll
    for (int i = WINL; i < 16; ++i) { VMW(WINL - 1); LDK(i); }
#pragma unroll
    for (int i = 0; i < QPOS; ++i) { VMW(WINL - 1); LDV(i); }
    v4u q2[2];
    {
      const int qo = WS_Q + h * 8192 + k * 1024 + lane * 16;
      v4u qv[2];
      qv[0] = __builtin_amdgcn_raw_buffer_load_b128(rw, qo, 0, 16);
      qv[1] = __builtin_amdgcn_raw_buffer_load_b128(rw, qo, 4096, 16);
      __builtin_amdgcn_sched_barrier(0);
      if (k == 0) STAMP(17);
#pragma unroll
      for (int i = QPOS; i < QPOS + WINL; ++i) { VMW(WINL + 1); LDV(i); }
      VMW(WINL);
      const bool qok = __all((qv[0].y == tag) & (qv[0].w == tag) & (qv[1].y == tag) & (qv[1].w == tag));
      q2[0] = __builtin_amdgcn_raw_buffer_load_b128(rw, qo, qok ? (1 << 30) : 0, 16);
      q2[1] = __builtin_amdgcn_raw_buffer_load_b128(rw, qo, qok ? (1 << 30) : 4096, 16);
      __builtin_amdgcn_sched_barrier(0);
#pragma unroll
      for (int i = QPOS + WINL; i < VB1; ++i) LDV(i);
      if (k == 0) STAMP(13);
      unsigned np = 1;
      if (!qok) {
        qv[0] = q2[0]; qv[1] = q2[1]; ++np;
        if (!__all((qv[0].y == tag) & (qv[0].w == tag) & (qv[1].y == tag) & (qv[1].w == tag))) np += poll<2>(rw, qo, 4096, tag, qv);
      }
      if (k == 0) RAW(28, np);
      if (k == 0) STAMP(3);
      L[L_Q + 64 * k + lane] = pk2(asf(qv[0].x) + asf(qv[1].x), asf(qv[0].z) + asf(qv[1].z));
    }
    BARRIER();
#pragma unroll
    for (int i = VB1; i < VB2; ++i) LDV(i);
    if (k == 0) STAMP(24);
    v4u q[4];
#pragma unroll
    for (int u = 0; u < 4; ++u) {
      const int qa = L_Q + ((lane >> 2) & 1) * 128 + (u & 1) * 64 + g4 * 16 + (lane & 3) * 4;
      q[u] = *(const LAS v4u*)(L + ((n16 >> 3) == (u >> 1) ? qa : L_Z));
    }
    float y[4];
#pragma unroll
    for (int t = 0; t < 4; ++t) {
      f32x4 d = {0.f, 0.f, 0.f, 0.f};
      d = mfma32(q[0], kf[2 * t], d); d = mfma32(q[1], kf[2 * t + 1], d);
      d = mfma32(q[2], kf[2 * t + 8], d); d = mfma32(q[3], kf[2 * t + 9], d);
      const float lo = (lane & 1) ? d[1] : d[0], hi = (lane & 1) ? d[3] : d[2];
      y[t] = (lane & 2) ? hi : lo;
    }
    {
      const float k0 = (lane & 2) ? y[2] : y[0], s0 = (lane & 2) ? y[0] : y[2], k1 = (lane & 2) ? y[3] : y[1], s1 = (lane & 2) ? y[1] : y[3];
      y[0] = k0 + dpp<XOR2>(s0); y[1] = k1 + dpp<XOR2>(s1);
    }
    const float sc = rbf(((lane & 1) ? y[1] : y[0]) + dpp<XOR1>((lane & 1) ? y[0] : y[1]));
    float m = sc; m = fmaxf(m, dpp<XOR1>(m)); m = fmaxf(m, dpp<XOR2>(m)); m = fmaxf(m, dpp<XOR7>(m)); m = fmaxf(m, dpp<XOR8>(m)); m = x32_max(m);
    const int a = g4 & 1;
    if ((lane & 0x2f) == 0) L[L_WM + 2 * k + a] = asu(m);
    BARRIER();
#pragma unroll
    for (int i = VB2; i < VB3; ++i) LDV(i);
    const float M = fmaxf(fmaxf(asf(L[L_WM + a]), asf(L[L_WM + 2 + a])), fmaxf(asf(L[L_WM + 4 + a]), asf(L[L_WM + 6 + a])));
    const float p = __builtin_amdgcn_exp2f((sc - M) * C2);
    L[L_P + a * 128 + (4 * (k & 1) + (n16 >> 2)) * 16 + 2 * ((lane & 3) + 4 * (g4 >> 1)) + (k >> 1)] = pk2(p, p);
    float l = p; l += dpp<XOR1>(l); l += dpp<XOR2>(l); l += dpp<XOR7>(l); l += dpp<XOR8>(l); l = x32_sum(l);
    if ((lane & 0x2f) == 0) L[L_WL + 2 * k + a] = asu(l);
    BARRIER();
    if (k == 0) STAMP(9);
    const int cA = 2 * k, sA = cA * 128, sB = k < 3 ? sA + 128 : (1 << 20);
    const __amdgpu_buffer_rsrc_t ro = mkrs(isE ? wo + (size_t)(512 * h + 32 * ei) * 896 : wd + (size_t)(16 * (ffn ? fi : 0)) * 896, (ABL & 2) ? 0 : isE ? 32 * 896 : ffn ? 16 * 896 : 0);
    const int oo = rs * 896 + c8 * 16;
    v4u wf[8];
#define LDW(i) wf[i] = ldnt(ro, oo, ((i) >> 1) * 8 * 896 + (((i) & 1) ? sB : sA))
    {
      v4u pa[4];
#pragma unroll
      for (int u = 0; u < 4; ++u) pa[u] = *(const LAS v4u*)(L + L_P + (n16 >> 3) * 128 + sa * 16 + 4 * u);
      f32x4 a0 = {0.f, 0.f, 0.f, 0.f}, a1 = {0.f, 0.f, 0.f, 0.f};
#pragma unroll
      for (int i = 0; i < 16; ++i) {
        const unsigned c = pa[i >> 2][i & 3];
        const v2u am = {c & msk2.x, c & msk2.y};
        a0 = mfma16(am, v2u{vf[i].x, vf[i].y}, a0);
        a1 = mfma16(am, v2u{vf[i].z, vf[i].w}, a1);
        __builtin_amdgcn_sched_barrier(0);
        if (i == QPOS + WINL) asm volatile("" ::"v"(q2[0]), "v"(q2[1]));
        {
          const int nx = i + WOWIN;
          if (nx >= VB3 && nx < 16) LDV(nx);
          if (nx >= 16 && nx < 16 + WOPRE) LDW(nx - 16);
        }
      }
      float v0, v1;
      fixup<true>(a0, a1, nlo, v0, v1);
      stsc(rw, WS_O + pb * O_PITCH + (lane >> 5) * 2048 + k * 512 + gidx * 16, v0, v1, tag);
      __builtin_amdgcn_sched_barrier(0);
    }
    if (k == 3) STAMP(14);
    if (k == 0) STAMP(15);
    const v4u msk = {(c8 >> 1) == 0 ? hm : 0u, (c8 >> 1) == 1 ? hm : 0u, (c8 >> 1) == 2 ? hm : 0u, (c8 >> 1) == 3 ? hm : 0u};
    if (isE) {
#pragma unroll
      for (int i = (WOPRE < WOWIN ? WOPRE : WOWIN); i < 8; ++i) LDW(i);
      if (ABL & 8) { VMW(0); if (k == 0) STAMP(26); }
      if (ABL & 16) { const unsigned long long t_ = __builtin_amdgcn_s_memrealtime(); while (__builtin_amdgcn_s_memrealtime() < t_ + ODLY) __builtin_amdgcn_s_sleep(1); if (k == 0) STAMP(27); }
      v4u ov[2];
      const int po = WS_O + (h + 8 * (8 * k + g4)) * O_PITCH + ei * 256 + n16 * 16;
      ov[0] = __builtin_amdgcn_raw_buffer_load_b128(rw, po, 0, 16);
      ov[1] = __builtin_amdgcn_raw_buffer_load_b128(rw, po, 32 * O_PITCH, 16);
      __builtin_amdgcn_sched_barrier(0);
      lds_spin(FL + 7, 1u);
      const float w0 = asf(L[L_W + 8 * k + g4]), w1 = asf(L[L_W + 8 * k + 4 + g4]);
      asm volatile("" ::"v"(w0), "v"(w1));
      unsigned np2 = 1;
      if (!__all((ov[0].y == tag) & (ov[0].w == tag) & (ov[1].y == tag) & (ov[1].w == tag))) np2 += poll<2>(rw, po, 32 * O_PITCH, tag, ov);
      if (k == 0) RAW(30, np2);
      if (k == 0) STAMP(7);
      float o0 = fmaf(w1, asf(ov[1].x), w0 * asf(ov[0].x)), o1 = fmaf(w1, asf(ov[1].z), w0 * asf(ov[0].z));
      o0 = x32_sum(x16_sum(o0)); o1 = x32_sum(x16_sum(o1));
      if (lane < 16) { const int r = 2 * lane;
        L[L_OP + k * 32 + (r & 7) * 4 + (r >> 3)] = asu(o0);
        L[L_OP + k * 32 + ((r + 1) & 7) * 4 + (r >> 3)] = asu(o1); }
      BARRIER();
      if (k == 0) STAMP(25);
      f32x4 os = __builtin_bit_cast(f32x4, *(const LAS v4u*)(L + L_OP + rs * 4)) + __builtin_bit_cast(f32x4, *(const LAS v4u*)(L + L_OP + 32 + rs * 4));
      os += __builtin_bit_cast(f32x4, *(const LAS v4u*)(L + L_OP + 64 + rs * 4)) + __builtin_bit_cast(f32x4, *(const LAS v4u*)(L + L_OP + 96 + rs * 4));
      f32x4 a0 = {0.f, 0.f, 0.f, 0.f}, a1 = {0.f, 0.f, 0.f, 0.f};
#pragma unroll
      for (int i = 0; i < 4; ++i) { const v4u am = amask(pk2(os[i], os[i]), msk); a0 = mfma32(am, wf[2 * i], a0); a1 = mfma32(am, wf[2 * i + 1], a1); }
      float v0, v1;
      fixup<false>(a0, a1, nlo, v0, v1);
      if (k < 3 || lane < 32) stsc(rw, WS_S + (h * 16 + ei) * S_PITCH + (cA + (lane >> 5)) * 512 + gidx * 16, v0, v1, tag);
      if (k == 0) STAMP(10);
    } else {
      BARRIER();
      if (ffn) {
        const v4u ov = *(const LAS v4u*)(L + L_O + rs * 4);
        f32x4 a0 = {0.f, 0.f, 0.f, 0.f}, a1 = {0.f, 0.f, 0.f, 0.f};
#pragma unroll
        for (int i = 0; i < 2; ++i) { const v4u am = amask(ov[i], msk); a0 = mfma32(am, wf[2 * i], a0); a1 = mfma32(am, wf[2 * i + 1], a1); }
        float v0, v1;
        fixup<false>(a0, a1, nlo, v0, v1);
        if (k < 3 || lane < 32) stsc(rw, WS_F + fi * S_PITCH + (cA + (lane >> 5)) * 512 + gidx * 16, v0, v1, tag);
        if (k == 0) STAMP(11);
      }
      if (fin) {
        if (ABL & 32) { const unsigned long long t_ = __builtin_amdgcn_s_memrealtime(); while (__builtin_amdgcn_s_memrealtime() < t_ + 200) __builtin_amdgcn_s_sleep(1); }
        v4u sv[4];
        const unsigned np3 = poll<4>(rw, WS_S + (32 * k + rs) * S_PITCH + fc * 128 + c8 * 16, 8 * S_PITCH, tag, sv); if (k == 0) RAW(31, np3); (void)np3;
        float s0 = (asf(sv[0].x) + asf(sv[1].x)) + (asf(sv[2].x) + asf(sv[3].x)), s1 = (asf(sv[0].z) + asf(sv[1].z)) + (asf(sv[2].z) + asf(sv[3].z));
        s0 += dpp<XOR8>(s0); s1 += dpp<XOR8>(s1);
        s0 = x32_sum(x16_sum(s0)); s1 = x32_sum(x16_sum(s1));
        if (lane < 8) *(LAS v2u*)(L + L_RED + 16 * k + 2 * c8) = v2u{asu(s0), asu(s1)};
        if (k == 0) STAMP(20); if (k == 1) STAMP(21); if (k == 2) STAMP(22); if (k == 3) STAMP(23);
        BARRIER();
      }
    }
    STAMP_FLUSH();
  }
}

extern "C" void kernel_launch(void* const* d_in, const int* in_sizes, int n_in, void* d_out, int out_size, void* d_ws, size_t ws_size,
                              hipStream_t stream) {
  fused_decode<<<NWG, NTHR, 0, stream>>>((const GAS char*)d_in[0], (const GAS char*)d_in[1], (const GAS char*)d_in[2], (const GAS char*)d_in[3],
                                         (const GAS char*)d_in[4], (const GAS char*)d_in[6], (const GAS char*)d_in[7], (const GAS char*)d_in[8],
                                         (GAS char*)d_out, (GAS char*)d_ws);
}
```

```cpp
#include <hip/hip_runtime.h>
#include <stdint.h>

#define GAS __attribute__((address_space(1)))
#define LAS __attribute__((address_space(3)))
typedef unsigned v4u __attribute__((ext_vector_type(4)));
typedef unsigned v2u __attribute__((ext_vector_type(2)));
typedef float f32x4 __attribute__((ext_vector_type(4)));
typedef __bf16 bf16x8 __attribute__((ext_vector_type(8)));
typedef __bf16 bf16x2 __attribute__((ext_vector_type(2)));
typedef short s16x4 __attribute__((ext_vector_type(4)));
#define RLX_AGENT __ATOMIC_RELAXED, __HIP_MEMORY_SCOPE_AGENT

#define WINL 6
#define VB1 10
#define VB2 12
#define VB3 14
#define VB4 VB3
#define QPOS 0
#define ABL 0
#define QLAUX 16
#define ROT 1
#define WOWIN 7
#define QSAUX 16
#define ODLY 12
#define CODE_LINES 64

constexpr int DM = 448, SK = 65536, WIN = 4096;
constexpr int NWG = 256, NTHR = 320;
constexpr int WS_Q = 4096;
constexpr int WS_GU = WS_Q + 8 * 8192;
constexpr int WS_ML = WS_GU + 28 * 2048;
constexpr int O_PITCH = 2176;
constexpr int WS_O = WS_ML + 8192;
constexpr int S_PITCH = 1792;
constexpr int WS_S = WS_O + 256 * O_PITCH;
constexpr int F_PITCH = 3584;
constexpr int WS_F = WS_S + 128 * S_PITCH;
constexpr int WS_END = WS_F + 56 * F_PITCH;
constexpr int OOB = 1 << 30;
constexpr int L_R1 = 0;
constexpr int L_Q = 256;
constexpr int L_P = 512;
constexpr int L_W = 800;
constexpr int L_WM = 832;
constexpr int L_WL = 840;
constexpr int L_FLAG = 928;
constexpr int L_Z = 944;
constexpr int L_OP = 960;
constexpr int L_TOTAL = 21504;
constexpr unsigned SPIN_CAP = 1u << 16;

#define RAW(k, v)
#define STAMP(k)
#define STAMP_DECL
#define STAMP_FLUSH()

__device__ __forceinline__ float asf(unsigned u) { return __builtin_bit_cast(float, u); }
__device__ __forceinline__ unsigned asu(float f) { return __builtin_bit_cast(unsigned, f); }
__device__ __forceinline__ float blo(unsigned u) { return asf(u << 16); }
__device__ __forceinline__ float bhi(unsigned u) { return asf(u & 0xffff0000u); }
__device__ __forceinline__ float rbf(float f) { return (float)(__bf16)f; }
__device__ __forceinline__ unsigned pk2(float lo, float hi) { bf16x2 t = {(__bf16)lo, (__bf16)hi}; return __builtin_bit_cast(unsigned, t); }
__device__ __forceinline__ f32x4 mfma32(v4u a, v4u b, f32x4 c) {
  return __builtin_amdgcn_mfma_f32_16x16x32_bf16(__builtin_bit_cast(bf16x8, a), __builtin_bit_cast(bf16x8, b), c, 0, 0, 0);
}
__device__ __forceinline__ f32x4 mfma16(v2u a, v2u b, f32x4 c) {
  return __builtin_amdgcn_mfma_f32_16x16x16bf16_1k(__builtin_bit_cast(s16x4, a), __builtin_bit_cast(s16x4, b), c, 0, 0, 0);
}
template <int CTRL> __device__ __forceinline__ float dpp(float x) {
  return asf((unsigned)__builtin_amdgcn_mov_dpp((int)asu(x), CTRL, 0xf, 0xf, true));
}
constexpr int XOR1 = 0xB1, XOR2 = 0x4E, XOR7 = 0x141, XOR8 = 0x128;
__device__ __forceinline__ float sw16(float a, float b) { auto s = __builtin_amdgcn_permlane16_swap(asu(a), asu(b), false, false); return asf(s[0]) + asf(s[1]); }
__device__ __forceinline__ float sw32(float a, float b) { auto s = __builtin_amdgcn_permlane32_swap(asu(a), asu(b), false, false); return asf(s[0]) + asf(s[1]); }
__device__ __forceinline__ float x16_max(float x) { auto s = __builtin_amdgcn_permlane16_swap(asu(x), asu(x), false, false); return fmaxf(asf(s[0]), asf(s[1])); }
__device__ __forceinline__ float x32_max(float x) { auto s = __builtin_amdgcn_permlane32_swap(asu(x), asu(x), false, false); return fmaxf(asf(s[0]), asf(s[1])); }
__device__ __forceinline__ float row_sum(float x) { x += dpp<XOR1>(x); x += dpp<XOR2>(x); x += dpp<XOR7>(x); x += dpp<XOR8>(x); return x; }
__device__ __forceinline__ float row_max(float x) { x = fmaxf(x, dpp<XOR1>(x)); x = fmaxf(x, dpp<XOR2>(x)); x = fmaxf(x, dpp<XOR7>(x)); return fmaxf(x, dpp<XOR8>(x)); }
__device__ __forceinline__ float wave_sum(float x) { x = row_sum(x); x = sw16(x, x); return sw32(x, x); }
__device__ __forceinline__ float red4(float v0, float v1, float v2, float v3) { const float t = sw16(sw32(v0, v2), sw32(v1, v3)); return t + dpp<XOR8>(t); }
__device__ __forceinline__ __amdgpu_buffer_rsrc_t mkrs(const GAS char* p, int bytes) { return __builtin_amdgcn_make_buffer_rsrc((void*)p, 0, bytes, 0x00020000); }
__device__ __forceinline__ v4u ldnt(__amdgpu_buffer_rsrc_t rs, int voff, int soff) {
  const v4u r = __builtin_amdgcn_raw_buffer_load_b128(rs, voff, soff, 2);
  __builtin_amdgcn_sched_barrier(0);
  return r;
}
__device__ __forceinline__ v4u ldsc(__amdgpu_buffer_rsrc_t rs, int voff, int soff) { return __builtin_amdgcn_raw_buffer_load_b128(rs, voff, soff, 16); }
template <int AUX = 16> __device__ __forceinline__ void stsc(__amdgpu_buffer_rsrc_t rs, int off, unsigned d0, unsigned d1, unsigned tag) {
  v4u g = {d0, tag, d1, tag};
  __builtin_amdgcn_raw_buffer_store_b128(g, rs, off, 0, AUX);
}
#define BARRIER() asm volatile("s_waitcnt lgkmcnt(0)\n\ts_barrier" ::: "memory")
#define CFENCE() asm volatile("" ::: "memory")
#define VMW(n) asm volatile("s_waitcnt vmcnt(%0)" ::"i"(n) : "memory")
__device__ __forceinline__ v4u amask(unsigned x, const v4u& m) { return v4u{x & m.x, x & m.y, x & m.z, x & m.w}; }
template <bool R16> __device__ __forceinline__ f32x4 fold4(const f32x4& A, const f32x4& B) {
  f32x4 s;
#pragma unroll
  for (int r = 0; r < 4; ++r) {
    auto t = R16 ? __builtin_amdgcn_permlane16_swap(asu(A[r]), asu(B[r]), false, false) : __builtin_amdgcn_permlane32_swap(asu(A[r]), asu(B[r]), false, false);
    s[r] = asf(t[0]) + dpp<XOR8>(asf(t[1]));
  }
  return s;
}
template <bool R16> __device__ __forceinline__ void fold2(const f32x4& A, const f32x4& B, bool nlo, float& v0, float& v1) {
  float s[4];
#pragma unroll
  for (int r = 0; r < 4; ++r) {
    auto t = R16 ? __builtin_amdgcn_permlane16_swap(asu(A[r]), asu(B[r]), false, false) : __builtin_amdgcn_permlane32_swap(asu(A[r]), asu(B[r]), false, false);
    const float a = asf(t[0]), b = asf(t[1]);
    s[r] = r < 2 ? a + dpp<XOR8>(b) : dpp<XOR8>(a) + b;
  }
  v0 = nlo ? s[0] : s[2];
  v1 = nlo ? s[1] : s[3];
}
template <int N> __device__ __forceinline__ bool tags_ok(const v4u (&v)[N], unsigned tag) {
  bool ok = true;
#pragma unroll
  for (int k = 0; k < N; ++k) ok &= (v[k].y == tag) & (v[k].w == tag);
  return __all(ok);
}
template <int N> __device__ __forceinline__ unsigned poll(__amdgpu_buffer_rsrc_t rs, int off, int stride, unsigned tag, v4u (&v)[N]) {
  unsigned spins = 0;
#pragma unroll 1
  for (; spins < SPIN_CAP; ++spins) {
    CFENCE();
#pragma unroll
    for (int k = 0; k < N; ++k) v[k] = ldsc(rs, off, k * stride);
    if (tags_ok<N>(v, tag)) break;
  }
  return spins + 1;
}
template <bool SLEEP> __device__ __forceinline__ void lds_wait(volatile LAS unsigned* w, unsigned want) {
#pragma unroll 1
  for (unsigned spins = 0; spins < (SPIN_CAP << 6); ++spins) {
    if (__builtin_amdgcn_readfirstlane(*w) >= want) break;
    if (SLEEP) __builtin_amdgcn_s_sleep(1);
  }
  CFENCE();
}
__device__ __forceinline__ float silu(float x) { return x * __builtin_amdgcn_rcpf(1.f + __expf(-x)); }

__global__ void __launch_bounds__(NTHR) fused_decode(const GAS char* __restrict__ x, const GAS char* __restrict__ kc, const GAS char* __restrict__ vc,
                                                     const GAS char* __restrict__ wq, const GAS char* __restrict__ wo, const GAS char* __restrict__ wg,
                                                     const GAS char* __restrict__ wu, const GAS char* __restrict__ wd, GAS char* out, GAS char* ws) {
  __shared__ __attribute__((aligned(16))) unsigned lds_[L_TOTAL];
  LAS unsigned* L = (LAS unsigned*)lds_;
  volatile LAS unsigned* FL = (volatile LAS unsigned*)(L + L_FLAG);
  const int b = blockIdx.x;
  const int h = (b >> 3) & 7, j = (b & 7) * 4 + (b >> 6);
  const int tid = threadIdx.x, lane = tid & 63, w = __builtin_amdgcn_readfirstlane(tid >> 6);
  const int u = (b & 63) + 64 * (b >> 7);
  STAMP_DECL;
  if (w == 0) STAMP(5);
  unsigned cpf = 0, cpf2 = 0;
  if (w == 4 && u >= 120) {
    const unsigned long long pc = __builtin_amdgcn_s_getpc() & ~255ull;
    cpf = *(const GAS unsigned*)(pc + 1024 + lane * 128);
    if (lane + 64 < CODE_LINES) cpf2 = *(const GAS unsigned*)(pc + 1024 + 8192 + lane * 128);
  }
  asm volatile("" ::"s"(x), "s"(kc), "s"(vc), "s"(wq), "s"(wo), "s"(wg), "s"(wu), "s"(wd), "s"(out), "s"(ws));
  const unsigned ep = *(const __attribute__((address_space(4))) unsigned*)(unsigned long long)ws;
  const bool isE = (j & 1) == 0;
  const int ei = j >> 1;
  const bool isD = !isE && u < 56;
  const bool fin = !isE && u >= 114;
  const int fc = u - 114;
  const int rs = lane >> 3, c8 = lane & 7, g4 = lane >> 4, n16 = lane & 15;
  const bool nlo = n16 < 8;
  const int gidx = 4 * c8 + 2 * (g4 & 1) + (n16 >> 3);
  const __amdgpu_buffer_rsrc_t rw = mkrs(ws, WS_END);
  constexpr float C2 = 0.0625f * 1.4426950408889634f;

  if (w == 4) {
    STAMP(0);
    __builtin_amdgcn_s_setprio(3);
    if (lane < 20) FL[lane] = 0u;
    asm volatile("s_waitcnt lgkmcnt(0)\n\ts_barrier" ::"s"(ep) : "memory");
    const unsigned tag = ep + 1u;
    if (u < 120) {
      const v2u r0 = *(const LAS v2u*)(L + L_R1 + 2 * (lane & 31)), r1 = *(const LAS v2u*)(L + L_R1 + 64 + 2 * (lane & 31)),
                r2 = *(const LAS v2u*)(L + L_R1 + 128 + 2 * (lane & 31)), r3 = *(const LAS v2u*)(L + L_R1 + 192 + 2 * (lane & 31));
      const float v0 = (asf(r0.x) + asf(r1.x)) + (asf(r2.x) + asf(r3.x)), v1 = (asf(r0.y) + asf(r1.y)) + (asf(r2.y) + asf(r3.y));
      const int hf = isE ? 1 : 0;
      const int dst = u < 64 ? WS_Q + (u >> 3) * 8192 + hf * 4096 + (u & 7) * 512 : u < 92 ? WS_GU + (u - 64) * 2048 + hf * 512 : WS_GU + (u - 92) * 2048 + 1024 + hf * 512;
      if (lane < 32) stsc<QSAUX>(rw, dst + lane * 16, asu(v0), asu(v1), tag);
    }
    STAMP(2);
    const unsigned xw = __builtin_amdgcn_raw_buffer_load_b16(mkrs(x, 896), (4 * c8 + g4) * 2, fin ? fc * 64 : 0, 0);
    const int go = WS_GU + (u >> 1) * 2048 + g4 * 512 + (u & 1) * 256 + n16 * 16;
    v4u gu[1];
    lds_wait<true>(FL + 5, 1u);
    gu[0] = ldsc(rw, go, isD ? 0 : OOB);
    asm volatile("" ::"v"(cpf), "v"(cpf2));
    BARRIER();
    BARRIER();
    BARRIER();
    if (lane < 2) {
      const float M = fmaxf(fmaxf(asf(L[L_WM + lane]), asf(L[L_WM + 2 + lane])), fmaxf(asf(L[L_WM + 4 + lane]), asf(L[L_WM + 6 + lane])));
      const float Ls = (asf(L[L_WL + lane]) + asf(L[L_WL + 2 + lane])) + (asf(L[L_WL + 4 + lane]) + asf(L[L_WL + 6 + lane]));
      stsc(rw, WS_ML + (h * 2 + lane) * 512 + j * 16, asu(M), asu(Ls), tag);
    }
    STAMP(4);
    if (isE) {
      lds_wait<true>(FL + 8, 1u);
      v4u ml[1];
      const unsigned np1 = poll<1>(rw, WS_ML + (h * 2 + (ei >> 3)) * 512 + (lane & 31) * 16, 0, tag, ml); RAW(29, np1); (void)np1;
      const float m = asf(ml[0].x);
      const float M = x16_max(row_max(m));
      const float e = __builtin_amdgcn_exp2f((m - M) * C2);
      float Ls = row_sum(e * asf(ml[0].z)); Ls = sw16(Ls, Ls);
      if (lane < 32) L[L_W + lane] = asu(e * __builtin_amdgcn_rcpf(Ls));
      if (lane == 0) FL[7] = 1u;
      STAMP(6);
    } else if (isD) {
      if (!tags_ok<1>(gu, tag)) poll<1>(rw, go, 0, tag, gu);
      const float y = rbf(sw16(asf(gu[0].x), asf(gu[0].z)));
      auto t = __builtin_amdgcn_permlane32_swap(asu(y), asu(y), false, false);
      const float hv = rbf(rbf(silu(asf(t[0]))) * asf(t[1]));
      const int r = 2 * n16 + (g4 & 1);
      if (lane < 32) L[L_OP + (r & 7) * 4 + (r >> 3)] = asu(hv);
      STAMP(6);
      BARRIER();
    } else if (fin) {
      lds_wait<true>(FL + 8, 1u);
      v4u fv[14];
      poll<14>(rw, WS_F + g4 * F_PITCH + fc * 256 + n16 * 16, 4 * F_PITCH, tag, fv);
      float f0 = 0.f, f1 = 0.f;
#pragma unroll
      for (int i = 0; i < 14; ++i) { f0 += asf(fv[i].x); f1 += asf(fv[i].z); }
      float f = sw16(f0, f1); f = sw32(f, f);
      const float g = rbf(silu(rbf(f)));
      if (lane < 32) L[L_W + 2 * n16 + (g4 & 1)] = asu(g);
      asm volatile("s_waitcnt lgkmcnt(0)" ::: "memory");
      const float gc = asf(L[L_W + 4 * c8 + g4]);
      STAMP(16);
      BARRIER();
      const int so = L_OP + g4 * 8 + c8;
      const float s = rbf((asf(L[so]) + asf(L[so + 32])) + (asf(L[so + 64]) + asf(L[so + 96])));
      const float res = blo(xw) + rbf(s * gc);
      if (nlo) *(GAS unsigned short*)(out + (fc * 32 + 4 * c8 + g4) * 2) = (unsigned short)pk2(res, res);
      if (fc == 0 && lane == 0) __hip_atomic_store((GAS unsigned*)ws, tag, RLX_AGENT);
      STAMP(8);
    }
    STAMP_FLUSH();
  } else {
    const int k = w;
    const unsigned hm = (lane & 1) ? 0xffff0000u : 0x0000ffffu;
    const int sa = 2 * g4 + ((lane >> 2) & 1);
    const v2u msk2 = {(lane & 2) ? 0u : hm, (lane & 2) ? hm : 0u};
    v4u kf[16], vf[16], wf[8];
    const int NI = isE ? 6 : 8;
    const int r0 = (isE ? 256 : 0) + 8 * NI * k;
    const __amdgpu_buffer_rsrc_t rx = mkrs(x, 896);
    const v4u xr = __builtin_amdgcn_raw_buffer_load_b128(rx, lane * 16, 0, 0);
    const v4u xs = __builtin_amdgcn_raw_buffer_load_b128(rx, sa * NI * 2, r0 * 2, 0);
    __builtin_amdgcn_sched_barrier(0);
    v4u fr[8];
    {
      const int pitch = u < 64 ? 8192 : 3584;
      const int sb = r0 * pitch + (u < 64 ? u : u < 92 ? u - 64 : u - 92) * 128;
      const __amdgpu_buffer_rsrc_t ra = mkrs(u < 64 ? wq : u < 92 ? wg : wu, u < 120 ? DM * pitch : 0);
      const int fo = rs * NI * pitch + c8 * 16;
#pragma unroll
      for (int i = 0; i < 8; ++i) fr[i] = ldnt(ra, fo, (i >= 6 && isE) ? OOB : sb + i * pitch);
    }
    const int cA = 2 * k, sA = cA * 128, sB = k < 3 ? sA + 128 : OOB;
    const __amdgpu_buffer_rsrc_t ro = mkrs(isE ? wo + (size_t)(512 * h + 32 * ei) * 896 : wd + (size_t)(32 * (isD ? u : 0)) * 896, (isE || isD) ? 32 * 896 : 0);
    const int oo = rs * 896 + c8 * 16;
#define LDW(i) wf[i] = ldnt(ro, oo, ((i) >> 1) * 8 * 896 + (((i) & 1) ? sB : sA))
    wf[0] = ldnt(ro, oo, isE ? sA : OOB);
    wf[1] = ldnt(ro, oo, isE ? sB : OOB);
    const __amdgpu_buffer_rsrc_t rk = mkrs(kc + ((size_t)h * SK + (SK - WIN) + 128 * j) * 512, 128 * 512);
    const __amdgpu_buffer_rsrc_t rv = mkrs(vc + ((size_t)h * SK + (SK - WIN) + 128 * j) * 512, 128 * 512);
    const int ko = k * 2048 + (n16 >> 2) * 512 + g4 * 64 + (lane & 3) * 16;
    const int vo = rs * 512 + c8 * 16 + k * 128;
    const int rot = ROT ? ((b + (b >> 3) + (b >> 6)) & 7) * 8192 : 0;
#define LDK(i) kf[i] = ldnt(rk, ko + ((i) & 1) * 256, (((i) >> 1) * 8192 + rot) & 0xffff)
#define LDV(i) vf[i] = ldnt(rv, vo, ((i) * 4096 + rot) & 0xffff)
#define LDS_(n) do { if ((n) < 16) LDV((n) < 16 ? (n) : 0); else LDW((n) >= 16 ? (n) - 14 : 2); } while (0)
    if (k == 0) STAMP(18);
    {
      float xv[8];
#pragma unroll
      for (int e = 0; e < 4; ++e) { xv[2 * e] = blo(xr[e]); xv[2 * e + 1] = bhi(xr[e]); }
      const float s1 = ((xv[0] + xv[1]) + (xv[2] + xv[3])) + ((xv[4] + xv[5]) + (xv[6] + xv[7]));
      const float mean = wave_sum(s1) * (1.f / DM);
      float s2 = 0.f;
#pragma unroll
      for (int e = 0; e < 8; ++e) { const float d = xv[e] - mean; s2 += d * d; }
      s2 = lane < 56 ? s2 : 0.f;
      const float var = wave_sum(s2) * (1.f / DM);
      const float mb = rbf(mean), rden = __builtin_amdgcn_rcpf(rbf(__builtin_amdgcn_sqrtf(rbf(rbf(var) + 1e-5f))));
      unsigned xc[8];
#pragma unroll
      for (int i = 0; i < 8; ++i) {
        const float xn = rbf(((i & 1) ? bhi(xs[i >> 1]) : blo(xs[i >> 1])) - mb) * rden;
        xc[i] = pk2(xn, xn);
      }
      if (k == 0) STAMP(19);
      __builtin_amdgcn_sched_barrier(0);
      f32x4 a0 = {0.f, 0.f, 0.f, 0.f}, a1 = {0.f, 0.f, 0.f, 0.f};
#pragma unroll
      for (int i = 0; i < 8; ++i) {
        const v2u am = {xc[i] & msk2.x, xc[i] & msk2.y};
        a0 = mfma16(am, v2u{fr[i].x, fr[i].y}, a0);
        a1 = mfma16(am, v2u{fr[i].z, fr[i].w}, a1);
        __builtin_amdgcn_sched_barrier(0);
        if (i >= 8 - WINL) LDK(i - (8 - WINL));
      }
      float v0, v1;
      fold2<true>(a0, a1, nlo, v0, v1);
      if (lane < 32) *(LAS v2u*)(L + L_R1 + 64 * k + 2 * gidx) = v2u{asu(v0), asu(v1)};
      asm volatile("s_waitcnt lgkmcnt(0)\n\ts_barrier" ::"s"(ep) : "memory");
      if (k == 0) STAMP(12);
    }
    const unsigned tag = ep + 1u;
#pragma unroll
    for (int i = WINL; i < 16; ++i) { VMW(WINL - 1); LDK(i); }
#pragma unroll
    for (int i = 0; i < QPOS; ++i) { VMW(WINL - 1); LDV(i); }
    v4u q2[2];
    {
      const int qo = WS_Q + h * 8192 + k * 1024 + lane * 16;
      v4u qv[2];
      qv[0] = __builtin_amdgcn_raw_buffer_load_b128(rw, qo, (ABL & 1) ? OOB : 0, QLAUX);
      qv[1] = __builtin_amdgcn_raw_buffer_load_b128(rw, qo, (ABL & 3) ? OOB : 4096, QLAUX);
      __builtin_amdgcn_sched_barrier(0);
      if (lane == 0) FL[5] = 1u;
      if (k == 0) STAMP(17);
#pragma unroll
      for (int i = QPOS; i < QPOS + WINL; ++i) { VMW(WINL + 1); LDV(i); }
      VMW(WINL);
      const bool qok = (ABL & 7) ? (qv[0].y != 12345u || tag != 77u) : tags_ok<2>(qv, tag);
      q2[0] = ldsc(rw, qo, qok ? OOB : 0);
      q2[1] = ldsc(rw, qo, qok ? OOB : 4096);
      __builtin_amdgcn_sched_barrier(0);
#pragma unroll
      for (int i = QPOS + WINL; i < VB1; ++i) LDS_(i);
      if (k == 0) STAMP(13);
      unsigned np = 1;
      if (!qok) {
        qv[0] = q2[0]; qv[1] = q2[1]; ++np;
        if (!tags_ok<2>(qv, tag)) np += poll<2>(rw, qo, 4096, tag, qv);
      }
      if (k == 0) RAW(28, np);
      if (k == 0) STAMP(3);
      if (k == 1) STAMP(26);
      if (k == 2) STAMP(27);
      if (k == 3) STAMP(1);
      L[L_Q + 64 * k + lane] = pk2(asf(qv[0].x) + asf(qv[1].x), asf(qv[0].z) + asf(qv[1].z));
    }
    BARRIER();
#pragma unroll
    for (int i = VB1; i < VB2; ++i) LDS_(i);
    if (k == 0) STAMP(24);
    v4u q[4];
#pragma unroll
    for (int t = 0; t < 4; ++t) {
      const int qa = L_Q + ((lane >> 2) & 1) * 128 + (t & 1) * 64 + g4 * 16 + (lane & 3) * 4;
      q[t] = *(const LAS v4u*)(L + ((n16 >> 3) == (t >> 1) ? qa : L_Z));
    }
    float y[4];
#pragma unroll
    for (int t = 0; t < 4; ++t) {
      f32x4 d = {0.f, 0.f, 0.f, 0.f};
      d = mfma32(q[0], kf[2 * t], d); d = mfma32(q[1], kf[2 * t + 1], d);
      d = mfma32(q[2], kf[2 * t + 8], d); d = mfma32(q[3], kf[2 * t + 9], d);
      const float lo = (lane & 1) ? d[1] : d[0], hi = (lane & 1) ? d[3] : d[2];
      y[t] = (lane & 2) ? hi : lo;
    }
    {
      const float k0 = (lane & 2) ? y[2] : y[0], s0 = (lane & 2) ? y[0] : y[2], k1 = (lane & 2) ? y[3] : y[1], s1 = (lane & 2) ? y[1] : y[3];
      y[0] = k0 + dpp<XOR2>(s0); y[1] = k1 + dpp<XOR2>(s1);
    }
    const float sc = rbf(((lane & 1) ? y[1] : y[0]) + dpp<XOR1>((lane & 1) ? y[0] : y[1]));
    const float m = x32_max(row_max(sc));
    const int a = g4 & 1;
    if ((lane & 0x2f) == 0) L[L_WM + 2 * k + a] = asu(m);
    BARRIER();
#pragma unroll
    for (int i = VB2; i < VB3; ++i) LDS_(i);
    const float M = fmaxf(fmaxf(asf(L[L_WM + a]), asf(L[L_WM + 2 + a])), fmaxf(asf(L[L_WM + 4 + a]), asf(L[L_WM + 6 + a])));
    const float p = __builtin_amdgcn_exp2f((sc - M) * C2);
    L[L_P + a * 128 + (4 * (k & 1) + (n16 >> 2)) * 16 + 2 * ((lane & 3) + 4 * (g4 >> 1)) + (k >> 1)] = pk2(p, p);
    float l = row_sum(p); l = sw32(l, l);
    if ((lane & 0x2f) == 0) L[L_WL + 2 * k + a] = asu(l);
    BARRIER();
#pragma unroll
    for (int i = VB3; i < VB4; ++i) LDS_(i);
    if (k == 0) STAMP(9);
    {
      v4u pa[4];
#pragma unroll
      for (int t = 0; t < 4; ++t) pa[t] = *(const LAS v4u*)(L + L_P + (n16 >> 3) * 128 + sa * 16 + 4 * t);
      const int od = WS_O + (h + 8 * j) * O_PITCH + (lane >> 5) * 1024 + k * 256 + (2 * c8 + (g4 & 1)) * 16 + (nlo ? 0 : OOB);
      f32x4 a0 = {0.f, 0.f, 0.f, 0.f}, a1 = {0.f, 0.f, 0.f, 0.f};
#pragma unroll
      for (int i = 0; i < 16; ++i) {
        const unsigned c = pa[i >> 2][i & 3];
        const v2u am = {c & msk2.x, c & msk2.y};
        a0 = mfma16(am, v2u{vf[i].x, vf[i].y}, a0);
        a1 = mfma16(am, v2u{vf[i].z, vf[i].w}, a1);
        __builtin_amdgcn_sched_barrier(0);
        if (i == QPOS + WINL) asm volatile("" ::"v"(q2[0]), "v"(q2[1]));
        {
          const int nx = i + WOWIN;
          if (nx >= VB4 && nx < 22 && i < 15) LDS_(nx);
        }
      }
      const f32x4 o = fold4<true>(a0, a1);
      stsc(rw, od, pk2(o[0], o[1]), pk2(o[2], o[3]), tag);
      __builtin_amdgcn_sched_barrier(0);
    }
    if (lane == 0) FL[8] = 1u;
    if (k == 3) STAMP(14);
    if (k == 0) STAMP(15);
#pragma unroll
    for (int i = (VB4 > WOWIN + 15 ? VB4 : WOWIN + 15); i < 22; ++i) LDS_(i);
    if (isE || isD) {
      if (isE) {
        if (ODLY) __builtin_amdgcn_s_sleep(ODLY);
        v4u ov[1];
        const int po = WS_O + (h + 8 * (8 * k + rs)) * O_PITCH + ei * 128 + c8 * 16;
        ov[0] = ldsc(rw, po, 0);
        __builtin_amdgcn_sched_barrier(0);
        lds_wait<false>(FL + 7, 1u);
        const float wt = asf(L[L_W + 8 * k + rs]);
        asm volatile("" ::"v"(wt));
        unsigned np2 = 1;
        if (!tags_ok<1>(ov, tag)) np2 += poll<1>(rw, po, 0, tag, ov);
        if (k == 0) RAW(30, np2);
        if (k == 0) STAMP(7);
        const float o = red4(wt * blo(ov[0].x), wt * bhi(ov[0].x), wt * blo(ov[0].z), wt * bhi(ov[0].z));
        if (nlo) L[L_OP + k * 32 + (4 * (c8 & 1) + g4) * 4 + (c8 >> 1)] = asu(o);
      }
      CFENCE();
      if (isD) { LDW(0); LDW(1); }
      BARRIER();
      if (k == 0) STAMP(25);
      f32x4 os = __builtin_bit_cast(f32x4, *(const LAS v4u*)(L + L_OP + rs * 4));
      if (isE) {
        os += __builtin_bit_cast(f32x4, *(const LAS v4u*)(L + L_OP + 32 + rs * 4));
        os += __builtin_bit_cast(f32x4, *(const LAS v4u*)(L + L_OP + 64 + rs * 4)) + __builtin_bit_cast(f32x4, *(const LAS v4u*)(L + L_OP + 96 + rs * 4));
      }
      const v4u msk = {(c8 >> 1) == 0 ? hm : 0u, (c8 >> 1) == 1 ? hm : 0u, (c8 >> 1) == 2 ? hm : 0u, (c8 >> 1) == 3 ? hm : 0u};
      f32x4 a0 = {0.f, 0.f, 0.f, 0.f}, a1 = {0.f, 0.f, 0.f, 0.f};
#pragma unroll
      for (int i = 0; i < 4; ++i) { const v4u am = amask(pk2(os[i], os[i]), msk); a0 = mfma32(am, wf[2 * i], a0); a1 = mfma32(am, wf[2 * i + 1], a1); }
      const int dead = (k < 3 || lane < 32) ? 0 : OOB;
      if (isE) {
        const f32x4 s = fold4<false>(a0, a1);
        stsc(rw, WS_S + (h * 16 + ei) * S_PITCH + (cA + (lane >> 5)) * 256 + (2 * c8 + (g4 & 1)) * 16 + (nlo ? dead : OOB), pk2(s[0], s[1]), pk2(s[2], s[3]), tag);
        if (k == 0) STAMP(10);
      } else {
        float v0, v1;
        fold2<false>(a0, a1, nlo, v0, v1);
        stsc(rw, WS_F + u * F_PITCH + (cA + (lane >> 5)) * 512 + gidx * 16 + dead, asu(v0), asu(v1), tag);
        if (k == 0) STAMP(11);
      }
    } else if (fin) {
      v4u sv[4];
      const unsigned np3 = poll<4>(rw, WS_S + (32 * k + rs) * S_PITCH + fc * 128 + c8 * 16, 8 * S_PITCH, tag, sv); if (k == 0) RAW(31, np3); (void)np3;
      const float s = red4((blo(sv[0].x) + blo(sv[1].x)) + (blo(sv[2].x) + blo(sv[3].x)), (bhi(sv[0].x) + bhi(sv[1].x)) + (bhi(sv[2].x) + bhi(sv[3].x)),
                           (blo(sv[0].z) + blo(sv[1].z)) + (blo(sv[2].z) + blo(sv[3].z)), (bhi(sv[0].z) + bhi(sv[1].z)) + (bhi(sv[2].z) + bhi(sv[3].z)));
      if (nlo) L[L_OP + 32 * k + g4 * 8 + c8] = asu(s);
      if (k == 0) STAMP(20); if (k == 1) STAMP(21); if (k == 2) STAMP(22); if (k == 3) STAMP(23);
      BARRIER();
    }
    STAMP_FLUSH();
  }
}

extern "C" void kernel_launch(void* const* d_in, const int* in_sizes, int n_in, void* d_out, int out_size, void* d_ws, size_t ws_size,
                              hipStream_t stream) {
  fused_decode<<<NWG, NTHR, 0, stream>>>((const GAS char*)d_in[0], (const GAS char*)d_in[1], (const GAS char*)d_in[2], (const GAS char*)d_in[3],
                                         (const GAS char*)d_in[4], (const GAS char*)d_in[6], (const GAS char*)d_in[7], (const GAS char*)d_in[8],
                                         (GAS char*)d_out, (GAS char*)d_ws);
}
```

```cpp
#include <hip/hip_runtime.h>
#include <stdint.h>

#define GAS __attribute__((address_space(1)))
#define LAS __attribute__((address_space(3)))
typedef unsigned v4u __attribute__((ext_vector_type(4)));
typedef unsigned v2u __attribute__((ext_vector_type(2)));
typedef float f32x4 __attribute__((ext_vector_type(4)));
typedef __bf16 bf16x8 __attribute__((ext_vector_type(8)));
typedef __bf16 bf16x2 __attribute__((ext_vector_type(2)));
typedef short s16x4 __attribute__((ext_vector_type(4)));
#define RLX_AGENT __ATOMIC_RELAXED, __HIP_MEMORY_SCOPE_AGENT

#define WINL 6
#define VB1 10
#define VB2 12
#define VB3 14
#define VB4 VB3
#define QPOS 0
#define ABL 0
#define QLAUX 16
#define ROT 1
#define OAUX 16
#define WPRE 6
#define ODLY2 0
#define WEARLY 2
#define WOWIN 7
#define QSAUX 16
#define ODLY 12
#define CODE_BYTES 9728

constexpr int DM = 448, SK = 65536, WIN = 4096;
constexpr int NWG = 256, NTHR = 320;
constexpr int WS_Q = 4096;
constexpr int WS_GU = WS_Q + 8 * 8192;
constexpr int WS_ML = WS_GU + 28 * 2048;
constexpr int O_PITCH = 2176;
constexpr int WS_O = WS_ML + 8192;
constexpr int S_PITCH = 1792;
constexpr int WS_S = WS_O + 256 * O_PITCH;
constexpr int F_PITCH = 3584;
constexpr int WS_F = WS_S + 128 * S_PITCH;
constexpr int WS_END = WS_F + 56 * F_PITCH;
constexpr int OOB = 1 << 30;
constexpr int L_R1 = 0;
constexpr int L_Q = 256;
constexpr int L_P = 512;
constexpr int L_W = 800;
constexpr int L_WM = 832;
constexpr int L_WL = 840;
constexpr int L_FLAG = 928;
constexpr int L_Z = 944;
constexpr int L_OP = 960;
constexpr int L_TOTAL = 21504;
constexpr unsigned SPIN_CAP = 1u << 16;

#define RAW(k, v)
#define STAMP(k)
#define STAMP_DECL
#define STAMP_FLUSH()

__device__ __forceinline__ float asf(unsigned u) { return __builtin_bit_cast(float, u); }
__device__ __forceinline__ unsigned asu(float f) { return __builtin_bit_cast(unsigned, f); }
__device__ __forceinline__ float blo(unsigned u) { return asf(u << 16); }
__device__ __forceinline__ float bhi(unsigned u) { return asf(u & 0xffff0000u); }
__device__ __forceinline__ float rbf(float f) { return (float)(__bf16)f; }
__device__ __forceinline__ unsigned pk2(float lo, float hi) { bf16x2 t = {(__bf16)lo, (__bf16)hi}; return __builtin_bit_cast(unsigned, t); }
__device__ __forceinline__ f32x4 mfma32(v4u a, v4u b, f32x4 c) {
  return __builtin_amdgcn_mfma_f32_16x16x32_bf16(__builtin_bit_cast(bf16x8, a), __builtin_bit_cast(bf16x8, b), c, 0, 0, 0);
}
__device__ __forceinline__ f32x4 mfma16(v2u a, v2u b, f32x4 c) {
  return __builtin_amdgcn_mfma_f32_16x16x16bf16_1k(__builtin_bit_cast(s16x4, a), __builtin_bit_cast(s16x4, b), c, 0, 0, 0);
}
template <int CTRL> __device__ __forceinline__ float dpp(float x) {
  return asf((unsigned)__builtin_amdgcn_mov_dpp((int)asu(x), CTRL, 0xf, 0xf, true));
}
constexpr int XOR1 = 0xB1, XOR2 = 0x4E, XOR7 = 0x141, XOR8 = 0x128;
__device__ __forceinline__ float sw16(float a, float b) { auto s = __builtin_amdgcn_permlane16_swap(asu(a), asu(b), false, false); return asf(s[0]) + asf(s[1]); }
__device__ __forceinline__ float sw32(float a, float b) { auto s = __builtin_amdgcn_permlane32_swap(asu(a), asu(b), false, false); return asf(s[0]) + asf(s[1]); }
__device__ __forceinline__ float x16_max(float x) { auto s = __builtin_amdgcn_permlane16_swap(asu(x), asu(x), false, false); return fmaxf(asf(s[0]), asf(s[1])); }
__device__ __forceinline__ float x32_max(float x) { auto s = __builtin_amdgcn_permlane32_swap(asu(x), asu(x), false, false); return fmaxf(asf(s[0]), asf(s[1])); }
__device__ __forceinline__ float row_sum(float x) { x += dpp<XOR1>(x); x += dpp<XOR2>(x); x += dpp<XOR7>(x); x += dpp<XOR8>(x); return x; }
__device__ __forceinline__ float row_max(float x) { x = fmaxf(x, dpp<XOR1>(x)); x = fmaxf(x, dpp<XOR2>(x)); x = fmaxf(x, dpp<XOR7>(x)); return fmaxf(x, dpp<XOR8>(x)); }
__device__ __forceinline__ float wave_sum(float x) { x = row_sum(x); x = sw16(x, x); return sw32(x, x); }
__device__ __forceinline__ float red4(float v0, float v1, float v2, float v3) { const float t = sw16(sw32(v0, v2), sw32(v1, v3)); return t + dpp<XOR8>(t); }
__device__ __forceinline__ __amdgpu_buffer_rsrc_t mkrs(const GAS char* p, int bytes) { return __builtin_amdgcn_make_buffer_rsrc((void*)p, 0, bytes, 0x00020000); }
__device__ __forceinline__ v4u ldnt(__amdgpu_buffer_rsrc_t rs, int voff, int soff) {
  const v4u r = __builtin_amdgcn_raw_buffer_load_b128(rs, voff, soff, 2);
  __builtin_amdgcn_sched_barrier(0);
  return r;
}
__device__ __forceinline__ v4u ldsc(__amdgpu_buffer_rsrc_t rs, int voff, int soff) { return __builtin_amdgcn_raw_buffer_load_b128(rs, voff, soff, 16); }
template <int AUX = 16> __device__ __forceinline__ void stsc(__amdgpu_buffer_rsrc_t rs, int off, unsigned d0, unsigned d1, unsigned tag) {
  v4u g = {d0, tag, d1, tag};
  __builtin_amdgcn_raw_buffer_store_b128(g, rs, off, 0, AUX);
}
#define BARRIER() asm volatile("s_waitcnt lgkmcnt(0)\n\ts_barrier" ::: "memory")
#define CFENCE() asm volatile("" ::: "memory")
#define VMW(n) asm volatile("s_waitcnt vmcnt(%0)" ::"i"(n) : "memory")
__device__ __forceinline__ v4u amask(unsigned x, const v4u& m) { return v4u{x & m.x, x & m.y, x & m.z, x & m.w}; }
template <bool R16> __device__ __forceinline__ f32x4 fold4(const f32x4& A, const f32x4& B) {
  f32x4 s;
#pragma unroll
  for (int r = 0; r < 4; ++r) {
    auto t = R16 ? __builtin_amdgcn_permlane16_swap(asu(A[r]), asu(B[r]), false, false) : __builtin_amdgcn_permlane32_swap(asu(A[r]), asu(B[r]), false, false);
    s[r] = asf(t[0]) + dpp<XOR8>(asf(t[1]));
  }
  return s;
}
template <bool R16> __device__ __forceinline__ void fold2(const f32x4& A, const f32x4& B, bool nlo, float& v0, float& v1) {
  float s[4];
#pragma unroll
  for (int r = 0; r < 4; ++r) {
    auto t = R16 ? __builtin_amdgcn_permlane16_swap(asu(A[r]), asu(B[r]), false, false) : __builtin_amdgcn_permlane32_swap(asu(A[r]), asu(B[r]), false, false);
    const float a = asf(t[0]), b = asf(t[1]);
    s[r] = r < 2 ? a + dpp<XOR8>(b) : dpp<XOR8>(a) + b;
  }
  v0 = nlo ? s[0] : s[2];
  v1 = nlo ? s[1] : s[3];
}
template <int N> __device__ __forceinline__ bool tags_ok(const v4u (&v)[N], unsigned tag) {
  bool ok = true;
#pragma unroll
  for (int k = 0; k < N; ++k) ok &= (v[k].y == tag) & (v[k].w == tag);
  return __builtin_amdgcn_ballot_w64(!ok) == 0ull;
}
template <int N> __device__ __forceinline__ unsigned poll(__amdgpu_buffer_rsrc_t rs, int off, int stride, unsigned tag, v4u (&v)[N]) {
  unsigned spins = 0;
#pragma unroll 1
  for (; spins < SPIN_CAP; ++spins) {
    CFENCE();
#pragma unroll
    for (int k = 0; k < N; ++k) v[k] = ldsc(rs, off, k * stride);
    if (tags_ok<N>(v, tag)) break;
  }
  return spins + 1;
}
template <bool SLEEP> __device__ __forceinline__ void lds_wait(volatile LAS unsigned* w, unsigned want) {
#pragma unroll 1
  for (unsigned spins = 0; spins < (SPIN_CAP << 6); ++spins) {
    if (__builtin_amdgcn_readfirstlane(*w) >= want) break;
    if (SLEEP) __builtin_amdgcn_s_sleep(1);
  }
  CFENCE();
}
__device__ __forceinline__ float silu(float x) { return x * __builtin_amdgcn_rcpf(1.f + __expf(-x)); }

typedef unsigned long long v2q __attribute__((ext_vector_type(2)));
typedef unsigned long long v4q __attribute__((ext_vector_type(4)));
__global__ void __launch_bounds__(NTHR) fused_decode(const void* x_, const void* kc_, const void* vc_, const void* wq_, const void* wo_, const void* wg_,
                                                     const void* wu_, const void* wd_, void* out_, void* ws_) {
  v4q ka, kb; v2q kd;
  asm volatile("s_load_dwordx8 %0, %3, 0x0\n\ts_load_dwordx8 %1, %3, 0x20\n\ts_load_dwordx4 %2, %3, 0x40"
               : "=&s"(ka), "=&s"(kb), "=&s"(kd) : "s"(__builtin_amdgcn_kernarg_segment_ptr()));
  __builtin_amdgcn_sched_barrier(0);
  __shared__ __attribute__((aligned(16))) unsigned lds_[L_TOTAL];
  LAS unsigned* L = (LAS unsigned*)lds_;
  volatile LAS unsigned* FL = (volatile LAS unsigned*)(L + L_FLAG);
  const int b = blockIdx.x;
  const int h = (b >> 3) & 7, j = (b & 7) * 4 + (b >> 6);
  const int tid = threadIdx.x, lane = tid & 63, w = __builtin_amdgcn_readfirstlane(tid >> 6);
  const int u = (b & 63) + 64 * (b >> 7);
  STAMP_DECL;
  if (w == 0) STAMP(5);
  const __amdgpu_buffer_rsrc_t rc = mkrs((const GAS char*)(__builtin_amdgcn_s_getpc() & ~255ull), (w == 4 && u >= 120) ? CODE_BYTES : 0);
  const unsigned cpf = __builtin_amdgcn_raw_buffer_load_b32(rc, lane * 128, 1024, 0), cpf2 = __builtin_amdgcn_raw_buffer_load_b32(rc, lane * 128, 9216, 0);
  const bool isE = (j & 1) == 0;
  const int ei = j >> 1;
  const bool isD = !isE && u < 56;
  const bool fin = !isE && u >= 114;
  const int fc = u - 114;
  const int rs = lane >> 3, c8 = lane & 7, g4 = lane >> 4, n16 = lane & 15;
  const bool nlo = n16 < 8;
  const int gidx = 4 * c8 + 2 * (g4 & 1) + (n16 >> 3);
  const int NI = isE ? 6 : 8;
  int r0 = (isE ? 256 : 0) + 8 * NI * (w & 3);
  int pitch = u < 64 ? 8192 : 3584;
  int sb = r0 * pitch + (u < 64 ? u : u < 92 ? u - 64 : u - 92) * 128;
  int nb1 = u < 120 ? DM * pitch : 0;
  const int sa = 2 * g4 + ((lane >> 2) & 1);
  int fo = rs * NI * pitch + c8 * 16, xo = lane * 16, xso = sa * NI * 2;
  const int wre = 512 * h + 32 * ei, wrd = u < 56 ? 32 * u : 0;
  int wrow = (wrd + ((wre - wrd) & ((j & 1) - 1))) * 896;
  int oo = rs * 896 + c8 * 16;
  asm volatile("" ::"s"(r0), "s"(pitch), "s"(sb), "s"(nb1), "s"(wrow), "v"(fo), "v"(xo), "v"(xso), "v"(oo));
  __builtin_amdgcn_sched_barrier(0);
  asm volatile("s_waitcnt lgkmcnt(0)" : "+s"(ka), "+s"(kb), "+s"(kd));
  const GAS char *x = (const GAS char*)ka.x, *kc = (const GAS char*)ka.y, *vc = (const GAS char*)ka.z, *wq = (const GAS char*)ka.w;
  const GAS char *wo = (const GAS char*)kb.x, *wg = (const GAS char*)kb.y, *wu = (const GAS char*)kb.z, *wd = (const GAS char*)kb.w;
  GAS char *out = (GAS char*)kd.x, *ws = (GAS char*)kd.y;
  const unsigned ep = *(const __attribute__((address_space(4))) unsigned*)(unsigned long long)ws;
  const __amdgpu_buffer_rsrc_t rw = mkrs(ws, WS_END);
  constexpr float C2 = 0.0625f * 1.4426950408889634f;

  if (w == 4) {
    STAMP(0);
    __builtin_amdgcn_s_setprio(3);
    if (lane < 20) FL[lane] = 0u;
    asm volatile("s_waitcnt lgkmcnt(0)\n\ts_barrier" ::"s"(ep) : "memory");
    const unsigned tag = ep + 1u;
    if (u < 120) {
      const v2u r0 = *(const LAS v2u*)(L + L_R1 + 2 * (lane & 31)), r1 = *(const LAS v2u*)(L + L_R1 + 64 + 2 * (lane & 31)),
                r2 = *(const LAS v2u*)(L + L_R1 + 128 + 2 * (lane & 31)), r3 = *(const LAS v2u*)(L + L_R1 + 192 + 2 * (lane & 31));
      const float v0 = (asf(r0.x) + asf(r1.x)) + (asf(r2.x) + asf(r3.x)), v1 = (asf(r0.y) + asf(r1.y)) + (asf(r2.y) + asf(r3.y));
      const int hf = isE ? 1 : 0;
      const int dst = u < 64 ? WS_Q + (u >> 3) * 8192 + hf * 4096 + (u & 7) * 512 : u < 92 ? WS_GU + (u - 64) * 2048 + hf * 512 : WS_GU + (u - 92) * 2048 + 1024 + hf * 512;
      if (lane < 32) stsc<QSAUX>(rw, dst + lane * 16, asu(v0), asu(v1), tag);
    }
    STAMP(2);
    const unsigned xw = __builtin_amdgcn_raw_buffer_load_b16(mkrs(x, 896), (4 * c8 + g4) * 2, fin ? fc * 64 : 0, 0);
    const int go = WS_GU + (u >> 1) * 2048 + g4 * 512 + (u & 1) * 256 + n16 * 16;
    v4u gu[1];
    lds_wait<true>(FL + 5, 1u);
    gu[0] = ldsc(rw, go, isD ? 0 : OOB);
    asm volatile("" ::"v"(cpf), "v"(cpf2));
    BARRIER();
    BARRIER();
    BARRIER();
    if (lane < 2) {
      const float M = fmaxf(fmaxf(asf(L[L_WM + lane]), asf(L[L_WM + 2 + lane])), fmaxf(asf(L[L_WM + 4 + lane]), asf(L[L_WM + 6 + lane])));
      const float Ls = (asf(L[L_WL + lane]) + asf(L[L_WL + 2 + lane])) + (asf(L[L_WL + 4 + lane]) + asf(L[L_WL + 6 + lane]));
      stsc(rw, WS_ML + (h * 2 + lane) * 512 + j * 16, asu(M), asu(Ls), tag);
    }
    STAMP(4);
    if (isE) {
      lds_wait<true>(FL + 8, 1u);
      v4u ml[1];
      const unsigned np1 = poll<1>(rw, WS_ML + (h * 2 + (ei >> 3)) * 512 + (lane & 31) * 16, 0, tag, ml); RAW(29, np1); (void)np1;
      const float m = asf(ml[0].x);
      const float M = x16_max(row_max(m));
      const float e = __builtin_amdgcn_exp2f((m - M) * C2);
      float Ls = row_sum(e * asf(ml[0].z)); Ls = sw16(Ls, Ls);
      if (lane < 32) L[L_W + lane] = asu(e * __builtin_amdgcn_rcpf(Ls));
      if (lane == 0) FL[7] = 1u;
      STAMP(6);
    } else if (isD) {
      if (!tags_ok<1>(gu, tag)) poll<1>(rw, go, 0, tag, gu);
      const float y = rbf(sw16(asf(gu[0].x), asf(gu[0].z)));
      auto t = __builtin_amdgcn_permlane32_swap(asu(y), asu(y), false, false);
      const float hv = rbf(rbf(silu(asf(t[0]))) * asf(t[1]));
      const int r = 2 * n16 + (g4 & 1);
      if (lane < 32) L[L_OP + (r & 7) * 4 + (r >> 3)] = asu(hv);
      STAMP(6);
      BARRIER();
    } else if (fin) {
      lds_wait<true>(FL + 8, 1u);
      v4u fv[14];
      poll<14>(rw, WS_F + g4 * F_PITCH + fc * 256 + n16 * 16, 4 * F_PITCH, tag, fv);
      float f0 = 0.f, f1 = 0.f;
#pragma unroll
      for (int i = 0; i < 14; ++i) { f0 += asf(fv[i].x); f1 += asf(fv[i].z); }
      float f = sw16(f0, f1); f = sw32(f, f);
      const float g = rbf(silu(rbf(f)));
      if (lane < 32) L[L_W + 2 * n16 + (g4 & 1)] = asu(g);
      asm volatile("s_waitcnt lgkmcnt(0)" ::: "memory");
      const float gc = asf(L[L_W + 4 * c8 + g4]);
      int so = L_OP + g4 * 8 + c8, oa = (4 * c8 + g4) * 2 + (nlo ? 0 : OOB);
      float xf = blo(xw);
      asm volatile("" : "+v"(so), "+v"(oa), "+v"(xf));
      STAMP(16);
      BARRIER();
      const float s = rbf((asf(L[so]) + asf(L[so + 32])) + (asf(L[so + 64]) + asf(L[so + 96])));
      const float res = xf + rbf(s * gc);
      __builtin_amdgcn_raw_buffer_store_b16((unsigned short)pk2(res, res), mkrs(out, 896), oa, fc * 64, OAUX);
      if (fc == 0 && lane == 0) __hip_atomic_store((GAS unsigned*)ws, tag, RLX_AGENT);
      STAMP(8);
    }
    STAMP_FLUSH();
  } else {
    const int k = w;
    const unsigned hm = (lane & 1) ? 0xffff0000u : 0x0000ffffu;
    v4u kf[16], vf[16], wf[8];
    const __amdgpu_buffer_rsrc_t rx = mkrs(x, 896);
    const v4u xr = __builtin_amdgcn_raw_buffer_load_b128(rx, xo, 0, 0);
    const v4u xs = __builtin_amdgcn_raw_buffer_load_b128(rx, xso, r0 * 2, 0);
    __builtin_amdgcn_sched_barrier(0);
    v4u fr[8];
    {
      const __amdgpu_buffer_rsrc_t ra = mkrs(u < 64 ? wq : u < 92 ? wg : wu, nb1);
#pragma unroll
      for (int i = 0; i < 8; ++i) fr[i] = ldnt(ra, fo, (i >= 6 && isE) ? OOB : sb + i * pitch);
    }
    const v2u msk2 = {(lane & 2) ? 0u : hm, (lane & 2) ? hm : 0u};
    const int cA = 2 * k, sA = cA * 128, sB = k < 3 ? sA + 128 : OOB;
    const __amdgpu_buffer_rsrc_t ro = mkrs((isE ? wo : wd) + wrow, (isE || isD) ? 32 * 896 : 0);
#define LDW(i) wf[i] = ldnt(ro, oo, ((i) >> 1) * 8 * 896 + (((i) & 1) ? sB : sA))
#pragma unroll
    for (int i = 0; i < 2; ++i) wf[i] = ldnt(ro, oo, isE ? (i >> 1) * 8 * 896 + ((i & 1) ? sB : sA) : OOB);
    const __amdgpu_buffer_rsrc_t rk = mkrs(kc + ((size_t)h * SK + (SK - WIN) + 128 * j) * 512, 128 * 512);
    const __amdgpu_buffer_rsrc_t rv = mkrs(vc + ((size_t)h * SK + (SK - WIN) + 128 * j) * 512, 128 * 512);
    const int ko = k * 2048 + (n16 >> 2) * 512 + g4 * 64 + (lane & 3) * 16;
    const int vo = rs * 512 + c8 * 16 + k * 128;
    const int rot = ROT ? ((b + (b >> 3) + (b >> 6)) & 7) * 8192 : 0;
#define LDK(i) kf[i] = ldnt(rk, ko + ((i) & 1) * 256, (((i) >> 1) * 8192 + rot) & 0xffff)
#define LDV(i) vf[i] = ldnt(rv, vo, ((i) * 4096 + rot) & 0xffff)
#define LDS_(n) do { if ((n) < 16) LDV((n) < 16 ? (n) : 0); else LDW((n) >= 16 ? (n) - 16 + WEARLY : 2); } while (0)
    if (k == 0) STAMP(18);
    {
      float xv[8];
#pragma unroll
      for (int e = 0; e < 4; ++e) { xv[2 * e] = blo(xr[e]); xv[2 * e + 1] = bhi(xr[e]); }
      const float s1 = ((xv[0] + xv[1]) + (xv[2] + xv[3])) + ((xv[4] + xv[5]) + (xv[6] + xv[7]));
      const float mean = wave_sum(s1) * (1.f / DM);
      float s2 = 0.f;
#pragma unroll
      for (int e = 0; e < 8; ++e) { const float d = xv[e] - mean; s2 += d * d; }
      s2 = lane < 56 ? s2 : 0.f;
      const float var = wave_sum(s2) * (1.f / DM);
      const float mb = rbf(mean), rden = __builtin_amdgcn_rcpf(rbf(__builtin_amdgcn_sqrtf(rbf(rbf(var) + 1e-5f))));
      unsigned xc[8];
#pragma unroll
      for (int i = 0; i < 8; ++i) {
        const float xn = rbf(((i & 1) ? bhi(xs[i >> 1]) : blo(xs[i >> 1])) - mb) * rden;
        xc[i] = pk2(xn, xn);
      }
      if (k == 0) STAMP(19);
      __builtin_amdgcn_sched_barrier(0);
      f32x4 a0 = {0.f, 0.f, 0.f, 0.f}, a1 = {0.f, 0.f, 0.f, 0.f};
#pragma unroll
      for (int i = 0; i < 8; ++i) {
        const v2u am = {xc[i] & msk2.x, xc[i] & msk2.y};
        a0 = mfma16(am, v2u{fr[i].x, fr[i].y}, a0);
        a1 = mfma16(am, v2u{fr[i].z, fr[i].w}, a1);
        __builtin_amdgcn_sched_barrier(0);
        if (i >= 8 - WINL) LDK(i - (8 - WINL));
      }
      float v0, v1;
      fold2<true>(a0, a1, nlo, v0, v1);
      if (lane < 32) *(LAS v2u*)(L + L_R1 + 64 * k + 2 * gidx) = v2u{asu(v0), asu(v1)};
      asm volatile("s_waitcnt lgkmcnt(0)\n\ts_barrier" ::"s"(ep), "v"(cpf), "v"(cpf2) : "memory");
      if (k == 0) STAMP(12);
    }
    const unsigned tag = ep + 1u;
#pragma unroll
    for (int i = WINL; i < 16; ++i) {
      VMW(WINL - 1); LDK(i);
      if (i - WINL + 2 < WEARLY) wf[i - WINL + 2] = ldnt(ro, oo, isE ? ((i - WINL + 2) >> 1) * 8 * 896 + (((i - WINL + 2) & 1) ? sB : sA) : OOB);
    }
#pragma unroll
    for (int i = 0; i < QPOS; ++i) { VMW(WINL - 1); LDV(i); }
    v4u q2[2];
    {
      const int qo = WS_Q + h * 8192 + k * 1024 + lane * 16;
      v4u qv[2];
      qv[0] = __builtin_amdgcn_raw_buffer_load_b128(rw, qo, (ABL & 1) ? OOB : 0, QLAUX);
      qv[1] = __builtin_amdgcn_raw_buffer_load_b128(rw, qo, (ABL & 3) ? OOB : 4096, QLAUX);
      __builtin_amdgcn_sched_barrier(0);
      if (lane == 0) FL[5] = 1u;
      if (k == 0) STAMP(17);
#pragma unroll
      for (int i = QPOS; i < QPOS + WINL; ++i) { VMW(WINL + 1); LDV(i); }
      VMW(WINL);
      const bool qok = (ABL & 7) ? (qv[0].y != 12345u || tag != 77u) : tags_ok<2>(qv, tag);
      q2[0] = ldsc(rw, qo, qok ? OOB : 0);
      q2[1] = ldsc(rw, qo, qok ? OOB : 4096);
      __builtin_amdgcn_sched_barrier(0);
#pragma unroll
      for (int i = QPOS + WINL; i < VB1; ++i) LDS_(i);
      if (k == 0) STAMP(13);
      unsigned np = 1;
      if (!qok) {
        qv[0] = q2[0]; qv[1] = q2[1]; ++np;
        if (!tags_ok<2>(qv, tag)) np += poll<2>(rw, qo, 4096, tag, qv);
      }
      if (k == 0) RAW(28, np);
      if (k == 0) STAMP(3);
      if (k == 1) STAMP(26);
      if (k == 2) STAMP(27);
      if (k == 3) STAMP(1);
      L[L_Q + 64 * k + lane] = pk2(asf(qv[0].x) + asf(qv[1].x), asf(qv[0].z) + asf(qv[1].z));
    }
    BARRIER();
#pragma unroll
    for (int i = VB1; i < VB2; ++i) LDS_(i);
    if (k == 0) STAMP(24);
    v4u q[4];
#pragma unroll
    for (int t = 0; t < 4; ++t) {
      const int qa = L_Q + ((lane >> 2) & 1) * 128 + (t & 1) * 64 + g4 * 16 + (lane & 3) * 4;
      q[t] = *(const LAS v4u*)(L + ((n16 >> 3) == (t >> 1) ? qa : L_Z));
    }
    float y[4];
#pragma unroll
    for (int t = 0; t < 4; ++t) {
      f32x4 d = {0.f, 0.f, 0.f, 0.f};
      d = mfma32(q[0], kf[2 * t], d); d = mfma32(q[1], kf[2 * t + 1], d);
      d = mfma32(q[2], kf[2 * t + 8], d); d = mfma32(q[3], kf[2 * t + 9], d);
      const float lo = (lane & 1) ? d[1] : d[0], hi = (lane & 1) ? d[3] : d[2];
      y[t] = (lane & 2) ? hi : lo;
    }
    {
      const float k0 = (lane & 2) ? y[2] : y[0], s0 = (lane & 2) ? y[0] : y[2], k1 = (lane & 2) ? y[3] : y[1], s1 = (lane & 2) ? y[1] : y[3];
      y[0] = k0 + dpp<XOR2>(s0); y[1] = k1 + dpp<XOR2>(s1);
    }
    const float sc = rbf(((lane & 1) ? y[1] : y[0]) + dpp<XOR1>((lane & 1) ? y[0] : y[1]));
    const float m = x32_max(row_max(sc));
    const int a = g4 & 1;
    if ((lane & 0x2f) == 0) L[L_WM + 2 * k + a] = asu(m);
    BARRIER();
#pragma unroll
    for (int i = VB2; i < VB3; ++i) LDS_(i);
    const float M = fmaxf(fmaxf(asf(L[L_WM + a]), asf(L[L_WM + 2 + a])), fmaxf(asf(L[L_WM + 4 + a]), asf(L[L_WM + 6 + a])));
    const float p = __builtin_amdgcn_exp2f((sc - M) * C2);
    L[L_P + a * 128 + (4 * (k & 1) + (n16 >> 2)) * 16 + 2 * ((lane & 3) + 4 * (g4 >> 1)) + (k >> 1)] = pk2(p, p);
    float l = row_sum(p); l = sw32(l, l);
    if ((lane & 0x2f) == 0) L[L_WL + 2 * k + a] = asu(l);
    BARRIER();
#pragma unroll
    for (int i = VB3; i < VB4; ++i) LDS_(i);
    if (k == 0) STAMP(9);
    {
      v4u pa[4];
#pragma unroll
      for (int t = 0; t < 4; ++t) pa[t] = *(const LAS v4u*)(L + L_P + (n16 >> 3) * 128 + sa * 16 + 4 * t);
      int od = WS_O + (h + 8 * j) * O_PITCH + (lane >> 5) * 1024 + k * 256 + (2 * c8 + (g4 & 1)) * 16 + (nlo ? 0 : OOB);
      asm volatile("" : "+v"(od));
      f32x4 a0 = {0.f, 0.f, 0.f, 0.f}, a1 = {0.f, 0.f, 0.f, 0.f};
#pragma unroll
      for (int i = 0; i < 16; ++i) {
        const unsigned c = pa[i >> 2][i & 3];
        const v2u am = {c & msk2.x, c & msk2.y};
        a0 = mfma16(am, v2u{vf[i].x, vf[i].y}, a0);
        a1 = mfma16(am, v2u{vf[i].z, vf[i].w}, a1);
        __builtin_amdgcn_sched_barrier(0);
        if (i == QPOS + WINL) asm volatile("" ::"v"(q2[0]), "v"(q2[1]));
        {
          const int nx = i + WOWIN;
          if (nx >= VB4 && nx < 16 + WPRE && nx < 24 - WEARLY && i < 15) LDS_(nx);
        }
      }
      const f32x4 o = fold4<true>(a0, a1);
      stsc(rw, od, pk2(o[0], o[1]), pk2(o[2], o[3]), tag);
      __builtin_amdgcn_sched_barrier(0);
    }
    if (lane == 0) FL[8] = 1u;
    if (k == 3) STAMP(14);
    if (k == 0) STAMP(15);
#pragma unroll
    for (int i = 16 + (WPRE < WOWIN - 1 ? WPRE : WOWIN - 1); i < 24 - WEARLY; ++i) LDS_(i);
    if (isE || isD) {
      v4u msk = {(c8 >> 1) == 0 ? hm : 0u, (c8 >> 1) == 1 ? hm : 0u, (c8 >> 1) == 2 ? hm : 0u, (c8 >> 1) == 3 ? hm : 0u};
      const int dead = (k < 3 || lane < 32) ? 0 : OOB;
      int sd = isE ? WS_S + (h * 16 + ei) * S_PITCH + (cA + (lane >> 5)) * 256 + (2 * c8 + (g4 & 1)) * 16 + (nlo ? dead : OOB)
                   : WS_F + u * F_PITCH + (cA + (lane >> 5)) * 512 + gidx * 16 + dead;
      int lw = L_OP + k * 32 + (4 * (c8 & 1) + g4) * 4 + (c8 >> 1) + (nlo ? 0 : 256), lr = L_OP + rs * 4;
      unsigned tv = tag;
      asm volatile("" : "+v"(msk), "+v"(sd), "+v"(lw), "+v"(lr), "+v"(tv));
      if (isE) {
        if (ODLY) __builtin_amdgcn_s_sleep(ODLY);
        v4u ov[1];
        const int po = WS_O + (h + 8 * (8 * k + rs)) * O_PITCH + ei * 128 + c8 * 16;
        ov[0] = ldsc(rw, po, 0);
        __builtin_amdgcn_sched_barrier(0);
        v4u ov2[1];
        if (ODLY2) { __builtin_amdgcn_s_sleep(ODLY2); CFENCE(); ov2[0] = ldsc(rw, po, 0); __builtin_amdgcn_sched_barrier(0); }
        lds_wait<false>(FL + 7, 1u);
        const float wt = asf(L[L_W + 8 * k + rs]);
        asm volatile("" ::"v"(wt));
        unsigned np2 = 1;
        if (!tags_ok<1>(ov, tag)) {
          if (ODLY2) ov[0] = ov2[0];
          if (!ODLY2 || !tags_ok<1>(ov, tag)) np2 += poll<1>(rw, po, 0, tag, ov);
        }
        if (k == 0) RAW(30, np2);
        if (k == 0) STAMP(7);
        L[lw] = asu(red4(wt * blo(ov[0].x), wt * bhi(ov[0].x), wt * blo(ov[0].z), wt * bhi(ov[0].z)));
        BARRIER();
        if (k == 0) STAMP(25);
        const f32x4 os = (__builtin_bit_cast(f32x4, *(const LAS v4u*)(L + lr)) + __builtin_bit_cast(f32x4, *(const LAS v4u*)(L + lr + 32))) +
                         (__builtin_bit_cast(f32x4, *(const LAS v4u*)(L + lr + 64)) + __builtin_bit_cast(f32x4, *(const LAS v4u*)(L + lr + 96)));
        f32x4 a0 = {0.f, 0.f, 0.f, 0.f}, a1 = {0.f, 0.f, 0.f, 0.f};
#pragma unroll
        for (int i = 0; i < 4; ++i) { const v4u am = amask(pk2(os[i], os[i]), msk); a0 = mfma32(am, wf[2 * i], a0); a1 = mfma32(am, wf[2 * i + 1], a1); }
        const f32x4 s = fold4<false>(a0, a1);
        stsc(rw, sd, pk2(s[0], s[1]), pk2(s[2], s[3]), tv);
        if (ODLY2) asm volatile("" ::"v"(ov2[0]));
        if (k == 0) STAMP(10);
      }
      CFENCE();
      if (isD) {
#pragma unroll
        for (int i = 0; i < WEARLY; ++i) LDW(i);
        BARRIER();
        const f32x4 os = __builtin_bit_cast(f32x4, *(const LAS v4u*)(L + lr));
        f32x4 a0 = {0.f, 0.f, 0.f, 0.f}, a1 = {0.f, 0.f, 0.f, 0.f};
#pragma unroll
        for (int i = 3; i >= 0; --i) { const v4u am = amask(pk2(os[i], os[i]), msk); a0 = mfma32(am, wf[2 * i], a0); a1 = mfma32(am, wf[2 * i + 1], a1); }
        float v0, v1;
        fold2<false>(a0, a1, nlo, v0, v1);
        stsc(rw, sd, asu(v0), asu(v1), tv);
        if (k == 0) STAMP(11);
      }
    } else if (fin) {
      int lw = L_OP + 32 * k + g4 * 8 + c8 + (nlo ? 0 : 256);
      asm volatile("" : "+v"(lw));
      v4u sv[4];
      const unsigned np3 = poll<4>(rw, WS_S + (32 * k + rs) * S_PITCH + fc * 128 + c8 * 16, 8 * S_PITCH, tag, sv); if (k == 0) RAW(31, np3); (void)np3;
      const float s = red4((blo(sv[0].x) + blo(sv[1].x)) + (blo(sv[2].x) + blo(sv[3].x)), (bhi(sv[0].x) + bhi(sv[1].x)) + (bhi(sv[2].x) + bhi(sv[3].x)),
                           (blo(sv[0].z) + blo(sv[1].z)) + (blo(sv[2].z) + blo(sv[3].z)), (bhi(sv[0].z) + bhi(sv[1].z)) + (bhi(sv[2].z) + bhi(sv[3].z)));
      L[lw] = asu(s);
      if (k == 0) STAMP(20); if (k == 1) STAMP(21); if (k == 2) STAMP(22); if (k == 3) STAMP(23);
      BARRIER();
    }
    STAMP_FLUSH();
  }
}

extern "C" void kernel_launch(void* const* d_in, const int* in_sizes, int n_in, void* d_out, int out_size, void* d_ws, size_t ws_size,
                              hipStream_t stream) {
  fused_decode<<<NWG, NTHR, 0, stream>>>(d_in[0], d_in[1], d_in[2], d_in[3], d_in[4], d_in[6], d_in[7], d_in[8], d_out, d_ws);
}
```

```cpp
#define PROBE 5
#include <hip/hip_runtime.h>
#include <stdint.h>

#define GAS __attribute__((address_space(1)))
#define LAS __attribute__((address_space(3)))
typedef unsigned v4u __attribute__((ext_vector_type(4)));
typedef unsigned v2u __attribute__((ext_vector_type(2)));
typedef float f32x4 __attribute__((ext_vector_type(4)));
typedef __bf16 bf16x8 __attribute__((ext_vector_type(8)));
typedef __bf16 bf16x2 __attribute__((ext_vector_type(2)));
typedef short s16x4 __attribute__((ext_vector_type(4)));
#define RLX_AGENT __ATOMIC_RELAXED, __HIP_MEMORY_SCOPE_AGENT

#define WINL 6
#define VB1 10
#define VB2 12
#define VB3 14
#define VB4 VB3
#define QPOS 0
#define ABL 0
#define QLAUX 16
#define ROT 1
#define OAUX 16
#define WPRE 6
#define ODLY2 0
#define WEARLY 2
#define WOWIN 7
#define QSAUX 16
#define ODLY 12
#define CODE_BYTES 9728

constexpr int DM = 448, SK = 65536, WIN = 4096;
constexpr int NWG = 256, NTHR = 320;
constexpr int WS_Q = 4096;
constexpr int WS_GU = WS_Q + 8 * 8192;
constexpr int WS_ML = WS_GU + 28 * 2048;
constexpr int O_PITCH = 2176;
constexpr int WS_O = WS_ML + 8192;
constexpr int S_PITCH = 1792;
constexpr int WS_S = WS_O + 256 * O_PITCH;
constexpr int F_PITCH = 3584;
constexpr int WS_F = WS_S + 128 * S_PITCH;
constexpr int WS_END = WS_F + 56 * F_PITCH;
constexpr int OOB = 1 << 30;
constexpr int L_R1 = 0;
constexpr int L_Q = 256;
constexpr int L_P = 512;
constexpr int L_W = 800;
constexpr int L_WM = 832;
constexpr int L_WL = 840;
constexpr int L_FLAG = 928;
constexpr int L_Z = 944;
constexpr int L_OP = 960;
constexpr int L_TOTAL = 21504;
constexpr unsigned SPIN_CAP = 1u << 16;

#define RAW(k, v)
#define STAMP(k) do { if ((k) == PROBE) stp = __builtin_amdgcn_s_memrealtime(); } while (0)
#define STAMP_DECL const unsigned long long st0 = __builtin_amdgcn_s_memrealtime(); unsigned long long stp = 0
#define STAMP_FLUSH() do { if (stp) { const unsigned long long tend = st0 + 3000 + 8 * (stp - st0); while (__builtin_amdgcn_s_memrealtime() < tend) __builtin_amdgcn_s_sleep(2); } } while (0)

__device__ __forceinline__ float asf(unsigned u) { return __builtin_bit_cast(float, u); }
__device__ __forceinline__ unsigned asu(float f) { return __builtin_bit_cast(unsigned, f); }
__device__ __forceinline__ float blo(unsigned u) { return asf(u << 16); }
__device__ __forceinline__ float bhi(unsigned u) { return asf(u & 0xffff0000u); }
__device__ __forceinline__ float rbf(float f) { return (float)(__bf16)f; }
__device__ __forceinline__ unsigned pk2(float lo, float hi) { bf16x2 t = {(__bf16)lo, (__bf16)hi}; return __builtin_bit_cast(unsigned, t); }
__device__ __forceinline__ f32x4 mfma32(v4u a, v4u b, f32x4 c) {
  return __builtin_amdgcn_mfma_f32_16x16x32_bf16(__builtin_bit_cast(bf16x8, a), __builtin_bit_cast(bf16x8, b), c, 0, 0, 0);
}
__device__ __forceinline__ f32x4 mfma16(v2u a, v2u b, f32x4 c) {
  return __builtin_amdgcn_mfma_f32_16x16x16bf16_1k(__builtin_bit_cast(s16x4, a), __builtin_bit_cast(s16x4, b), c, 0, 0, 0);
}
template <int CTRL> __device__ __forceinline__ float dpp(float x) {
  return asf((unsigned)__builtin_amdgcn_mov_dpp((int)asu(x), CTRL, 0xf, 0xf, true));
}
constexpr int XOR1 = 0xB1, XOR2 = 0x4E, XOR7 = 0x141, XOR8 = 0x128;
__device__ __forceinline__ float sw16(float a, float b) { auto s = __builtin_amdgcn_permlane16_swap(asu(a), asu(b), false, false); return asf(s[0]) + asf(s[1]); }
__device__ __forceinline__ float sw32(float a, float b) { auto s = __builtin_amdgcn_permlane32_swap(asu(a), asu(b), false, false); return asf(s[0]) + asf(s[1]); }
__device__ __forceinline__ float x16_max(float x) { auto s = __builtin_amdgcn_permlane16_swap(asu(x), asu(x), false, false); return fmaxf(asf(s[0]), asf(s[1])); }
__device__ __forceinline__ float x32_max(float x) { auto s = __builtin_amdgcn_permlane32_swap(asu(x), asu(x), false, false); return fmaxf(asf(s[0]), asf(s[1])); }
__device__ __forceinline__ float row_sum(float x) { x += dpp<XOR1>(x); x += dpp<XOR2>(x); x += dpp<XOR7>(x); x += dpp<XOR8>(x); return x; }
__device__ __forceinline__ float row_max(float x) { x = fmaxf(x, dpp<XOR1>(x)); x = fmaxf(x, dpp<XOR2>(x)); x = fmaxf(x, dpp<XOR7>(x)); return fmaxf(x, dpp<XOR8>(x)); }
__device__ __forceinline__ float wave_sum(float x) { x = row_sum(x); x = sw16(x, x); return sw32(x, x); }
__device__ __forceinline__ float red4(float v0, float v1, float v2, float v3) { const float t = sw16(sw32(v0, v2), sw32(v1, v3)); return t + dpp<XOR8>(t); }
__device__ __forceinline__ __amdgpu_buffer_rsrc_t mkrs(const GAS char* p, int bytes) { return __builtin_amdgcn_make_buffer_rsrc((void*)p, 0, bytes, 0x00020000); }
__device__ __forceinline__ v4u ldnt(__amdgpu_buffer_rsrc_t rs, int voff, int soff) {
  const v4u r = __builtin_amdgcn_raw_buffer_load_b128(rs, voff, soff, 2);
  __builtin_amdgcn_sched_barrier(0);
  return r;
}
__device__ __forceinline__ v4u ldsc(__amdgpu_buffer_rsrc_t rs, int voff, int soff) { return __builtin_amdgcn_raw_buffer_load_b128(rs, voff, soff, 16); }
template <int AUX = 16> __device__ __forceinline__ void stsc(__amdgpu_buffer_rsrc_t rs, int off, unsigned d0, unsigned d1, unsigned tag) {
  v4u g = {d0, tag, d1, tag};
  __builtin_amdgcn_raw_buffer_store_b128(g, rs, off, 0, AUX);
}
#define BARRIER() asm volatile("s_waitcnt lgkmcnt(0)\n\ts_barrier" ::: "memory")
#define CFENCE() asm volatile("" ::: "memory")
#define VMW(n) asm volatile("s_waitcnt vmcnt(%0)" ::"i"(n) : "memory")
__device__ __forceinline__ v4u amask(unsigned x, const v4u& m) { return v4u{x & m.x, x & m.y, x & m.z, x & m.w}; }
template <bool R16> __device__ __forceinline__ f32x4 fold4(const f32x4& A, const f32x4& B) {
  f32x4 s;
#pragma unroll
  for (int r = 0; r < 4; ++r) {
    auto t = R16 ? __builtin_amdgcn_permlane16_swap(asu(A[r]), asu(B[r]), false, false) : __builtin_amdgcn_permlane32_swap(asu(A[r]), asu(B[r]), false, false);
    s[r] = asf(t[0]) + dpp<XOR8>(asf(t[1]));
  }
  return s;
}
template <bool R16> __device__ __forceinline__ void fold2(const f32x4& A, const f32x4& B, bool nlo, float& v0, float& v1) {
  float s[4];
#pragma unroll
  for (int r = 0; r < 4; ++r) {
    auto t = R16 ? __builtin_amdgcn_permlane16_swap(asu(A[r]), asu(B[r]), false, false) : __builtin_amdgcn_permlane32_swap(asu(A[r]), asu(B[r]), false, false);
    const float a = asf(t[0]), b = asf(t[1]);
    s[r] = r < 2 ? a + dpp<XOR8>(b) : dpp<XOR8>(a) + b;
  }
  v0 = nlo ? s[0] : s[2];
  v1 = nlo ? s[1] : s[3];
}
template <int N> __device__ __forceinline__ bool tags_ok(const v4u (&v)[N], unsigned tag) {
  bool ok = true;
#pragma unroll
  for (int k = 0; k < N; ++k) ok &= (v[k].y == tag) & (v[k].w == tag);
  return __builtin_amdgcn_ballot_w64(!ok) == 0ull;
}
template <int N> __device__ __forceinline__ unsigned poll(__amdgpu_buffer_rsrc_t rs, int off, int stride, unsigned tag, v4u (&v)[N]) {
  unsigned spins = 0;
#pragma unroll 1
  for (; spins < SPIN_CAP; ++spins) {
    CFENCE();
#pragma unroll
    for (int k = 0; k < N; ++k) v[k] = ldsc(rs, off, k * stride);
    if (tags_ok<N>(v, tag)) break;
  }
  return spins + 1;
}
template <bool SLEEP> __device__ __forceinline__ void lds_wait(volatile LAS unsigned* w, unsigned want) {
#pragma unroll 1
  for (unsigned spins = 0; spins < (SPIN_CAP << 6); ++spins) {
    if (__builtin_amdgcn_readfirstlane(*w) >= want) break;
    if (SLEEP) __builtin_amdgcn_s_sleep(1);
  }
  CFENCE();
}
__device__ __forceinline__ float silu(float x) { return x * __builtin_amdgcn_rcpf(1.f + __expf(-x)); }

typedef unsigned long long v2q __attribute__((ext_vector_type(2)));
typedef unsigned long long v4q __attribute__((ext_vector_type(4)));
__global__ void __launch_bounds__(NTHR) fused_decode(const void* x_, const void* kc_, const void* vc_, const void* wq_, const void* wo_, const void* wg_,
                                                     const void* wu_, const void* wd_, void* out_, void* ws_) {
  v4q ka, kb; v2q kd;
  asm volatile("s_load_dwordx8 %0, %3, 0x0\n\ts_load_dwordx8 %1, %3, 0x20\n\ts_load_dwordx4 %2, %3, 0x40"
               : "=&s"(ka), "=&s"(kb), "=&s"(kd) : "s"(__builtin_amdgcn_kernarg_segment_ptr()));
  __builtin_amdgcn_sched_barrier(0);
  __shared__ __attribute__((aligned(16))) unsigned lds_[L_TOTAL];
  LAS unsigned* L = (LAS unsigned*)lds_;
  volatile LAS unsigned* FL = (volatile LAS unsigned*)(L + L_FLAG);
  const int b = blockIdx.x;
  const int h = (b >> 3) & 7, j = (b & 7) * 4 + (b >> 6);
  const int tid = threadIdx.x, lane = tid & 63, w = __builtin_amdgcn_readfirstlane(tid >> 6);
  const int u = (b & 63) + 64 * (b >> 7);
  STAMP_DECL;
  if (w == 0) STAMP(5);
  const __amdgpu_buffer_rsrc_t rc = mkrs((const GAS char*)(__builtin_amdgcn_s_getpc() & ~255ull), (w == 4 && u >= 120) ? CODE_BYTES : 0);
  const unsigned cpf = __builtin_amdgcn_raw_buffer_load_b32(rc, lane * 128, 1024, 0), cpf2 = __builtin_amdgcn_raw_buffer_load_b32(rc, lane * 128, 9216, 0);
  const bool isE = (j & 1) == 0;
  const int ei = j >> 1;
  const bool isD = !isE && u < 56;
  const bool fin = !isE && u >= 114;
  const int fc = u - 114;
  const int rs = lane >> 3, c8 = lane & 7, g4 = lane >> 4, n16 = lane & 15;
  const bool nlo = n16 < 8;
  const int gidx = 4 * c8 + 2 * (g4 & 1) + (n16 >> 3);
  const int NI = isE ? 6 : 8;
  int r0 = (isE ? 256 : 0) + 8 * NI * (w & 3);
  int pitch = u < 64 ? 8192 : 3584;
  int sb = r0 * pitch + (u < 64 ? u : u < 92 ? u - 64 : u - 92) * 128;
  int nb1 = u < 120 ? DM * pitch : 0;
  const int sa = 2 * g4 + ((lane >> 2) & 1);
  int fo = rs * NI * pitch + c8 * 16, xo = lane * 16, xso = sa * NI * 2;
  const int wre = 512 * h + 32 * ei, wrd = u < 56 ? 32 * u : 0;
  int wrow = (wrd + ((wre - wrd) & ((j & 1) - 1))) * 896;
  int oo = rs * 896 + c8 * 16;
  asm volatile("" ::"s"(r0), "s"(pitch), "s"(sb), "s"(nb1), "s"(wrow), "v"(fo), "v"(xo), "v"(xso), "v"(oo));
  __builtin_amdgcn_sched_barrier(0);
  asm volatile("s_waitcnt lgkmcnt(0)" : "+s"(ka), "+s"(kb), "+s"(kd));
  const GAS char *x = (const GAS char*)ka.x, *kc = (const GAS char*)ka.y, *vc = (const GAS char*)ka.z, *wq = (const GAS char*)ka.w;
  const GAS char *wo = (const GAS char*)kb.x, *wg = (const GAS char*)kb.y, *wu = (const GAS char*)kb.z, *wd = (const GAS char*)kb.w;
  GAS char *out = (GAS char*)kd.x, *ws = (GAS char*)kd.y;
  const unsigned ep = *(const __attribute__((address_space(4))) unsigned*)(unsigned long long)ws;
  const __amdgpu_buffer_rsrc_t rw = mkrs(ws, WS_END);
  constexpr float C2 = 0.0625f * 1.4426950408889634f;

  if (w == 4) {
    STAMP(0);
    __builtin_amdgcn_s_setprio(3);
    if (lane < 20) FL[lane] = 0u;
    asm volatile("s_waitcnt lgkmcnt(0)\n\ts_barrier" ::"s"(ep) : "memory");
    const unsigned tag = ep + 1u;
    if (u < 120) {
      const v2u r0 = *(const LAS v2u*)(L + L_R1 + 2 * (lane & 31)), r1 = *(const LAS v2u*)(L + L_R1 + 64 + 2 * (lane & 31)),
                r2 = *(const LAS v2u*)(L + L_R1 + 128 + 2 * (lane & 31)), r3 = *(const LAS v2u*)(L + L_R1 + 192 + 2 * (lane & 31));
      const float v0 = (asf(r0.x) + asf(r1.x)) + (asf(r2.x) + asf(r3.x)), v1 = (asf(r0.y) + asf(r1.y)) + (asf(r2.y) + asf(r3.y));
      const int hf = isE ? 1 : 0;
      const int dst = u < 64 ? WS_Q + (u >> 3) * 8192 + hf * 4096 + (u & 7) * 512 : u < 92 ? WS_GU + (u - 64) * 2048 + hf * 512 : WS_GU + (u - 92) * 2048 + 1024 + hf * 512;
      if (lane < 32) stsc<QSAUX>(rw, dst + lane * 16, asu(v0), asu(v1), tag);
    }
    STAMP(2);
    const unsigned xw = __builtin_amdgcn_raw_buffer_load_b16(mkrs(x, 896), (4 * c8 + g4) * 2, fin ? fc * 64 : 0, 0);
    const int go = WS_GU + (u >> 1) * 2048 + g4 * 512 + (u & 1) * 256 + n16 * 16;
    v4u gu[1];
    lds_wait<true>(FL + 5, 1u);
    gu[0] = ldsc(rw, go, isD ? 0 : OOB);
    asm volatile("" ::"v"(cpf), "v"(cpf2));
    BARRIER();
    BARRIER();
    BARRIER();
    if (lane < 2) {
      const float M = fmaxf(fmaxf(asf(L[L_WM + lane]), asf(L[L_WM + 2 + lane])), fmaxf(asf(L[L_WM + 4 + lane]), asf(L[L_WM + 6 + lane])));
      const float Ls = (asf(L[L_WL + lane]) + asf(L[L_WL + 2 + lane])) + (asf(L[L_WL + 4 + lane]) + asf(L[L_WL + 6 + lane]));
      stsc(rw, WS_ML + (h * 2 + lane) * 512 + j * 16, asu(M), asu(Ls), tag);
    }
    STAMP(4);
    if (isE) {
      lds_wait<true>(FL + 8, 1u);
      v4u ml[1];
      const unsigned np1 = poll<1>(rw, WS_ML + (h * 2 + (ei >> 3)) * 512 + (lane & 31) * 16, 0, tag, ml); RAW(29, np1); (void)np1;
      const float m = asf(ml[0].x);
      const float M = x16_max(row_max(m));
      const float e = __builtin_amdgcn_exp2f((m - M) * C2);
      float Ls = row_sum(e * asf(ml[0].z)); Ls = sw16(Ls, Ls);
      if (lane < 32) L[L_W + lane] = asu(e * __builtin_amdgcn_rcpf(Ls));
      if (lane == 0) FL[7] = 1u;
      STAMP(6);
    } else if (isD) {
      if (!tags_ok<1>(gu, tag)) poll<1>(rw, go, 0, tag, gu);
      const float y = rbf(sw16(asf(gu[0].x), asf(gu[0].z)));
      auto t = __builtin_amdgcn_permlane32_swap(asu(y), asu(y), false, false);
      const float hv = rbf(rbf(silu(asf(t[0]))) * asf(t[1]));
      const int r = 2 * n16 + (g4 & 1);
      if (lane < 32) L[L_OP + (r & 7) * 4 + (r >> 3)] = asu(hv);
      STAMP(6);
      BARRIER();
    } else if (fin) {
      lds_wait<true>(FL + 8, 1u);
      v4u fv[14];
      poll<14>(rw, WS_F + g4 * F_PITCH + fc * 256 + n16 * 16, 4 * F_PITCH, tag, fv);
      float f0 = 0.f, f1 = 0.f;
#pragma unroll
      for (int i = 0; i < 14; ++i) { f0 += asf(fv[i].x); f1 += asf(fv[i].z); }
      float f = sw16(f0, f1); f = sw32(f, f);
      const float g = rbf(silu(rbf(f)));
      if (lane < 32) L[L_W + 2 * n16 + (g4 & 1)] = asu(g);
      asm volatile("s_waitcnt lgkmcnt(0)" ::: "memory");
      const float gc = asf(L[L_W + 4 * c8 + g4]);
      int so = L_OP + g4 * 8 + c8, oa = (4 * c8 + g4) * 2 + (nlo ? 0 : OOB);
      float xf = blo(xw);
      asm volatile("" : "+v"(so), "+v"(oa), "+v"(xf));
      STAMP(16);
      BARRIER();
      const float s = rbf((asf(L[so]) + asf(L[so + 32])) + (asf(L[so + 64]) + asf(L[so + 96])));
      const float res = xf + rbf(s * gc);
      __builtin_amdgcn_raw_buffer_store_b16((unsigned short)pk2(res, res), mkrs(out, 896), oa, fc * 64, OAUX);
      if (fc == 0 && lane == 0) __hip_atomic_store((GAS unsigned*)ws, tag, RLX_AGENT);
      STAMP(8);
    }
    STAMP_FLUSH();
  } else {
    const int k = w;
    const unsigned hm = (lane & 1) ? 0xffff0000u : 0x0000ffffu;
    v4u kf[16], vf[16], wf[8];
    const __amdgpu_buffer_rsrc_t rx = mkrs(x, 896);
    const v4u xr = __builtin_amdgcn_raw_buffer_load_b128(rx, xo, 0, 0);
    const v4u xs = __builtin_amdgcn_raw_buffer_load_b128(rx, xso, r0 * 2, 0);
    __builtin_amdgcn_sched_barrier(0);
    v4u fr[8];
    {
      const __amdgpu_buffer_rsrc_t ra = mkrs(u < 64 ? wq : u < 92 ? wg : wu, nb1);
#pragma unroll
      for (int i = 0; i < 8; ++i) fr[i] = ldnt(ra, fo, (i >= 6 && isE) ? OOB : sb + i * pitch);
    }
    const v2u msk2 = {(lane & 2) ? 0u : hm, (lane & 2) ? hm : 0u};
    const int cA = 2 * k, sA = cA * 128, sB = k < 3 ? sA + 128 : OOB;
    const __amdgpu_buffer_rsrc_t ro = mkrs((isE ? wo : wd) + wrow, (isE || isD) ? 32 * 896 : 0);
#define LDW(i) wf[i] = ldnt(ro, oo, ((i) >> 1) * 8 * 896 + (((i) & 1) ? sB : sA))
#pragma unroll
    for (int i = 0; i < 2; ++i) wf[i] = ldnt(ro, oo, isE ? (i >> 1) * 8 * 896 + ((i & 1) ? sB : sA) : OOB);
    const __amdgpu_buffer_rsrc_t rk = mkrs(kc + ((size_t)h * SK + (SK - WIN) + 128 * j) * 512, 128 * 512);
    const __amdgpu_buffer_rsrc_t rv = mkrs(vc + ((size_t)h * SK + (SK - WIN) + 128 * j) * 512, 128 * 512);
    const int ko = k * 2048 + (n16 >> 2) * 512 + g4 * 64 + (lane & 3) * 16;
    const int vo = rs * 512 + c8 * 16 + k * 128;
    const int rot = ROT ? ((b + (b >> 3) + (b >> 6)) & 7) * 8192 : 0;
#define LDK(i) kf[i] = ldnt(rk, ko + ((i) & 1) * 256, (((i) >> 1) * 8192 + rot) & 0xffff)
#define LDV(i) vf[i] = ldnt(rv, vo, ((i) * 4096 + rot) & 0xffff)
#define LDS_(n) do { if ((n) < 16) LDV((n) < 16 ? (n) : 0); else LDW((n) >= 16 ? (n) - 16 + WEARLY : 2); } while (0)
    if (k == 0) STAMP(18);
    {
      float xv[8];
#pragma unroll
      for (int e = 0; e < 4; ++e) { xv[2 * e] = blo(xr[e]); xv[2 * e + 1] = bhi(xr[e]); }
      const float s1 = ((xv[0] + xv[1]) + (xv[2] + xv[3])) + ((xv[4] + xv[5]) + (xv[6] + xv[7]));
      const float mean = wave_sum(s1) * (1.f / DM);
      float s2 = 0.f;
#pragma unroll
      for (int e = 0; e < 8; ++e) { const float d = xv[e] - mean; s2 += d * d; }
      s2 = lane < 56 ? s2 : 0.f;
      const float var = wave_sum(s2) * (1.f / DM);
      const float mb = rbf(mean), rden = __builtin_amdgcn_rcpf(rbf(__builtin_amdgcn_sqrtf(rbf(rbf(var) + 1e-5f))));
      unsigned xc[8];
#pragma unroll
      for (int i = 0; i < 8; ++i) {
        const float xn = rbf(((i & 1) ? bhi(xs[i >> 1]) : blo(xs[i >> 1])) - mb) * rden;
        xc[i] = pk2(xn, xn);
      }
      if (k == 0) STAMP(19);
      __builtin_amdgcn_sched_barrier(0);
      f32x4 a0 = {0.f, 0.f, 0.f, 0.f}, a1 = {0.f, 0.f, 0.f, 0.f};
#pragma unroll
      for (int i = 0; i < 8; ++i) {
        const v2u am = {xc[i] & msk2.x, xc[i] & msk2.y};
        a0 = mfma16(am, v2u{fr[i].x, fr[i].y}, a0);
        a1 = mfma16(am, v2u{fr[i].z, fr[i].w}, a1);
        __builtin_amdgcn_sched_barrier(0);
        if (i >= 8 - WINL) LDK(i - (8 - WINL));
      }
      float v0, v1;
      fold2<true>(a0, a1, nlo, v0, v1);
      if (lane < 32) *(LAS v2u*)(L + L_R1 + 64 * k + 2 * gidx) = v2u{asu(v0), asu(v1)};
      asm volatile("s_waitcnt lgkmcnt(0)\n\ts_barrier" ::"s"(ep), "v"(cpf), "v"(cpf2) : "memory");
      if (k == 0) STAMP(12);
    }
    const unsigned tag = ep + 1u;
#pragma unroll
    for (int i = WINL; i < 16; ++i) {
      VMW(WINL - 1); LDK(i);
      if (i - WINL + 2 < WEARLY) wf[i - WINL + 2] = ldnt(ro, oo, isE ? ((i - WINL + 2) >> 1) * 8 * 896 + (((i - WINL + 2) & 1) ? sB : sA) : OOB);
    }
#pragma unroll
    for (int i = 0; i < QPOS; ++i) { VMW(WINL - 1); LDV(i); }
    v4u q2[2];
    {
      const int qo = WS_Q + h * 8192 + k * 1024 + lane * 16;
      v4u qv[2];
      qv[0] = __builtin_amdgcn_raw_buffer_load_b128(rw, qo, (ABL & 1) ? OOB : 0, QLAUX);
      qv[1] = __builtin_amdgcn_raw_buffer_load_b128(rw, qo, (ABL & 3) ? OOB : 4096, QLAUX);
      __builtin_amdgcn_sched_barrier(0);
      if (lane == 0) FL[5] = 1u;
      if (k == 0) STAMP(17);
#pragma unroll
      for (int i = QPOS; i < QPOS + WINL; ++i) { VMW(WINL + 1); LDV(i); }
      VMW(WINL);
      const bool qok = (ABL & 7) ? (qv[0].y != 12345u || tag != 77u) : tags_ok<2>(qv, tag);
      q2[0] = ldsc(rw, qo, qok ? OOB : 0);
      q2[1] = ldsc(rw, qo, qok ? OOB : 4096);
      __builtin_amdgcn_sched_barrier(0);
#pragma unroll
      for (int i = QPOS + WINL; i < VB1; ++i) LDS_(i);
      if (k == 0) STAMP(13);
      unsigned np = 1;
      if (!qok) {
        qv[0] = q2[0]; qv[1] = q2[1]; ++np;
        if (!tags_ok<2>(qv, tag)) np += poll<2>(rw, qo, 4096, tag, qv);
      }
      if (k == 0) RAW(28, np);
      if (k == 0) STAMP(3);
      if (k == 1) STAMP(26);
      if (k == 2) STAMP(27);
      if (k == 3) STAMP(1);
      L[L_Q + 64 * k + lane] = pk2(asf(qv[0].x) + asf(qv[1].x), asf(qv[0].z) + asf(qv[1].z));
    }
    BARRIER();
#pragma unroll
    for (int i = VB1; i < VB2; ++i) LDS_(i);
    if (k == 0) STAMP(24);
    v4u q[4];
#pragma unroll
    for (int t = 0; t < 4; ++t) {
      const int qa = L_Q + ((lane >> 2) & 1) * 128 + (t & 1) * 64 + g4 * 16 + (lane & 3) * 4;
      q[t] = *(const LAS v4u*)(L + ((n16 >> 3) == (t >> 1) ? qa : L_Z));
    }
    float y[4];
#pragma unroll
    for (int t = 0; t < 4; ++t) {
      f32x4 d = {0.f, 0.f, 0.f, 0.f};
      d = mfma32(q[0], kf[2 * t], d); d = mfma32(q[1], kf[2 * t + 1], d);
      d = mfma32(q[2], kf[2 * t + 8], d); d = mfma32(q[3], kf[2 * t + 9], d);
      const float lo = (lane & 1) ? d[1] : d[0], hi = (lane & 1) ? d[3] : d[2];
      y[t] = (lane & 2) ? hi : lo;
    }
    {
      const float k0 = (lane & 2) ? y[2] : y[0], s0 = (lane & 2) ? y[0] : y[2], k1 = (lane & 2) ? y[3] : y[1], s1 = (lane & 2) ? y[1] : y[3];
      y[0] = k0 + dpp<XOR2>(s0); y[1] = k1 + dpp<XOR2>(s1);
    }
    const float sc = rbf(((lane & 1) ? y[1] : y[0]) + dpp<XOR1>((lane & 1) ? y[0] : y[1]));
    const float m = x32_max(row_max(sc));
    const int a = g4 & 1;
    if ((lane & 0x2f) == 0) L[L_WM + 2 * k + a] = asu(m);
    BARRIER();
#pragma unroll
    for (int i = VB2; i < VB3; ++i) LDS_(i);
    const float M = fmaxf(fmaxf(asf(L[L_WM + a]), asf(L[L_WM + 2 + a])), fmaxf(asf(L[L_WM + 4 + a]), asf(L[L_WM + 6 + a])));
    const float p = __builtin_amdgcn_exp2f((sc - M) * C2);
    L[L_P + a * 128 + (4 * (k & 1) + (n16 >> 2)) * 16 + 2 * ((lane & 3) + 4 * (g4 >> 1)) + (k >> 1)] = pk2(p, p);
    float l = row_sum(p); l = sw32(l, l);
    if ((lane & 0x2f) == 0) L[L_WL + 2 * k + a] = asu(l);
    BARRIER();
#pragma unroll
    for (int i = VB3; i < VB4; ++i) LDS_(i);
    if (k == 0) STAMP(9);
    {
      v4u pa[4];
#pragma unroll
      for (int t = 0; t < 4; ++t) pa[t] = *(const LAS v4u*)(L + L_P + (n16 >> 3) * 128 + sa * 16 + 4 * t);
      int od = WS_O + (h + 8 * j) * O_PITCH + (lane >> 5) * 1024 + k * 256 + (2 * c8 + (g4 & 1)) * 16 + (nlo ? 0 : OOB);
      asm volatile("" : "+v"(od));
      f32x4 a0 = {0.f, 0.f, 0.f, 0.f}, a1 = {0.f, 0.f, 0.f, 0.f};
#pragma unroll
      for (int i = 0; i < 16; ++i) {
        const unsigned c = pa[i >> 2][i & 3];
        const v2u am = {c & msk2.x, c & msk2.y};
        a0 = mfma16(am, v2u{vf[i].x, vf[i].y}, a0);
        a1 = mfma16(am, v2u{vf[i].z, vf[i].w}, a1);
        __builtin_amdgcn_sched_barrier(0);
        if (i == QPOS + WINL) asm volatile("" ::"v"(q2[0]), "v"(q2[1]));
        {
          const int nx = i + WOWIN;
          if (nx >= VB4 && nx < 16 + WPRE && nx < 24 - WEARLY && i < 15) LDS_(nx);
        }
      }
      const f32x4 o = fold4<true>(a0, a1);
      stsc(rw, od, pk2(o[0], o[1]), pk2(o[2], o[3]), tag);
      __builtin_amdgcn_sched_barrier(0);
    }
    if (lane == 0) FL[8] = 1u;
    if (k == 3) STAMP(14);
    if (k == 0) STAMP(15);
#pragma unroll
    for (int i = 16 + (WPRE < WOWIN - 1 ? WPRE : WOWIN - 1); i < 24 - WEARLY; ++i) LDS_(i);
    if (isE || isD) {
      v4u msk = {(c8 >> 1) == 0 ? hm : 0u, (c8 >> 1) == 1 ? hm : 0u, (c8 >> 1) == 2 ? hm : 0u, (c8 >> 1) == 3 ? hm : 0u};
      const int dead = (k < 3 || lane < 32) ? 0 : OOB;
      int sd = isE ? WS_S + (h * 16 + ei) * S_PITCH + (cA + (lane >> 5)) * 256 + (2 * c8 + (g4 & 1)) * 16 + (nlo ? dead : OOB)
                   : WS_F + u * F_PITCH + (cA + (lane >> 5)) * 512 + gidx * 16 + dead;
      int lw = L_OP + k * 32 + (4 * (c8 & 1) + g4) * 4 + (c8 >> 1) + (nlo ? 0 : 256), lr = L_OP + rs * 4;
      unsigned tv = tag;
      asm volatile("" : "+v"(msk), "+v"(sd), "+v"(lw), "+v"(lr), "+v"(tv));
      if (isE) {
        if (ODLY) __builtin_amdgcn_s_sleep(ODLY);
        v4u ov[1];
        const int po = WS_O + (h + 8 * (8 * k + rs)) * O_PITCH + ei * 128 + c8 * 16;
        ov[0] = ldsc(rw, po, 0);
        __builtin_amdgcn_sched_barrier(0);
        v4u ov2[1];
        if (ODLY2) { __builtin_amdgcn_s_sleep(ODLY2); CFENCE(); ov2[0] = ldsc(rw, po, 0); __builtin_amdgcn_sched_barrier(0); }
        lds_wait<false>(FL + 7, 1u);
        const float wt = asf(L[L_W + 8 * k + rs]);
        asm volatile("" ::"v"(wt));
        unsigned np2 = 1;
        if (!tags_ok<1>(ov, tag)) {
          if (ODLY2) ov[0] = ov2[0];
          if (!ODLY2 || !tags_ok<1>(ov, tag)) np2 += poll<1>(rw, po, 0, tag, ov);
        }
        if (k == 0) RAW(30, np2);
        if (k == 0) STAMP(7);
        L[lw] = asu(red4(wt * blo(ov[0].x), wt * bhi(ov[0].x), wt * blo(ov[0].z), wt * bhi(ov[0].z)));
        BARRIER();
        if (k == 0) STAMP(25);
        const f32x4 os = (__builtin_bit_cast(f32x4, *(const LAS v4u*)(L + lr)) + __builtin_bit_cast(f32x4, *(const LAS v4u*)(L + lr + 32))) +
                         (__builtin_bit_cast(f32x4, *(const LAS v4u*)(L + lr + 64)) + __builtin_bit_cast(f32x4, *(const LAS v4u*)(L + lr + 96)));
        f32x4 a0 = {0.f, 0.f, 0.f, 0.f}, a1 = {0.f, 0.f, 0.f, 0.f};
#pragma unroll
        for (int i = 0; i < 4; ++i) { const v4u am = amask(pk2(os[i], os[i]), msk); a0 = mfma32(am, wf[2 * i], a0); a1 = mfma32(am, wf[2 * i + 1], a1); }
        const f32x4 s = fold4<false>(a0, a1);
        stsc(rw, sd, pk2(s[0], s[1]), pk2(s[2], s[3]), tv);
        if (ODLY2) asm volatile("" ::"v"(ov2[0]));
        if (k == 0) STAMP(10);
      }
      CFENCE();
      if (isD) {
#pragma unroll
        for (int i = 0; i < WEARLY; ++i) LDW(i);
        BARRIER();
        const f32x4 os = __builtin_bit_cast(f32x4, *(const LAS v4u*)(L + lr));
        f32x4 a0 = {0.f, 0.f, 0.f, 0.f}, a1 = {0.f, 0.f, 0.f, 0.f};
#pragma unroll
        for (int i = 3; i >= 0; --i) { const v4u am = amask(pk2(os[i], os[i]), msk); a0 = mfma32(am, wf[2 * i], a0); a1 = mfma32(am, wf[2 * i + 1], a1); }
        float v0, v1;
        fold2<false>(a0, a1, nlo, v0, v1);
        stsc(rw, sd, asu(v0), asu(v1), tv);
        if (k == 0) STAMP(11);
      }
    } else if (fin) {
      int lw = L_OP + 32 * k + g4 * 8 + c8 + (nlo ? 0 : 256);
      asm volatile("" : "+v"(lw));
      v4u sv[4];
      const unsigned np3 = poll<4>(rw, WS_S + (32 * k + rs) * S_PITCH + fc * 128 + c8 * 16, 8 * S_PITCH, tag, sv); if (k == 0) RAW(31, np3); (void)np3;
      const float s = red4((blo(sv[0].x) + blo(sv[1].x)) + (blo(sv[2].x) + blo(sv[3].x)), (bhi(sv[0].x) + bhi(sv[1].x)) + (bhi(sv[2].x) + bhi(sv[3].x)),
                           (blo(sv[0].z) + blo(sv[1].z)) + (blo(sv[2].z) + blo(sv[3].z)), (bhi(sv[0].z) + bhi(sv[1].z)) + (bhi(sv[2].z) + bhi(sv[3].z)));
      L[lw] = asu(s);
      if (k == 0) STAMP(20); if (k == 1) STAMP(21); if (k == 2) STAMP(22); if (k == 3) STAMP(23);
      BARRIER();
    }
    STAMP_FLUSH();
  }
}

extern "C" void kernel_launch(void* const* d_in, const int* in_sizes, int n_in, void* d_out, int out_size, void* d_ws, size_t ws_size,
                              hipStream_t stream) {
  fused_decode<<<NWG, NTHR, 0, stream>>>(d_in[0], d_in[1], d_in[2], d_in[3], d_in[4], d_in[6], d_in[7], d_in[8], d_out, d_ws);
}
```

```cpp
#define PROBE 27
#include <hip/hip_runtime.h>
#include <stdint.h>

#define GAS __attribute__((address_space(1)))
#define LAS __attribute__((address_space(3)))
typedef unsigned v4u __attribute__((ext_vector_type(4)));
typedef unsigned v2u __attribute__((ext_vector_type(2)));
typedef float f32x4 __attribute__((ext_vector_type(4)));
typedef __bf16 bf16x8 __attribute__((ext_vector_type(8)));
typedef __bf16 bf16x2 __attribute__((ext_vector_type(2)));
typedef short s16x4 __attribute__((ext_vector_type(4)));
#define RLX_AGENT __ATOMIC_RELAXED, __HIP_MEMORY_SCOPE_AGENT

#define WINL 6
#define VB1 10
#define VB2 12
#define VB3 14
#define VB4 VB3
#define QPOS 0
#define ABL 0
#define QLAUX 16
#define ROT 1
#define OAUX 16
#define WPRE 6
#define ODLY2 0
#define WEARLY 2
#define WOWIN 7
#define QSAUX 16
#define ODLY 12
#define CODE_BYTES 9728

constexpr int DM = 448, SK = 65536, WIN = 4096;
constexpr int NWG = 256, NTHR = 320;
constexpr int WS_Q = 4096;
constexpr int WS_GU = WS_Q + 8 * 8192;
constexpr int WS_ML = WS_GU + 28 * 2048;
constexpr int O_PITCH = 2176;
constexpr int WS_O = WS_ML + 8192;
constexpr int S_PITCH = 1792;
constexpr int WS_S = WS_O + 256 * O_PITCH;
constexpr int F_PITCH = 3584;
constexpr int WS_F = WS_S + 128 * S_PITCH;
constexpr int WS_END = WS_F + 56 * F_PITCH;
constexpr int OOB = 1 << 30;
constexpr int L_R1 = 0;
constexpr int L_Q = 256;
constexpr int L_P = 512;
constexpr int L_W = 800;
constexpr int L_WM = 832;
constexpr int L_WL = 840;
constexpr int L_FLAG = 928;
constexpr int L_Z = 944;
constexpr int L_OP = 960;
constexpr int L_TOTAL = 21504;
constexpr unsigned SPIN_CAP = 1u << 16;

#define RAW(k, v)
#define STAMP(k) do { if ((k) == PROBE) stp = __builtin_amdgcn_s_memrealtime(); } while (0)
#define STAMP_DECL const unsigned long long st0 = __builtin_amdgcn_s_memrealtime(); unsigned long long stp = 0
#define STAMP_FLUSH() do { if (stp) { const unsigned long long tend = st0 + 3000 + 8 * (stp - st0); while (__builtin_amdgcn_s_memrealtime() < tend) __builtin_amdgcn_s_sleep(2); } } while (0)

__device__ __forceinline__ float asf(unsigned u) { return __builtin_bit_cast(float, u); }
__device__ __forceinline__ unsigned asu(float f) { return __builtin_bit_cast(unsigned, f); }
__device__ __forceinline__ float blo(unsigned u) { return asf(u << 16); }
__device__ __forceinline__ float bhi(unsigned u) { return asf(u & 0xffff0000u); }
__device__ __forceinline__ float rbf(float f) { return (float)(__bf16)f; }
__device__ __forceinline__ unsigned pk2(float lo, float hi) { bf16x2 t = {(__bf16)lo, (__bf16)hi}; return __builtin_bit_cast(unsigned, t); }
__device__ __forceinline__ f32x4 mfma32(v4u a, v4u b, f32x4 c) {
  return __builtin_amdgcn_mfma_f32_16x16x32_bf16(__builtin_bit_cast(bf16x8, a), __builtin_bit_cast(bf16x8, b), c, 0, 0, 0);
}
__device__ __forceinline__ f32x4 mfma16(v2u a, v2u b, f32x4 c) {
  return __builtin_amdgcn_mfma_f32_16x16x16bf16_1k(__builtin_bit_cast(s16x4, a), __builtin_bit_cast(s16x4, b), c, 0, 0, 0);
}
template <int CTRL> __device__ __forceinline__ float dpp(float x) {
  return asf((unsigned)__builtin_amdgcn_mov_dpp((int)asu(x), CTRL, 0xf, 0xf, true));
}
constexpr int XOR1 = 0xB1, XOR2 = 0x4E, XOR7 = 0x141, XOR8 = 0x128;
__device__ __forceinline__ float sw16(float a, float b) { auto s = __builtin_amdgcn_permlane16_swap(asu(a), asu(b), false, false); return asf(s[0]) + asf(s[1]); }
__device__ __forceinline__ float sw32(float a, float b) { auto s = __builtin_amdgcn_permlane32_swap(asu(a), asu(b), false, false); return asf(s[0]) + asf(s[1]); }
__device__ __forceinline__ float x16_max(float x) { auto s = __builtin_amdgcn_permlane16_swap(asu(x), asu(x), false, false); return fmaxf(asf(s[0]), asf(s[1])); }
__device__ __forceinline__ float x32_max(float x) { auto s = __builtin_amdgcn_permlane32_swap(asu(x), asu(x), false, false); return fmaxf(asf(s[0]), asf(s[1])); }
__device__ __forceinline__ float row_sum(float x) { x += dpp<XOR1>(x); x += dpp<XOR2>(x); x += dpp<XOR7>(x); x += dpp<XOR8>(x); return x; }
__device__ __forceinline__ float row_max(float x) { x = fmaxf(x, dpp<XOR1>(x)); x = fmaxf(x, dpp<XOR2>(x)); x = fmaxf(x, dpp<XOR7>(x)); return fmaxf(x, dpp<XOR8>(x)); }
__device__ __forceinline__ float wave_sum(float x) { x = row_sum(x); x = sw16(x, x); return sw32(x, x); }
__device__ __forceinline__ float red4(float v0, float v1, float v2, float v3) { const float t = sw16(sw32(v0, v2), sw32(v1, v3)); return t + dpp<XOR8>(t); }
__device__ __forceinline__ __amdgpu_buffer_rsrc_t mkrs(const GAS char* p, int bytes) { return __builtin_amdgcn_make_buffer_rsrc((void*)p, 0, bytes, 0x00020000); }
__device__ __forceinline__ v4u ldnt(__amdgpu_buffer_rsrc_t rs, int voff, int soff) {
  const v4u r = __builtin_amdgcn_raw_buffer_load_b128(rs, voff, soff, 2);
  __builtin_amdgcn_sched_barrier(0);
  return r;
}
__device__ __forceinline__ v4u ldsc(__amdgpu_buffer_rsrc_t rs, int voff, int soff) { return __builtin_amdgcn_raw_buffer_load_b128(rs, voff, soff, 16); }
template <int AUX = 16> __device__ __forceinline__ void stsc(__amdgpu_buffer_rsrc_t rs, int off, unsigned d0, unsigned d1, unsigned tag) {
  v4u g = {d0, tag, d1, tag};
  __builtin_amdgcn_raw_buffer_store_b128(g, rs, off, 0, AUX);
}
#define BARRIER() asm volatile("s_waitcnt lgkmcnt(0)\n\ts_barrier" ::: "memory")
#define CFENCE() asm volatile("" ::: "memory")
#define VMW(n) asm volatile("s_waitcnt vmcnt(%0)" ::"i"(n) : "memory")
__device__ __forceinline__ v4u amask(unsigned x, const v4u& m) { return v4u{x & m.x, x & m.y, x & m.z, x & m.w}; }
template <bool R16> __device__ __forceinline__ f32x4 fold4(const f32x4& A, const f32x4& B) {
  f32x4 s;
#pragma unroll
  for (int r = 0; r < 4; ++r) {
    auto t = R16 ? __builtin_amdgcn_permlane16_swap(asu(A[r]), asu(B[r]), false, false) : __builtin_amdgcn_permlane32_swap(asu(A[r]), asu(B[r]), false, false);
    s[r] = asf(t[0]) + dpp<XOR8>(asf(t[1]));
  }
  return s;
}
template <bool R16> __device__ __forceinline__ void fold2(const f32x4& A, const f32x4& B, bool nlo, float& v0, float& v1) {
  float s[4];
#pragma unroll
  for (int r = 0; r < 4; ++r) {
    auto t = R16 ? __builtin_amdgcn_permlane16_swap(asu(A[r]), asu(B[r]), false, false) : __builtin_amdgcn_permlane32_swap(asu(A[r]), asu(B[r]), false, false);
    const float a = asf(t[0]), b = asf(t[1]);
    s[r] = r < 2 ? a + dpp<XOR8>(b) : dpp<XOR8>(a) + b;
  }
  v0 = nlo ? s[0] : s[2];
  v1 = nlo ? s[1] : s[3];
}
template <int N> __device__ __forceinline__ bool tags_ok(const v4u (&v)[N], unsigned tag) {
  bool ok = true;
#pragma unroll
  for (int k = 0; k < N; ++k) ok &= (v[k].y == tag) & (v[k].w == tag);
  return __builtin_amdgcn_ballot_w64(!ok) == 0ull;
}
template <int N> __device__ __forceinline__ unsigned poll(__amdgpu_buffer_rsrc_t rs, int off, int stride, unsigned tag, v4u (&v)[N]) {
  unsigned spins = 0;
#pragma unroll 1
  for (; spins < SPIN_CAP; ++spins) {
    CFENCE();
#pragma unroll
    for (int k = 0; k < N; ++k) v[k] = ldsc(rs, off, k * stride);
    if (tags_ok<N>(v, tag)) break;
  }
  return spins + 1;
}
template <bool SLEEP> __device__ __forceinline__ void lds_wait(volatile LAS unsigned* w, unsigned want) {
#pragma unroll 1
  for (unsigned spins = 0; spins < (SPIN_CAP << 6); ++spins) {
    if (__builtin_amdgcn_readfirstlane(*w) >= want) break;
    if (SLEEP) __builtin_amdgcn_s_sleep(1);
  }
  CFENCE();
}
__device__ __forceinline__ float silu(float x) { return x * __builtin_amdgcn_rcpf(1.f + __expf(-x)); }

typedef unsigned long long v2q __attribute__((ext_vector_type(2)));
typedef unsigned long long v4q __attribute__((ext_vector_type(4)));
__global__ void __launch_bounds__(NTHR) fused_decode(const void* x_, const void* kc_, const void* vc_, const void* wq_, const void* wo_, const void* wg_,
                                                     const void* wu_, const void* wd_, void* out_, void* ws_) {
  v4q ka, kb; v2q kd;
  asm volatile("s_load_dwordx8 %0, %3, 0x0\n\ts_load_dwordx8 %1, %3, 0x20\n\ts_load_dwordx4 %2, %3, 0x40"
               : "=&s"(ka), "=&s"(kb), "=&s"(kd) : "s"(__builtin_amdgcn_kernarg_segment_ptr()));
  __builtin_amdgcn_sched_barrier(0);
  __shared__ __attribute__((aligned(16))) unsigned lds_[L_TOTAL];
  LAS unsigned* L = (LAS unsigned*)lds_;
  volatile LAS unsigned* FL = (volatile LAS unsigned*)(L + L_FLAG);
  const int b = blockIdx.x;
  const int h = (b >> 3) & 7, j = (b & 7) * 4 + (b >> 6);
  const int tid = threadIdx.x, lane = tid & 63, w = __builtin_amdgcn_readfirstlane(tid >> 6);
  const int u = (b & 63) + 64 * (b >> 7);
  STAMP_DECL;
  if (w == 0) STAMP(5);
  const __amdgpu_buffer_rsrc_t rc = mkrs((const GAS char*)(__builtin_amdgcn_s_getpc() & ~255ull), (w == 4 && u >= 120) ? CODE_BYTES : 0);
  const unsigned cpf = __builtin_amdgcn_raw_buffer_load_b32(rc, lane * 128, 1024, 0), cpf2 = __builtin_amdgcn_raw_buffer_load_b32(rc, lane * 128, 9216, 0);
  const bool isE = (j & 1) == 0;
  const int ei = j >> 1;
  const bool isD = !isE && u < 56;
  const bool fin = !isE && u >= 114;
  const int fc = u - 114;
  const int rs = lane >> 3, c8 = lane & 7, g4 = lane >> 4, n16 = lane & 15;
  const bool nlo = n16 < 8;
  const int gidx = 4 * c8 + 2 * (g4 & 1) + (n16 >> 3);
  const int NI = isE ? 6 : 8;
  int r0 = (isE ? 256 : 0) + 8 * NI * (w & 3);
  int pitch = u < 64 ? 8192 : 3584;
  int sb = r0 * pitch + (u < 64 ? u : u < 92 ? u - 64 : u - 92) * 128;
  int nb1 = u < 120 ? DM * pitch : 0;
  const int sa = 2 * g4 + ((lane >> 2) & 1);
  int fo = rs * NI * pitch + c8 * 16, xo = lane * 16, xso = sa * NI * 2;
  const int wre = 512 * h + 32 * ei, wrd = u < 56 ? 32 * u : 0;
  int wrow = (wrd + ((wre - wrd) & ((j & 1) - 1))) * 896;
  int oo = rs * 896 + c8 * 16;
  asm volatile("" ::"s"(r0), "s"(pitch), "s"(sb), "s"(nb1), "s"(wrow), "v"(fo), "v"(xo), "v"(xso), "v"(oo));
  __builtin_amdgcn_sched_barrier(0);
  STAMP(27);
  __builtin_amdgcn_sched_barrier(0);
  asm volatile("s_waitcnt lgkmcnt(0)" : "+s"(ka), "+s"(kb), "+s"(kd));
  __builtin_amdgcn_sched_barrier(0);
  STAMP(26);
  __builtin_amdgcn_sched_barrier(0);
  const GAS char *x = (const GAS char*)ka.x, *kc = (const GAS char*)ka.y, *vc = (const GAS char*)ka.z, *wq = (const GAS char*)ka.w;
  const GAS char *wo = (const GAS char*)kb.x, *wg = (const GAS char*)kb.y, *wu = (const GAS char*)kb.z, *wd = (const GAS char*)kb.w;
  GAS char *out = (GAS char*)kd.x, *ws = (GAS char*)kd.y;
  const unsigned ep = *(const __attribute__((address_space(4))) unsigned*)(unsigned long long)ws;
  const __amdgpu_buffer_rsrc_t rw = mkrs(ws, WS_END);
  constexpr float C2 = 0.0625f * 1.4426950408889634f;

  if (w == 4) {
    STAMP(0);
    __builtin_amdgcn_s_setprio(3);
    if (lane < 20) FL[lane] = 0u;
    asm volatile("s_waitcnt lgkmcnt(0)\n\ts_barrier" ::"s"(ep) : "memory");
    const unsigned tag = ep + 1u;
    if (u < 120) {
      const v2u r0 = *(const LAS v2u*)(L + L_R1 + 2 * (lane & 31)), r1 = *(const LAS v2u*)(L + L_R1 + 64 + 2 * (lane & 31)),
                r2 = *(const LAS v2u*)(L + L_R1 + 128 + 2 * (lane & 31)), r3 = *(const LAS v2u*)(L + L_R1 + 192 + 2 * (lane & 31));
      const float v0 = (asf(r0.x) + asf(r1.x)) + (asf(r2.x) + asf(r3.x)), v1 = (asf(r0.y) + asf(r1.y)) + (asf(r2.y) + asf(r3.y));
      const int hf = isE ? 1 : 0;
      const int dst = u < 64 ? WS_Q + (u >> 3) * 8192 + hf * 4096 + (u & 7) * 512 : u < 92 ? WS_GU + (u - 64) * 2048 + hf * 512 : WS_GU + (u - 92) * 2048 + 1024 + hf * 512;
      if (lane < 32) stsc<QSAUX>(rw, dst + lane * 16, asu(v0), asu(v1), tag);
    }
    STAMP(2);
    const unsigned xw = __builtin_amdgcn_raw_buffer_load_b16(mkrs(x, 896), (4 * c8 + g4) * 2, fin ? fc * 64 : 0, 0);
    const int go = WS_GU + (u >> 1) * 2048 + g4 * 512 + (u & 1) * 256 + n16 * 16;
    v4u gu[1];
    lds_wait<true>(FL + 5, 1u);
    gu[0] = ldsc(rw, go, isD ? 0 : OOB);
    asm volatile("" ::"v"(cpf), "v"(cpf2));
    BARRIER();
    BARRIER();
    BARRIER();
    if (lane < 2) {
      const float M = fmaxf(fmaxf(asf(L[L_WM + lane]), asf(L[L_WM + 2 + lane])), fmaxf(asf(L[L_WM + 4 + lane]), asf(L[L_WM + 6 + lane])));
      const float Ls = (asf(L[L_WL + lane]) + asf(L[L_WL + 2 + lane])) + (asf(L[L_WL + 4 + lane]) + asf(L[L_WL + 6 + lane]));
      stsc(rw, WS_ML + (h * 2 + lane) * 512 + j * 16, asu(M), asu(Ls), tag);
    }
    STAMP(4);
    if (isE) {
      lds_wait<true>(FL + 8, 1u);
      v4u ml[1];
      const unsigned np1 = poll<1>(rw, WS_ML + (h * 2 + (ei >> 3)) * 512 + (lane & 31) * 16, 0, tag, ml); RAW(29, np1); (void)np1;
      const float m = asf(ml[0].x);
      const float M = x16_max(row_max(m));
      const float e = __builtin_amdgcn_exp2f((m - M) * C2);
      float Ls = row_sum(e * asf(ml[0].z)); Ls = sw16(Ls, Ls);
      if (lane < 32) L[L_W + lane] = asu(e * __builtin_amdgcn_rcpf(Ls));
      if (lane == 0) FL[7] = 1u;
      STAMP(6);
    } else if (isD) {
      if (!tags_ok<1>(gu, tag)) poll<1>(rw, go, 0, tag, gu);
      const float y = rbf(sw16(asf(gu[0].x), asf(gu[0].z)));
      auto t = __builtin_amdgcn_permlane32_swap(asu(y), asu(y), false, false);
      const float hv = rbf(rbf(silu(asf(t[0]))) * asf(t[1]));
      const int r = 2 * n16 + (g4 & 1);
      if (lane < 32) L[L_OP + (r & 7) * 4 + (r >> 3)] = asu(hv);
      STAMP(6);
      BARRIER();
    } else if (fin) {
      lds_wait<true>(FL + 8, 1u);
      v4u fv[14];
      poll<14>(rw, WS_F + g4 * F_PITCH + fc * 256 + n16 * 16, 4 * F_PITCH, tag, fv);
      float f0 = 0.f, f1 = 0.f;
#pragma unroll
      for (int i = 0; i < 14; ++i) { f0 += asf(fv[i].x); f1 += asf(fv[i].z); }
      float f = sw16(f0, f1); f = sw32(f, f);
      const float g = rbf(silu(rbf(f)));
      if (lane < 32) L[L_W + 2 * n16 + (g4 & 1)] = asu(g);
      asm volatile("s_waitcnt lgkmcnt(0)" ::: "memory");
      const float gc = asf(L[L_W + 4 * c8 + g4]);
      int so = L_OP + g4 * 8 + c8, oa = (4 * c8 + g4) * 2 + (nlo ? 0 : OOB);
      float xf = blo(xw);
      asm volatile("" : "+v"(so), "+v"(oa), "+v"(xf));
      STAMP(16);
      BARRIER();
      const float s = rbf((asf(L[so]) + asf(L[so + 32])) + (asf(L[so + 64]) + asf(L[so + 96])));
      const float res = xf + rbf(s * gc);
      __builtin_amdgcn_raw_buffer_store_b16((unsigned short)pk2(res, res), mkrs(out, 896), oa, fc * 64, OAUX);
      if (fc == 0 && lane == 0) __hip_atomic_store((GAS unsigned*)ws, tag, RLX_AGENT);
      STAMP(8);
    }
    STAMP_FLUSH();
  } else {
    const int k = w;
    const unsigned hm = (lane & 1) ? 0xffff0000u : 0x0000ffffu;
    v4u kf[16], vf[16], wf[8];
    const __amdgpu_buffer_rsrc_t rx = mkrs(x, 896);
    const v4u xr = __builtin_amdgcn_raw_buffer_load_b128(rx, xo, 0, 0);
    const v4u xs = __builtin_amdgcn_raw_buffer_load_b128(rx, xso, r0 * 2, 0);
    __builtin_amdgcn_sched_barrier(0);
    STAMP(13 + 100);
    v4u fr[8];
    {
      const __amdgpu_buffer_rsrc_t ra = mkrs(u < 64 ? wq : u < 92 ? wg : wu, nb1);
#pragma unroll
      for (int i = 0; i < 8; ++i) fr[i] = ldnt(ra, fo, (i >= 6 && isE) ? OOB : sb + i * pitch);
    }
    const v2u msk2 = {(lane & 2) ? 0u : hm, (lane & 2) ? hm : 0u};
    const int cA = 2 * k, sA = cA * 128, sB = k < 3 ? sA + 128 : OOB;
    const __amdgpu_buffer_rsrc_t ro = mkrs((isE ? wo : wd) + wrow, (isE || isD) ? 32 * 896 : 0);
#define LDW(i) wf[i] = ldnt(ro, oo, ((i) >> 1) * 8 * 896 + (((i) & 1) ? sB : sA))
#pragma unroll
    for (int i = 0; i < 2; ++i) wf[i] = ldnt(ro, oo, isE ? (i >> 1) * 8 * 896 + ((i & 1) ? sB : sA) : OOB);
    const __amdgpu_buffer_rsrc_t rk = mkrs(kc + ((size_t)h * SK + (SK - WIN) + 128 * j) * 512, 128 * 512);
    const __amdgpu_buffer_rsrc_t rv = mkrs(vc + ((size_t)h * SK + (SK - WIN) + 128 * j) * 512, 128 * 512);
    const int ko = k * 2048 + (n16 >> 2) * 512 + g4 * 64 + (lane & 3) * 16;
    const int vo = rs * 512 + c8 * 16 + k * 128;
    const int rot = ROT ? ((b + (b >> 3) + (b >> 6)) & 7) * 8192 : 0;
#define LDK(i) kf[i] = ldnt(rk, ko + ((i) & 1) * 256, (((i) >> 1) * 8192 + rot) & 0xffff)
#define LDV(i) vf[i] = ldnt(rv, vo, ((i) * 4096 + rot) & 0xffff)
#define LDS_(n) do { if ((n) < 16) LDV((n) < 16 ? (n) : 0); else LDW((n) >= 16 ? (n) - 16 + WEARLY : 2); } while (0)
    if (k == 0) STAMP(18);
    {
      float xv[8];
#pragma unroll
      for (int e = 0; e < 4; ++e) { xv[2 * e] = blo(xr[e]); xv[2 * e + 1] = bhi(xr[e]); }
      const float s1 = ((xv[0] + xv[1]) + (xv[2] + xv[3])) + ((xv[4] + xv[5]) + (xv[6] + xv[7]));
      const float mean = wave_sum(s1) * (1.f / DM);
      float s2 = 0.f;
#pragma unroll
      for (int e = 0; e < 8; ++e) { const float d = xv[e] - mean; s2 += d * d; }
      s2 = lane < 56 ? s2 : 0.f;
      const float var = wave_sum(s2) * (1.f / DM);
      const float mb = rbf(mean), rden = __builtin_amdgcn_rcpf(rbf(__builtin_amdgcn_sqrtf(rbf(rbf(var) + 1e-5f))));
      unsigned xc[8];
#pragma unroll
      for (int i = 0; i < 8; ++i) {
        const float xn = rbf(((i & 1) ? bhi(xs[i >> 1]) : blo(xs[i >> 1])) - mb) * rden;
        xc[i] = pk2(xn, xn);
      }
      if (k == 0) STAMP(19);
      __builtin_amdgcn_sched_barrier(0);
      f32x4 a0 = {0.f, 0.f, 0.f, 0.f}, a1 = {0.f, 0.f, 0.f, 0.f};
#pragma unroll
      for (int i = 0; i < 8; ++i) {
        const v2u am = {xc[i] & msk2.x, xc[i] & msk2.y};
        a0 = mfma16(am, v2u{fr[i].x, fr[i].y}, a0);
        a1 = mfma16(am, v2u{fr[i].z, fr[i].w}, a1);
        __builtin_amdgcn_sched_barrier(0);
        if (i >= 8 - WINL) LDK(i - (8 - WINL));
      }
      float v0, v1;
      fold2<true>(a0, a1, nlo, v0, v1);
      if (lane < 32) *(LAS v2u*)(L + L_R1 + 64 * k + 2 * gidx) = v2u{asu(v0), asu(v1)};
      asm volatile("s_waitcnt lgkmcnt(0)\n\ts_barrier" ::"s"(ep), "v"(cpf), "v"(cpf2) : "memory");
      if (k == 0) STAMP(12);
    }
    const unsigned tag = ep + 1u;
#pragma unroll
    for (int i = WINL; i < 16; ++i) {
      VMW(WINL - 1); LDK(i);
      if (i - WINL + 2 < WEARLY) wf[i - WINL + 2] = ldnt(ro, oo, isE ? ((i - WINL + 2) >> 1) * 8 * 896 + (((i - WINL + 2) & 1) ? sB : sA) : OOB);
    }
#pragma unroll
    for (int i = 0; i < QPOS; ++i) { VMW(WINL - 1); LDV(i); }
    v4u q2[2];
    {
      const int qo = WS_Q + h * 8192 + k * 1024 + lane * 16;
      v4u qv[2];
      qv[0] = __builtin_amdgcn_raw_buffer_load_b128(rw, qo, (ABL & 1) ? OOB : 0, QLAUX);
      qv[1] = __builtin_amdgcn_raw_buffer_load_b128(rw, qo, (ABL & 3) ? OOB : 4096, QLAUX);
      __builtin_amdgcn_sched_barrier(0);
      if (lane == 0) FL[5] = 1u;
      if (k == 0) STAMP(17);
#pragma unroll
      for (int i = QPOS; i < QPOS + WINL; ++i) { VMW(WINL + 1); LDV(i); }
      VMW(WINL);
      const bool qok = (ABL & 7) ? (qv[0].y != 12345u || tag != 77u) : tags_ok<2>(qv, tag);
      q2[0] = ldsc(rw, qo, qok ? OOB : 0);
      q2[1] = ldsc(rw, qo, qok ? OOB : 4096);
      __builtin_amdgcn_sched_barrier(0);
#pragma unroll
      for (int i = QPOS + WINL; i < VB1; ++i) LDS_(i);
      if (k == 0) STAMP(13);
      unsigned np = 1;
      if (!qok) {
        qv[0] = q2[0]; qv[1] = q2[1]; ++np;
        if (!tags_ok<2>(qv, tag)) np += poll<2>(rw, qo, 4096, tag, qv);
      }
      if (k == 0) RAW(28, np);
      if (k == 0) STAMP(3);
      if (k == 3) STAMP(1);
      L[L_Q + 64 * k + lane] = pk2(asf(qv[0].x) + asf(qv[1].x), asf(qv[0].z) + asf(qv[1].z));
    }
    BARRIER();
#pragma unroll
    for (int i = VB1; i < VB2; ++i) LDS_(i);
    if (k == 0) STAMP(24);
    v4u q[4];
#pragma unroll
    for (int t = 0; t < 4; ++t) {
      const int qa = L_Q + ((lane >> 2) & 1) * 128 + (t & 1) * 64 + g4 * 16 + (lane & 3) * 4;
      q[t] = *(const LAS v4u*)(L + ((n16 >> 3) == (t >> 1) ? qa : L_Z));
    }
    float y[4];
#pragma unroll
    for (int t = 0; t < 4; ++t) {
      f32x4 d = {0.f, 0.f, 0.f, 0.f};
      d = mfma32(q[0], kf[2 * t], d); d = mfma32(q[1], kf[2 * t + 1], d);
      d = mfma32(q[2], kf[2 * t + 8], d); d = mfma32(q[3], kf[2 * t + 9], d);
      const float lo = (lane & 1) ? d[1] : d[0], hi = (lane & 1) ? d[3] : d[2];
      y[t] = (lane & 2) ? hi : lo;
    }
    {
      const float k0 = (lane & 2) ? y[2] : y[0], s0 = (lane & 2) ? y[0] : y[2], k1 = (lane & 2) ? y[3] : y[1], s1 = (lane & 2) ? y[1] : y[3];
      y[0] = k0 + dpp<XOR2>(s0); y[1] = k1 + dpp<XOR2>(s1);
    }
    const float sc = rbf(((lane & 1) ? y[1] : y[0]) + dpp<XOR1>((lane & 1) ? y[0] : y[1]));
    const float m = x32_max(row_max(sc));
    const int a = g4 & 1;
    if ((lane & 0x2f) == 0) L[L_WM + 2 * k + a] = asu(m);
    BARRIER();
#pragma unroll
    for (int i = VB2; i < VB3; ++i) LDS_(i);
    const float M = fmaxf(fmaxf(asf(L[L_WM + a]), asf(L[L_WM + 2 + a])), fmaxf(asf(L[L_WM + 4 + a]), asf(L[L_WM + 6 + a])));
    const float p = __builtin_amdgcn_exp2f((sc - M) * C2);
    L[L_P + a * 128 + (4 * (k & 1) + (n16 >> 2)) * 16 + 2 * ((lane & 3) + 4 * (g4 >> 1)) + (k >> 1)] = pk2(p, p);
    float l = row_sum(p); l = sw32(l, l);
    if ((lane & 0x2f) == 0) L[L_WL + 2 * k + a] = asu(l);
    BARRIER();
#pragma unroll
    for (int i = VB3; i < VB4; ++i) LDS_(i);
    if (k == 0) STAMP(9);
    {
      v4u pa[4];
#pragma unroll
      for (int t = 0; t < 4; ++t) pa[t] = *(const LAS v4u*)(L + L_P + (n16 >> 3) * 128 + sa * 16 + 4 * t);
      int od = WS_O + (h + 8 * j) * O_PITCH + (lane >> 5) * 1024 + k * 256 + (2 * c8 + (g4 & 1)) * 16 + (nlo ? 0 : OOB);
      asm volatile("" : "+v"(od));
      f32x4 a0 = {0.f, 0.f, 0.f, 0.f}, a1 = {0.f, 0.f, 0.f, 0.f};
#pragma unroll
      for (int i = 0; i < 16; ++i) {
        const unsigned c = pa[i >> 2][i & 3];
        const v2u am = {c & msk2.x, c & msk2.y};
        a0 = mfma16(am, v2u{vf[i].x, vf[i].y}, a0);
        a1 = mfma16(am, v2u{vf[i].z, vf[i].w}, a1);
        __builtin_amdgcn_sched_barrier(0);
        if (i == QPOS + WINL) asm volatile("" ::"v"(q2[0]), "v"(q2[1]));
        {
          const int nx = i + WOWIN;
          if (nx >= VB4 && nx < 16 + WPRE && nx < 24 - WEARLY && i < 15) LDS_(nx);
        }
      }
      const f32x4 o = fold4<true>(a0, a1);
      stsc(rw, od, pk2(o[0], o[1]), pk2(o[2], o[3]), tag);
      __builtin_amdgcn_sched_barrier(0);
    }
    if (lane == 0) FL[8] = 1u;
    if (k == 3) STAMP(14);
    if (k == 0) STAMP(15);
#pragma unroll
    for (int i = 16 + (WPRE < WOWIN - 1 ? WPRE : WOWIN - 1); i < 24 - WEARLY; ++i) LDS_(i);
    if (isE || isD) {
      v4u msk = {(c8 >> 1) == 0 ? hm : 0u, (c8 >> 1) == 1 ? hm : 0u, (c8 >> 1) == 2 ? hm : 0u, (c8 >> 1) == 3 ? hm : 0u};
      const int dead = (k < 3 || lane < 32) ? 0 : OOB;
      int sd = isE ? WS_S + (h * 16 + ei) * S_PITCH + (cA + (lane >> 5)) * 256 + (2 * c8 + (g4 & 1)) * 16 + (nlo ? dead : OOB)
                   : WS_F + u * F_PITCH + (cA + (lane >> 5)) * 512 + gidx * 16 + dead;
      int lw = L_OP + k * 32 + (4 * (c8 & 1) + g4) * 4 + (c8 >> 1) + (nlo ? 0 : 256), lr = L_OP + rs * 4;
      unsigned tv = tag;
      asm volatile("" : "+v"(msk), "+v"(sd), "+v"(lw), "+v"(lr), "+v"(tv));
      if (isE) {
        if (ODLY) __builtin_amdgcn_s_sleep(ODLY);
        v4u ov[1];
        const int po = WS_O + (h + 8 * (8 * k + rs)) * O_PITCH + ei * 128 + c8 * 16;
        ov[0] = ldsc(rw, po, 0);
        __builtin_amdgcn_sched_barrier(0);
        v4u ov2[1];
        if (ODLY2) { __builtin_amdgcn_s_sleep(ODLY2); CFENCE(); ov2[0] = ldsc(rw, po, 0); __builtin_amdgcn_sched_barrier(0); }
        lds_wait<false>(FL + 7, 1u);
        const float wt = asf(L[L_W + 8 * k + rs]);
        asm volatile("" ::"v"(wt));
        unsigned np2 = 1;
        if (!tags_ok<1>(ov, tag)) {
          if (ODLY2) ov[0] = ov2[0];
          if (!ODLY2 || !tags_ok<1>(ov, tag)) np2 += poll<1>(rw, po, 0, tag, ov);
        }
        if (k == 0) RAW(30, np2);
        if (k == 0) STAMP(7);
        L[lw] = asu(red4(wt * blo(ov[0].x), wt * bhi(ov[0].x), wt * blo(ov[0].z), wt * bhi(ov[0].z)));
        BARRIER();
        if (k == 0) STAMP(25);
        const f32x4 os = (__builtin_bit_cast(f32x4, *(const LAS v4u*)(L + lr)) + __builtin_bit_cast(f32x4, *(const LAS v4u*)(L + lr + 32))) +
                         (__builtin_bit_cast(f32x4, *(const LAS v4u*)(L + lr + 64)) + __builtin_bit_cast(f32x4, *(const LAS v4u*)(L + lr + 96)));
        f32x4 a0 = {0.f, 0.f, 0.f, 0.f}, a1 = {0.f, 0.f, 0.f, 0.f};
#pragma unroll
        for (int i = 0; i < 4; ++i) { const v4u am = amask(pk2(os[i], os[i]), msk); a0 = mfma32(am, wf[2 * i], a0); a1 = mfma32(am, wf[2 * i + 1], a1); }
        const f32x4 s = fold4<false>(a0, a1);
        stsc(rw, sd, pk2(s[0], s[1]), pk2(s[2], s[3]), tv);
        if (ODLY2) asm volatile("" ::"v"(ov2[0]));
        if (k == 0) STAMP(10);
      }
      CFENCE();
      if (isD) {
#pragma unroll
        for (int i = 0; i < WEARLY; ++i) LDW(i);
        BARRIER();
        const f32x4 os = __builtin_bit_cast(f32x4, *(const LAS v4u*)(L + lr));
        f32x4 a0 = {0.f, 0.f, 0.f, 0.f}, a1 = {0.f, 0.f, 0.f, 0.f};
#pragma unroll
        for (int i = 3; i >= 0; --i) { const v4u am = amask(pk2(os[i], os[i]), msk); a0 = mfma32(am, wf[2 * i], a0); a1 = mfma32(am, wf[2 * i + 1], a1); }
        float v0, v1;
        fold2<false>(a0, a1, nlo, v0, v1);
        stsc(rw, sd, asu(v0), asu(v1), tv);
        if (k == 0) STAMP(11);
      }
    } else if (fin) {
      int lw = L_OP + 32 * k + g4 * 8 + c8 + (nlo ? 0 : 256);
      asm volatile("" : "+v"(lw));
      v4u sv[4];
      const unsigned np3 = poll<4>(rw, WS_S + (32 * k + rs) * S_PITCH + fc * 128 + c8 * 16, 8 * S_PITCH, tag, sv); if (k == 0) RAW(31, np3); (void)np3;
      const float s = red4((blo(sv[0].x) + blo(sv[1].x)) + (blo(sv[2].x) + blo(sv[3].x)), (bhi(sv[0].x) + bhi(sv[1].x)) + (bhi(sv[2].x) + bhi(sv[3].x)),
                           (blo(sv[0].z) + blo(sv[1].z)) + (blo(sv[2].z) + blo(sv[3].z)), (bhi(sv[0].z) + bhi(sv[1].z)) + (bhi(sv[2].z) + bhi(sv[3].z)));
      L[lw] = asu(s);
      if (k == 0) STAMP(20); if (k == 1) STAMP(21); if (k == 2) STAMP(22); if (k == 3) STAMP(23);
      BARRIER();
    }
    STAMP_FLUSH();
  }
}

extern "C" void kernel_launch(void* const* d_in, const int* in_sizes, int n_in, void* d_out, int out_size, void* d_ws, size_t ws_size,
                              hipStream_t stream) {
  fused_decode<<<NWG, NTHR, 0, stream>>>(d_in[0], d_in[1], d_in[2], d_in[3], d_in[4], d_in[6], d_in[7], d_in[8], d_out, d_ws);
}
```

```cpp
#define PROBE 8
#define WSOFF ((ws_size - (4u << 20)) & ~(size_t)0x1fffff)
#include <hip/hip_runtime.h>
#include <stdint.h>

#define GAS __attribute__((address_space(1)))
#define LAS __attribute__((address_space(3)))
typedef unsigned v4u __attribute__((ext_vector_type(4)));
typedef unsigned v2u __attribute__((ext_vector_type(2)));
typedef float f32x4 __attribute__((ext_vector_type(4)));
typedef __bf16 bf16x8 __attribute__((ext_vector_type(8)));
typedef __bf16 bf16x2 __attribute__((ext_vector_type(2)));
typedef short s16x4 __attribute__((ext_vector_type(4)));
#define RLX_AGENT __ATOMIC_RELAXED, __HIP_MEMORY_SCOPE_AGENT

#define WINL 6
#define VB1 (10 + VFIRST)
#define VB2 (12 + VFIRST)
#define VB3 (14 + VFIRST)
#define VB4 VB3
#define QPOS VFIRST
#define ABL 0
#define QLAUX 16
#define ROT 1
#define OAUX 16
#define WPRE 6
#define ODLY2 0
#define WEARLY 2
#define KFIRST 1
#define VFIRST 1
#define PTOUCH 0
#define PFMODE 1
#define PFU 120
#define PFOFF 256
#define PFMASK 0x18
#define TOUCH 0
#define VLATE 1
#define WOWIN 7
#define QSAUX 16
#define ODLY 12
#define CODE_BYTES 10496

constexpr int DM = 448, SK = 65536, WIN = 4096;
constexpr int NWG = 256, NTHR = 320;
constexpr int WS_Q = 4096;
constexpr int WS_GU = WS_Q + 8 * 8192;
constexpr int WS_ML = WS_GU + 28 * 2048;
constexpr int O_PITCH = 2176;
constexpr int WS_O = WS_ML + 8192;
constexpr int S_PITCH = 1792;
constexpr int WS_S = WS_O + 256 * O_PITCH;
constexpr int F_PITCH = 3584;
constexpr int WS_F = WS_S + 128 * S_PITCH;
constexpr int WS_END = WS_F + 56 * F_PITCH;
constexpr int OOB = 1 << 30;
constexpr int L_R1 = 0;
constexpr int L_Q = 256;
constexpr int L_P = 512;
constexpr int L_W = 800;
constexpr int L_WM = 832;
constexpr int L_WL = 840;
constexpr int L_FLAG = 928;
constexpr int L_Z = 944;
constexpr int L_OP = 960;
constexpr int L_TOTAL = 21504;
constexpr unsigned SPIN_CAP = 1u << 16;

#define RAW(k, v)
#define STAMP(k) do { if ((k) == PROBE) stp = __builtin_amdgcn_s_memrealtime(); } while (0)
#define STAMP_DECL const unsigned long long st0 = __builtin_amdgcn_s_memrealtime(); unsigned long long stp = 0
#define STAMP_FLUSH() do { if (stp) { const unsigned long long tend = st0 + 3000 + 8 * (stp - st0); while (__builtin_amdgcn_s_memrealtime() < tend) __builtin_amdgcn_s_sleep(2); } } while (0)

__device__ __forceinline__ float asf(unsigned u) { return __builtin_bit_cast(float, u); }
__device__ __forceinline__ unsigned asu(float f) { return __builtin_bit_cast(unsigned, f); }
__device__ __forceinline__ float blo(unsigned u) { return asf(u << 16); }
__device__ __forceinline__ float bhi(unsigned u) { return asf(u & 0xffff0000u); }
__device__ __forceinline__ float rbf(float f) { return (float)(__bf16)f; }
__device__ __forceinline__ unsigned pk2(float lo, float hi) { bf16x2 t = {(__bf16)lo, (__bf16)hi}; return __builtin_bit_cast(unsigned, t); }
__device__ __forceinline__ f32x4 mfma32(v4u a, v4u b, f32x4 c) {
  return __builtin_amdgcn_mfma_f32_16x16x32_bf16(__builtin_bit_cast(bf16x8, a), __builtin_bit_cast(bf16x8, b), c, 0, 0, 0);
}
__device__ __forceinline__ f32x4 mfma16(v2u a, v2u b, f32x4 c) {
  return __builtin_amdgcn_mfma_f32_16x16x16bf16_1k(__builtin_bit_cast(s16x4, a), __builtin_bit_cast(s16x4, b), c, 0, 0, 0);
}
template <int CTRL> __device__ __forceinline__ float dpp(float x) {
  return asf((unsigned)__builtin_amdgcn_mov_dpp((int)asu(x), CTRL, 0xf, 0xf, true));
}
constexpr int XOR1 = 0xB1, XOR2 = 0x4E, XOR7 = 0x141, XOR8 = 0x128;
__device__ __forceinline__ float sw16(float a, float b) { auto s = __builtin_amdgcn_permlane16_swap(asu(a), asu(b), false, false); return asf(s[0]) + asf(s[1]); }
__device__ __forceinline__ float sw32(float a, float b) { auto s = __builtin_amdgcn_permlane32_swap(asu(a), asu(b), false, false); return asf(s[0]) + asf(s[1]); }
__device__ __forceinline__ float x16_max(float x) { auto s = __builtin_amdgcn_permlane16_swap(asu(x), asu(x), false, false); return fmaxf(asf(s[0]), asf(s[1])); }
__device__ __forceinline__ float x32_max(float x) { auto s = __builtin_amdgcn_permlane32_swap(asu(x), asu(x), false, false); return fmaxf(asf(s[0]), asf(s[1])); }
__device__ __forceinline__ float row_sum(float x) { x += dpp<XOR1>(x); x += dpp<XOR2>(x); x += dpp<XOR7>(x); x += dpp<XOR8>(x); return x; }
__device__ __forceinline__ float row_max(float x) { x = fmaxf(x, dpp<XOR1>(x)); x = fmaxf(x, dpp<XOR2>(x)); x = fmaxf(x, dpp<XOR7>(x)); return fmaxf(x, dpp<XOR8>(x)); }
__device__ __forceinline__ float wave_sum(float x) { x = row_sum(x); x = sw16(x, x); return sw32(x, x); }
__device__ __forceinline__ float red4(float v0, float v1, float v2, float v3) { const float t = sw16(sw32(v0, v2), sw32(v1, v3)); return t + dpp<XOR8>(t); }
__device__ __forceinline__ __amdgpu_buffer_rsrc_t mkrs(const GAS char* p, int bytes) { return __builtin_amdgcn_make_buffer_rsrc((void*)p, 0, bytes, 0x00020000); }
__device__ __forceinline__ v4u ldnt(__amdgpu_buffer_rsrc_t rs, int voff, int soff) {
  const v4u r = __builtin_amdgcn_raw_buffer_load_b128(rs, voff, soff, 2);
  __builtin_amdgcn_sched_barrier(0);
  return r;
}
__device__ __forceinline__ v4u ldsc(__amdgpu_buffer_rsrc_t rs, int voff, int soff) { return __builtin_amdgcn_raw_buffer_load_b128(rs, voff, soff, 16); }
template <int AUX = 16> __device__ __forceinline__ void stsc(__amdgpu_buffer_rsrc_t rs, int off, unsigned d0, unsigned d1, unsigned tag) {
  v4u g = {d0, tag, d1, tag};
  __builtin_amdgcn_raw_buffer_store_b128(g, rs, off, 0, AUX);
}
#define BARRIER() asm volatile("s_waitcnt lgkmcnt(0)\n\ts_barrier" ::: "memory")
#define CFENCE() asm volatile("" ::: "memory")
#define VMW(n) asm volatile("s_waitcnt vmcnt(%0)" ::"i"(n) : "memory")
__device__ __forceinline__ v4u amask(unsigned x, const v4u& m) { return v4u{x & m.x, x & m.y, x & m.z, x & m.w}; }
template <bool R16> __device__ __forceinline__ f32x4 fold4(const f32x4& A, const f32x4& B) {
  f32x4 s;
#pragma unroll
  for (int r = 0; r < 4; ++r) {
    auto t = R16 ? __builtin_amdgcn_permlane16_swap(asu(A[r]), asu(B[r]), false, false) : __builtin_amdgcn_permlane32_swap(asu(A[r]), asu(B[r]), false, false);
    s[r] = asf(t[0]) + dpp<XOR8>(asf(t[1]));
  }
  return s;
}
template <bool R16> __device__ __forceinline__ void fold2(const f32x4& A, const f32x4& B, bool nlo, float& v0, float& v1) {
  float s[4];
#pragma unroll
  for (int r = 0; r < 4; ++r) {
    auto t = R16 ? __builtin_amdgcn_permlane16_swap(asu(A[r]), asu(B[r]), false, false) : __builtin_amdgcn_permlane32_swap(asu(A[r]), asu(B[r]), false, false);
    const float a = asf(t[0]), b = asf(t[1]);
    s[r] = r < 2 ? a + dpp<XOR8>(b) : dpp<XOR8>(a) + b;
  }
  v0 = nlo ? s[0] : s[2];
  v1 = nlo ? s[1] : s[3];
}
template <int N> __device__ __forceinline__ bool tags_ok(const v4u (&v)[N], unsigned tag) {
  bool ok = true;
#pragma unroll
  for (int k = 0; k < N; ++k) ok &= (v[k].y == tag) & (v[k].w == tag);
  return __builtin_amdgcn_ballot_w64(!ok) == 0ull;
}
template <int N> __device__ __forceinline__ unsigned poll(__amdgpu_buffer_rsrc_t rs, int off, int stride, unsigned tag, v4u (&v)[N]) {
  unsigned spins = 0;
#pragma unroll 1
  for (; spins < SPIN_CAP; ++spins) {
    CFENCE();
#pragma unroll
    for (int k = 0; k < N; ++k) v[k] = ldsc(rs, off, k * stride);
    if (tags_ok<N>(v, tag)) break;
  }
  return spins + 1;
}
template <bool SLEEP> __device__ __forceinline__ void lds_wait(volatile LAS unsigned* w, unsigned want) {
#pragma unroll 1
  for (unsigned spins = 0; spins < (SPIN_CAP << 6); ++spins) {
    if (__builtin_amdgcn_readfirstlane(*w) >= want) break;
    if (SLEEP) __builtin_amdgcn_s_sleep(1);
  }
  CFENCE();
}
__device__ __forceinline__ float silu(float x) { return x * __builtin_amdgcn_rcpf(1.f + __expf(-x)); }

typedef unsigned long long v2q __attribute__((ext_vector_type(2)));
typedef unsigned long long v4q __attribute__((ext_vector_type(4)));
__global__ void __launch_bounds__(NTHR) fused_decode(const void* x_, const void* kc_, const void* vc_, const void* wq_, const void* wo_, const void* wg_,
                                                     const void* wu_, const void* wd_, void* out_, void* ws_) {
  v4q ka, kb; v2q kd; unsigned long long pc;
  int b = blockIdx.x, tid = threadIdx.x;
  asm volatile("s_load_dwordx8 %0, %5, 0x0\n\ts_load_dwordx8 %1, %5, 0x20\n\ts_load_dwordx4 %2, %5, 0x40\n\ts_getpc_b64 %3"
               : "=&s"(ka), "=&s"(kb), "=&s"(kd), "=&s"(pc), "+s"(b) : "s"(__builtin_amdgcn_kernarg_segment_ptr()));
  asm volatile("" : "+v"(tid));
  __builtin_amdgcn_sched_barrier(0);
  __shared__ __attribute__((aligned(16))) unsigned lds_[L_TOTAL];
  LAS unsigned* L = (LAS unsigned*)lds_;
  volatile LAS unsigned* FL = (volatile LAS unsigned*)(L + L_FLAG);
  const int w = __builtin_amdgcn_readfirstlane(tid >> 6);
  STAMP_DECL;
  if (w == 0) STAMP(5);
  unsigned cpf = 0, cpf2 = 0;
  if (w == 4 && (b & PFMASK) == PFMASK) {
    const __amdgpu_buffer_rsrc_t rc = mkrs((const GAS char*)(pc & ~255ull), CODE_BYTES);
    cpf = __builtin_amdgcn_raw_buffer_load_b32(rc, (tid & 63) * 128, PFOFF, 0);
    cpf2 = __builtin_amdgcn_raw_buffer_load_b32(rc, (tid & 63) * 128, PFOFF + 8192, 0);
  }
  const bool e0 = (b & 64) == 0, q0 = b < 128;
  const int t0 = b & 63;
  const int NI = e0 ? 6 : 8;
  const int r0 = (e0 ? 256 : 0) + 8 * NI * w;
  const int pitch = q0 ? 8192 : 3584;
  const int sb = r0 * pitch + ((q0 || t0 < 28) ? t0 : t0 - 28) * 128;
  const int nb1 = (q0 || t0 < 56) ? DM * pitch : 0;
  const int sa = 2 * ((tid >> 4) & 3) + ((tid >> 2) & 1);
  const int fo = ((tid >> 3) & 7) * (NI * pitch) + (tid & 7) * 16, xo = (tid & 63) * 16, xso = sa * (NI * 2);
  asm volatile("" ::"s"(r0 * 2), "s"(pitch), "s"(sb), "s"(nb1), "v"(fo), "v"(xo), "v"(xso));
  __builtin_amdgcn_sched_barrier(0);
  if (w == 0) STAMP(27);
  asm volatile("s_waitcnt lgkmcnt(0)" : "+s"(ka), "+s"(kb), "+s"(kd));
  if (w == 0) STAMP(26);
  const GAS char *x = (const GAS char*)ka.x, *kc = (const GAS char*)ka.y, *vc = (const GAS char*)ka.z, *wq = (const GAS char*)ka.w;
  const GAS char *wo = (const GAS char*)kb.x, *wg = (const GAS char*)kb.y, *wu = (const GAS char*)kb.z, *wd = (const GAS char*)kb.w;
  GAS char *out = (GAS char*)kd.x, *ws = (GAS char*)kd.y;
  const __amdgpu_buffer_rsrc_t rx = mkrs(x, 896);
  typedef const __attribute__((address_space(4))) unsigned* cptr;
  unsigned ep, tk, tv_;
  asm volatile("s_cmp_lg_u32 %1, 4\n\ts_cbranch_scc1 1f\n\ts_load_dword %0, %2, 0x0\n1:" : "=&s"(ep) : "s"(w), "s"(ws) : "scc");
  tk = tv_ = 0;
  constexpr float C2 = 0.0625f * 1.4426950408889634f;
#define IDS() \
  asm volatile("" : "+s"(b)); \
  asm volatile("" : "+v"(tid)); \
  __builtin_amdgcn_sched_barrier(0); \
  const int h = (b >> 3) & 7, j = (b & 7) * 4 + (b >> 6); \
  const int lane = tid & 63; \
  const int u = (b & 63) + 64 * (b >> 7); \
  const bool isE = (j & 1) == 0; \
  const int ei = j >> 1; \
  const bool isD = !isE && u < 56; \
  const bool fin = !isE && u >= 114; \
  const int fc = u - 114; \
  const int rs = lane >> 3, c8 = lane & 7, g4 = lane >> 4, n16 = lane & 15; \
  const bool nlo = n16 < 8; \
  const int gidx = 4 * c8 + 2 * (g4 & 1) + (n16 >> 3); \
  const __amdgpu_buffer_rsrc_t rw = mkrs(ws, WS_END); \
  (void)rs; (void)gidx

  if (w == 4) {
    IDS();
    STAMP(0);
    __builtin_amdgcn_s_setprio(3);
    asm volatile("s_barrier\n\ts_waitcnt lgkmcnt(0)" : "+s"(ep) : "s"(tk), "s"(tv_) : "memory");
    const unsigned tag = ep + 1u;
    if (lane == 0) *(volatile LAS v2u*)(L + L_FLAG + 2) = v2u{tag, 1u};
    if (u < 120) {
      const v2u r0 = *(const LAS v2u*)(L + L_R1 + 2 * (lane & 31)), r1 = *(const LAS v2u*)(L + L_R1 + 64 + 2 * (lane & 31)),
                r2 = *(const LAS v2u*)(L + L_R1 + 128 + 2 * (lane & 31)), r3 = *(const LAS v2u*)(L + L_R1 + 192 + 2 * (lane & 31));
      const float v0 = (asf(r0.x) + asf(r1.x)) + (asf(r2.x) + asf(r3.x)), v1 = (asf(r0.y) + asf(r1.y)) + (asf(r2.y) + asf(r3.y));
      const int hf = isE ? 1 : 0;
      const int dst = u < 64 ? WS_Q + (u >> 3) * 8192 + hf * 4096 + (u & 7) * 512 : u < 92 ? WS_GU + (u - 64) * 2048 + hf * 512 : WS_GU + (u - 92) * 2048 + 1024 + hf * 512;
      if (lane < 32) stsc<QSAUX>(rw, dst + lane * 16, asu(v0), asu(v1), tag);
    }
    STAMP(2);
    unsigned pt0 = 0, pt1 = 0;
    if (PTOUCH) { pt0 = *(cptr)(unsigned long long)(wd + (u < 56 ? u : 0) * 32 * 896); pt1 = *(cptr)(unsigned long long)out; }
    const unsigned xw = __builtin_amdgcn_raw_buffer_load_b16(rx, (4 * c8 + g4) * 2, fin ? fc * 64 : 0, 0);
    const int go = WS_GU + (u >> 1) * 2048 + g4 * 512 + (u & 1) * 256 + n16 * 16;
    v4u gu[1];
    lds_wait<true>(FL + 5, 1u);
    gu[0] = ldsc(rw, go, isD ? 0 : OOB);
    asm volatile("" ::"s"(pt0), "s"(pt1));
    asm volatile("" ::"v"(cpf), "v"(cpf2));
    BARRIER();
    BARRIER();
    BARRIER();
    if (lane < 2) {
      const float M = fmaxf(fmaxf(asf(L[L_WM + lane]), asf(L[L_WM + 2 + lane])), fmaxf(asf(L[L_WM + 4 + lane]), asf(L[L_WM + 6 + lane])));
      const float Ls = (asf(L[L_WL + lane]) + asf(L[L_WL + 2 + lane])) + (asf(L[L_WL + 4 + lane]) + asf(L[L_WL + 6 + lane]));
      stsc(rw, WS_ML + (h * 2 + lane) * 512 + j * 16, asu(M), asu(Ls), tag);
    }
    STAMP(4);
    if (isE) {
      lds_wait<true>(FL + 8, 1u);
      v4u ml[1];
      const unsigned np1 = poll<1>(rw, WS_ML + (h * 2 + (ei >> 3)) * 512 + (lane & 31) * 16, 0, tag, ml); RAW(29, np1); (void)np1;
      const float m = asf(ml[0].x);
      const float M = x16_max(row_max(m));
      const float e = __builtin_amdgcn_exp2f((m - M) * C2);
      float Ls = row_sum(e * asf(ml[0].z)); Ls = sw16(Ls, Ls);
      if (lane < 32) L[L_W + lane] = asu(e * __builtin_amdgcn_rcpf(Ls));
      if (lane == 0) FL[7] = 1u;
      STAMP(6);
    } else if (isD) {
      if (!tags_ok<1>(gu, tag)) poll<1>(rw, go, 0, tag, gu);
      const float y = rbf(sw16(asf(gu[0].x), asf(gu[0].z)));
      auto t = __builtin_amdgcn_permlane32_swap(asu(y), asu(y), false, false);
      const float hv = rbf(rbf(silu(asf(t[0]))) * asf(t[1]));
      const int r = 2 * n16 + (g4 & 1);
      if (lane < 32) L[L_OP + (r & 7) * 4 + (r >> 3)] = asu(hv);
      STAMP(6);
      BARRIER();
    } else if (fin) {
      lds_wait<true>(FL + 8, 1u);
      v4u fv[14];
      poll<14>(rw, WS_F + g4 * F_PITCH + fc * 256 + n16 * 16, 4 * F_PITCH, tag, fv);
      float f0 = 0.f, f1 = 0.f;
#pragma unroll
      for (int i = 0; i < 14; ++i) { f0 += asf(fv[i].x); f1 += asf(fv[i].z); }
      float f = sw16(f0, f1); f = sw32(f, f);
      const float g = rbf(silu(rbf(f)));
      if (lane < 32) L[L_W + 2 * n16 + (g4 & 1)] = asu(g);
      asm volatile("s_waitcnt lgkmcnt(0)" ::: "memory");
      const float gc = asf(L[L_W + 4 * c8 + g4]);
      int so = L_OP + g4 * 8 + c8, oa = (4 * c8 + g4) * 2 + (nlo ? 0 : OOB);
      float xf = blo(xw);
      asm volatile("" : "+v"(so), "+v"(oa), "+v"(xf));
      STAMP(16);
      BARRIER();
      const float s = rbf((asf(L[so]) + asf(L[so + 32])) + (asf(L[so + 64]) + asf(L[so + 96])));
      const float res = xf + rbf(s * gc);
      __builtin_amdgcn_raw_buffer_store_b16((unsigned short)pk2(res, res), mkrs(out, 896), oa, fc * 64, OAUX);
      if (fc == 0 && lane == 0) __hip_atomic_store((GAS unsigned*)ws, tag, RLX_AGENT);
      STAMP(8);
    }
    STAMP_FLUSH();
  } else {
    const v4u xr = __builtin_amdgcn_raw_buffer_load_b128(rx, xo, 0, 0);
    const v4u xs = __builtin_amdgcn_raw_buffer_load_b128(rx, xso, r0 * 2, 0);
    __builtin_amdgcn_sched_barrier(0);
    if (w == 0) STAMP(1);
    v4u fr[8];
    const GAS char* w1 = q0 ? wq : t0 < 28 ? wg : wu;
    {
      const __amdgpu_buffer_rsrc_t ra = mkrs(w1, nb1);
#pragma unroll
      for (int i = 0; i < 6; ++i) fr[i] = ldnt(ra, fo, sb + i * pitch);
    }
    IDS();
    const int k = w;
    const unsigned hm = (lane & 1) ? 0xffff0000u : 0x0000ffffu;
    v4u kf[16], vf[16], wf[8];
    if (tid < 20) FL[tid] = 0u;
    const int wre = 512 * h + 32 * ei, wrd = u < 56 ? 32 * u : 0;
    const int wrow = (wrd + ((wre - wrd) & ((j & 1) - 1))) * 896;
    const int oo = rs * 896 + c8 * 16;
    const v2u msk2 = {(lane & 2) ? 0u : hm, (lane & 2) ? hm : 0u};
    const int cA = 2 * k, sA = cA * 128, sB = k < 3 ? sA + 128 : OOB;
    const __amdgpu_buffer_rsrc_t ro = mkrs((isE ? wo : wd) + wrow, (isE || isD) ? 32 * 896 : 0);
#define LDW(i) wf[i] = ldnt(ro, oo, ((i) >> 1) * 8 * 896 + (((i) & 1) ? sB : sA))
    {
      const __amdgpu_buffer_rsrc_t r67 = mkrs(isE ? wo + wrow : w1, isE ? 32 * 896 : nb1);
      const int o67 = isE ? oo : fo;
      fr[6] = ldnt(r67, o67, isE ? sA : sb + 6 * pitch);
      fr[7] = ldnt(r67, o67, isE ? sB : sb + 7 * pitch);
      wf[0] = fr[6]; wf[1] = fr[7];
    }
    const int kvo = (h * SK + (SK - WIN) + j * 128) * 512;
    const int rot = ((b + (b >> 3) + (b >> 6)) & 7) * 8192;
    const int ko = k * 2048 + (n16 >> 2) * 512 + g4 * 64 + (lane & 3) * 16;
    const int vo = rs * 512 + c8 * 16 + k * 128;
    const __amdgpu_buffer_rsrc_t rk = mkrs(kc, 8 * SK * 512), rv = mkrs(vc, 8 * SK * 512);
#define LDK(i) kf[i] = ldnt(rk, ko + ((i) & 1) * 256, kvo + ((((i) >> 1) * 8192 + rot) & 0xffff))
#define LDV(i) vf[i] = ldnt(rv, vo, kvo + (((i) * 4096 + rot) & 0xffff))
    if (KFIRST) LDK(0);
    if (VFIRST && !VLATE) LDV(0);
#define LDS_(n) do { if ((n) < 16) LDV((n) < 16 ? (n) : 0); else LDW((n) >= 16 ? (n) - 16 + WEARLY : 2); } while (0)
    if (k == 0) STAMP(18);
    {
      float xv[8];
#pragma unroll
      for (int e = 0; e < 4; ++e) { xv[2 * e] = blo(xr[e]); xv[2 * e + 1] = bhi(xr[e]); }
      const float s1 = ((xv[0] + xv[1]) + (xv[2] + xv[3])) + ((xv[4] + xv[5]) + (xv[6] + xv[7]));
      const float mean = wave_sum(s1) * (1.f / DM);
      float s2 = 0.f;
#pragma unroll
      for (int e = 0; e < 8; ++e) { const float d = xv[e] - mean; s2 += d * d; }
      s2 = lane < 56 ? s2 : 0.f;
      const float var = wave_sum(s2) * (1.f / DM);
      const float mb = rbf(mean), rden = __builtin_amdgcn_rcpf(rbf(__builtin_amdgcn_sqrtf(rbf(rbf(var) + 1e-5f))));
      unsigned xc[8];
#pragma unroll
      for (int i = 0; i < 8; ++i) {
        const float xn = rbf(((i & 1) ? bhi(xs[i >> 1]) : blo(xs[i >> 1])) - mb) * rden;
        xc[i] = (i >= 6 && isE) ? 0u : pk2(xn, xn);
      }
      if (k == 0) STAMP(19);
      __builtin_amdgcn_sched_barrier(0);
      f32x4 a0 = {0.f, 0.f, 0.f, 0.f}, a1 = {0.f, 0.f, 0.f, 0.f};
#pragma unroll
      for (int i = 0; i < 8; ++i) {
        const v2u am = {xc[i] & msk2.x, xc[i] & msk2.y};
        a0 = mfma16(am, v2u{fr[i].x, fr[i].y}, a0);
        a1 = mfma16(am, v2u{fr[i].z, fr[i].w}, a1);
        __builtin_amdgcn_sched_barrier(0);
        if (i >= 8 - WINL) LDK(KFIRST + i - (8 - WINL));
      }
      float v0, v1;
      fold2<true>(a0, a1, nlo, v0, v1);
      if (lane < 32) *(LAS v2u*)(L + L_R1 + 64 * k + 2 * gidx) = v2u{asu(v0), asu(v1)};
      BARRIER();
      if (k == 0) STAMP(12);
    }
    if (VFIRST && VLATE) { VMW(WINL - 1); LDV(0); }
#pragma unroll
    for (int i = KFIRST + WINL; i < 16; ++i) { VMW(WINL - 1); LDK(i); }
#pragma unroll
    for (int i = VFIRST; i < QPOS; ++i) { VMW(WINL - 1); LDV(i); }
    unsigned tag;
    v4u q2[2];
    {
      const int qo = WS_Q + h * 8192 + k * 1024 + lane * 16;
      v4u qv[2];
      qv[0] = __builtin_amdgcn_raw_buffer_load_b128(rw, qo, (ABL & 1) ? OOB : 0, QLAUX);
      qv[1] = __builtin_amdgcn_raw_buffer_load_b128(rw, qo, (ABL & 3) ? OOB : 4096, QLAUX);
      __builtin_amdgcn_sched_barrier(0);
      if (lane == 0) FL[5] = 1u;
      if (k == 0) STAMP(17);
#pragma unroll
      for (int i = QPOS; i < QPOS + WINL; ++i) { VMW(WINL + 1); LDV(i); }
      lds_wait<false>(FL + 3, 1u);
      tag = __builtin_amdgcn_readfirstlane(FL[2]);
      VMW(WINL);
      const bool qok = (ABL & 7) ? (qv[0].y != 12345u || tag != 77u) : tags_ok<2>(qv, tag);
      q2[0] = ldsc(rw, qo, qok ? OOB : 0);
      q2[1] = ldsc(rw, qo, qok ? OOB : 4096);
      __builtin_amdgcn_sched_barrier(0);
#pragma unroll
      for (int i = QPOS + WINL; i < VB1; ++i) LDS_(i);
      if (k == 0) STAMP(13);
      unsigned np = 1;
      if (!qok) {
        qv[0] = q2[0]; qv[1] = q2[1]; ++np;
        if (!tags_ok<2>(qv, tag)) np += poll<2>(rw, qo, 4096, tag, qv);
      }
      if (k == 0) RAW(28, np);
      if (k == 0) STAMP(3);
      L[L_Q + 64 * k + lane] = pk2(asf(qv[0].x) + asf(qv[1].x), asf(qv[0].z) + asf(qv[1].z));
    }
    BARRIER();
#pragma unroll
    for (int i = VB1; i < VB2; ++i) LDS_(i);
    if (k == 0) STAMP(24);
    v4u q[4];
#pragma unroll
    for (int t = 0; t < 4; ++t) {
      const int qa = L_Q + ((lane >> 2) & 1) * 128 + (t & 1) * 64 + g4 * 16 + (lane & 3) * 4;
      q[t] = *(const LAS v4u*)(L + ((n16 >> 3) == (t >> 1) ? qa : L_Z));
    }
    float y[4];
#pragma unroll
    for (int t = 0; t < 4; ++t) {
      f32x4 d = {0.f, 0.f, 0.f, 0.f};
      d = mfma32(q[0], kf[2 * t], d); d = mfma32(q[1], kf[2 * t + 1], d);
      d = mfma32(q[2], kf[2 * t + 8], d); d = mfma32(q[3], kf[2 * t + 9], d);
      const float lo = (lane & 1) ? d[1] : d[0], hi = (lane & 1) ? d[3] : d[2];
      y[t] = (lane & 2) ? hi : lo;
    }
    {
      const float k0 = (lane & 2) ? y[2] : y[0], s0 = (lane & 2) ? y[0] : y[2], k1 = (lane & 2) ? y[3] : y[1], s1 = (lane & 2) ? y[1] : y[3];
      y[0] = k0 + dpp<XOR2>(s0); y[1] = k1 + dpp<XOR2>(s1);
    }
    const float sc = rbf(((lane & 1) ? y[1] : y[0]) + dpp<XOR1>((lane & 1) ? y[0] : y[1]));
    const float m = x32_max(row_max(sc));
    const int a = g4 & 1;
    if ((lane & 0x2f) == 0) L[L_WM + 2 * k + a] = asu(m);
    BARRIER();
#pragma unroll
    for (int i = VB2; i < VB3; ++i) LDS_(i);
    const float M = fmaxf(fmaxf(asf(L[L_WM + a]), asf(L[L_WM + 2 + a])), fmaxf(asf(L[L_WM + 4 + a]), asf(L[L_WM + 6 + a])));
    const float p = __builtin_amdgcn_exp2f((sc - M) * C2);
    L[L_P + a * 128 + (4 * (k & 1) + (n16 >> 2)) * 16 + 2 * ((lane & 3) + 4 * (g4 >> 1)) + (k >> 1)] = pk2(p, p);
    float l = row_sum(p); l = sw32(l, l);
    if ((lane & 0x2f) == 0) L[L_WL + 2 * k + a] = asu(l);
    BARRIER();
#pragma unroll
    for (int i = VB3; i < VB4; ++i) LDS_(i);
    if (k == 0) STAMP(9);
    {
      v4u pa[4];
#pragma unroll
      for (int t = 0; t < 4; ++t) pa[t] = *(const LAS v4u*)(L + L_P + (n16 >> 3) * 128 + sa * 16 + 4 * t);
      int od = WS_O + (h + 8 * j) * O_PITCH + (lane >> 5) * 1024 + k * 256 + (2 * c8 + (g4 & 1)) * 16 + (nlo ? 0 : OOB);
      asm volatile("" : "+v"(od));
      f32x4 a0 = {0.f, 0.f, 0.f, 0.f}, a1 = {0.f, 0.f, 0.f, 0.f};
#pragma unroll
      for (int i = 0; i < 16; ++i) {
        const unsigned c = pa[i >> 2][i & 3];
        const v2u am = {c & msk2.x, c & msk2.y};
        a0 = mfma16(am, v2u{vf[i].x, vf[i].y}, a0);
        a1 = mfma16(am, v2u{vf[i].z, vf[i].w}, a1);
        __builtin_amdgcn_sched_barrier(0);
        if (i == QPOS + WINL) asm volatile("" ::"v"(q2[0]), "v"(q2[1]));
        {
          const int nx = i + WOWIN;
          if (nx >= VB4 && nx < 16 + WPRE && nx < 24 - WEARLY && i < 15) LDS_(nx);
        }
      }
      const f32x4 o = fold4<true>(a0, a1);
      stsc(rw, od, pk2(o[0], o[1]), pk2(o[2], o[3]), tag);
      __builtin_amdgcn_sched_barrier(0);
    }
    if (lane == 0) FL[8] = 1u;
    if (k == 3) STAMP(14);
    if (k == 0) STAMP(15);
#pragma unroll
    for (int i = 16 + (WPRE < WOWIN - 1 ? WPRE : WOWIN - 1); i < 24 - WEARLY; ++i) LDS_(i);
    if (isE || isD) {
      v4u msk = {(c8 >> 1) == 0 ? hm : 0u, (c8 >> 1) == 1 ? hm : 0u, (c8 >> 1) == 2 ? hm : 0u, (c8 >> 1) == 3 ? hm : 0u};
      const int dead = (k < 3 || lane < 32) ? 0 : OOB;
      int sd = isE ? WS_S + (h * 16 + ei) * S_PITCH + (cA + (lane >> 5)) * 256 + (2 * c8 + (g4 & 1)) * 16 + (nlo ? dead : OOB)
                   : WS_F + u * F_PITCH + (cA + (lane >> 5)) * 512 + gidx * 16 + dead;
      int lw = L_OP + k * 32 + (4 * (c8 & 1) + g4) * 4 + (c8 >> 1) + (nlo ? 0 : 256), lr = L_OP + rs * 4;
      unsigned tv = tag;
      asm volatile("" : "+v"(msk), "+v"(sd), "+v"(lw), "+v"(lr), "+v"(tv));
      if (isE) {
        if (ODLY) __builtin_amdgcn_s_sleep(ODLY);
        v4u ov[1];
        const int po = WS_O + (h + 8 * (8 * k + rs)) * O_PITCH + ei * 128 + c8 * 16;
        ov[0] = ldsc(rw, po, 0);
        __builtin_amdgcn_sched_barrier(0);
        v4u ov2[1];
        if (ODLY2) { __builtin_amdgcn_s_sleep(ODLY2); CFENCE(); ov2[0] = ldsc(rw, po, 0); __builtin_amdgcn_sched_barrier(0); }
        lds_wait<false>(FL + 7, 1u);
        const float wt = asf(L[L_W + 8 * k + rs]);
        asm volatile("" ::"v"(wt));
        unsigned np2 = 1;
        if (!tags_ok<1>(ov, tag)) {
          if (ODLY2) ov[0] = ov2[0];
          if (!ODLY2 || !tags_ok<1>(ov, tag)) np2 += poll<1>(rw, po, 0, tag, ov);
        }
        if (k == 0) RAW(30, np2);
        if (k == 0) STAMP(7);
        L[lw] = asu(red4(wt * blo(ov[0].x), wt * bhi(ov[0].x), wt * blo(ov[0].z), wt * bhi(ov[0].z)));
        BARRIER();
        if (k == 0) STAMP(25);
        const f32x4 os = (__builtin_bit_cast(f32x4, *(const LAS v4u*)(L + lr)) + __builtin_bit_cast(f32x4, *(const LAS v4u*)(L + lr + 32))) +
                         (__builtin_bit_cast(f32x4, *(const LAS v4u*)(L + lr + 64)) + __builtin_bit_cast(f32x4, *(const LAS v4u*)(L + lr + 96)));
        f32x4 a0 = {0.f, 0.f, 0.f, 0.f}, a1 = {0.f, 0.f, 0.f, 0.f};
#pragma unroll
        for (int i = 0; i < 4; ++i) { const v4u am = amask(pk2(os[i], os[i]), msk); a0 = mfma32(am, wf[2 * i], a0); a1 = mfma32(am, wf[2 * i + 1], a1); }
        const f32x4 s = fold4<false>(a0, a1);
        stsc(rw, sd, pk2(s[0], s[1]), pk2(s[2], s[3]), tv);
        if (ODLY2) asm volatile("" ::"v"(ov2[0]));
        if (k == 0) STAMP(10);
      }
      CFENCE();
      if (isD) {
#pragma unroll
        for (int i = 0; i < WEARLY; ++i) LDW(i);
        BARRIER();
        const f32x4 os = __builtin_bit_cast(f32x4, *(const LAS v4u*)(L + lr));
        f32x4 a0 = {0.f, 0.f, 0.f, 0.f}, a1 = {0.f, 0.f, 0.f, 0.f};
#pragma unroll
        for (int i = 3; i >= 0; --i) { const v4u am = amask(pk2(os[i], os[i]), msk); a0 = mfma32(am, wf[2 * i], a0); a1 = mfma32(am, wf[2 * i + 1], a1); }
        float v0, v1;
        fold2<false>(a0, a1, nlo, v0, v1);
        stsc(rw, sd, asu(v0), asu(v1), tv);
        if (k == 0) STAMP(11);
      }
    } else if (fin) {
      int lw = L_OP + 32 * k + g4 * 8 + c8 + (nlo ? 0 : 256);
      asm volatile("" : "+v"(lw));
      v4u sv[4];
      const unsigned np3 = poll<4>(rw, WS_S + (32 * k + rs) * S_PITCH + fc * 128 + c8 * 16, 8 * S_PITCH, tag, sv); if (k == 0) RAW(31, np3); (void)np3;
      const float s = red4((blo(sv[0].x) + blo(sv[1].x)) + (blo(sv[2].x) + blo(sv[3].x)), (bhi(sv[0].x) + bhi(sv[1].x)) + (bhi(sv[2].x) + bhi(sv[3].x)),
                           (blo(sv[0].z) + blo(sv[1].z)) + (blo(sv[2].z) + blo(sv[3].z)), (bhi(sv[0].z) + bhi(sv[1].z)) + (bhi(sv[2].z) + bhi(sv[3].z)));
      L[lw] = asu(s);
      if (k == 0) STAMP(20); if (k == 1) STAMP(21); if (k == 2) STAMP(22); if (k == 3) STAMP(23);
      BARRIER();
    }
    STAMP_FLUSH();
  }
}

extern "C" void kernel_launch(void* const* d_in, const int* in_sizes, int n_in, void* d_out, int out_size, void* d_ws, size_t ws_size,
                              hipStream_t stream) {
  fused_decode<<<NWG, NTHR, 0, stream>>>(d_in[0], d_in[1], d_in[2], d_in[3], d_in[4], d_in[6], d_in[7], d_in[8], d_out, (char*)d_ws + WSOFF);
}
```

```cpp
#include <hip/hip_runtime.h>
#include <stdint.h>

#define GAS __attribute__((address_space(1)))
#define LAS __attribute__((address_space(3)))
typedef unsigned v4u __attribute__((ext_vector_type(4)));
typedef unsigned v2u __attribute__((ext_vector_type(2)));
typedef float f32x4 __attribute__((ext_vector_type(4)));
typedef __bf16 bf16x8 __attribute__((ext_vector_type(8)));
typedef __bf16 bf16x2 __attribute__((ext_vector_type(2)));
typedef short s16x4 __attribute__((ext_vector_type(4)));
#define RLX_AGENT __ATOMIC_RELAXED, __HIP_MEMORY_SCOPE_AGENT

#define WINL 6
#define VB1 10
#define VB2 12
#define VB3 14
#define VB4 VB3
#define QPOS 0
#define ABL 0
#define QLAUX 16
#define ROT 1
#define OAUX 16
#define WPRE 6
#define ODLY2 0
#define WEARLY 2
#define WOWIN 7
#define QSAUX 16
#define ODLY 12
#define CODE_BYTES 9728

constexpr int DM = 448, SK = 65536, WIN = 4096;
constexpr int NWG = 256, NTHR = 320;
constexpr int WS_Q = 4096;
constexpr int WS_GU = WS_Q + 8 * 8192;
constexpr int WS_ML = WS_GU + 28 * 2048;
constexpr int O_PITCH = 2176;
constexpr int WS_O = WS_ML + 8192;
constexpr int S_PITCH = 1792;
constexpr int WS_S = WS_O + 256 * O_PITCH;
constexpr int F_PITCH = 3584;
constexpr int WS_F = WS_S + 128 * S_PITCH;
constexpr int WS_END = WS_F + 56 * F_PITCH;
constexpr int OOB = 1 << 30;
constexpr int L_R1 = 0;
constexpr int L_Q = 256;
constexpr int L_P = 512;
constexpr int L_W = 800;
constexpr int L_WM = 832;
constexpr int L_WL = 840;
constexpr int L_FLAG = 928;
constexpr int L_Z = 944;
constexpr int L_OP = 960;
constexpr int L_TOTAL = 21504;
constexpr unsigned SPIN_CAP = 1u << 16;

#define RAW(k, v)
#define STAMP(k)
#define STAMP_DECL
#define STAMP_FLUSH()

__device__ __forceinline__ float asf(unsigned u) { return __builtin_bit_cast(float, u); }
__device__ __forceinline__ unsigned asu(float f) { return __builtin_bit_cast(unsigned, f); }
__device__ __forceinline__ float blo(unsigned u) { return asf(u << 16); }
__device__ __forceinline__ float bhi(unsigned u) { return asf(u & 0xffff0000u); }
__device__ __forceinline__ float rbf(float f) { return (float)(__bf16)f; }
__device__ __forceinline__ unsigned pk2(float lo, float hi) { bf16x2 t = {(__bf16)lo, (__bf16)hi}; return __builtin_bit_cast(unsigned, t); }
__device__ __forceinline__ f32x4 mfma32(v4u a, v4u b, f32x4 c) {
  return __builtin_amdgcn_mfma_f32_16x16x32_bf16(__builtin_bit_cast(bf16x8, a), __builtin_bit_cast(bf16x8, b), c, 0, 0, 0);
}
__device__ __forceinline__ f32x4 mfma16(v2u a, v2u b, f32x4 c) {
  return __builtin_amdgcn_mfma_f32_16x16x16bf16_1k(__builtin_bit_cast(s16x4, a), __builtin_bit_cast(s16x4, b), c, 0, 0, 0);
}
template <int CTRL> __device__ __forceinline__ float dpp(float x) {
  return asf((unsigned)__builtin_amdgcn_mov_dpp((int)asu(x), CTRL, 0xf, 0xf, true));
}
constexpr int XOR1 = 0xB1, XOR2 = 0x4E, XOR7 = 0x141, XOR8 = 0x128;
__device__ __forceinline__ float sw16(float a, float b) { auto s = __builtin_amdgcn_permlane16_swap(asu(a), asu(b), false, false); return asf(s[0]) + asf(s[1]); }
__device__ __forceinline__ float sw32(float a, float b) { auto s = __builtin_amdgcn_permlane32_swap(asu(a), asu(b), false, false); return asf(s[0]) + asf(s[1]); }
__device__ __forceinline__ float x16_max(float x) { auto s = __builtin_amdgcn_permlane16_swap(asu(x), asu(x), false, false); return fmaxf(asf(s[0]), asf(s[1])); }
__device__ __forceinline__ float x32_max(float x) { auto s = __builtin_amdgcn_permlane32_swap(asu(x), asu(x), false, false); return fmaxf(asf(s[0]), asf(s[1])); }
__device__ __forceinline__ float row_sum(float x) { x += dpp<XOR1>(x); x += dpp<XOR2>(x); x += dpp<XOR7>(x); x += dpp<XOR8>(x); return x; }
__device__ __forceinline__ float row_max(float x) { x = fmaxf(x, dpp<XOR1>(x)); x = fmaxf(x, dpp<XOR2>(x)); x = fmaxf(x, dpp<XOR7>(x)); return fmaxf(x, dpp<XOR8>(x)); }
__device__ __forceinline__ float wave_sum(float x) { x = row_sum(x); x = sw16(x, x); return sw32(x, x); }
__device__ __forceinline__ float red4(float v0, float v1, float v2, float v3) { const float t = sw16(sw32(v0, v2), sw32(v1, v3)); return t + dpp<XOR8>(t); }
__device__ __forceinline__ __amdgpu_buffer_rsrc_t mkrs(const GAS char* p, int bytes) { return __builtin_amdgcn_make_buffer_rsrc((void*)p, 0, bytes, 0x00020000); }
__device__ __forceinline__ v4u ldnt(__amdgpu_buffer_rsrc_t rs, int voff, int soff) {
  const v4u r = __builtin_amdgcn_raw_buffer_load_b128(rs, voff, soff, 2);
  __builtin_amdgcn_sched_barrier(0);
  return r;
}
__device__ __forceinline__ v4u ldsc(__amdgpu_buffer_rsrc_t rs, int voff, int soff) { return __builtin_amdgcn_raw_buffer_load_b128(rs, voff, soff, 16); }
template <int AUX = 16> __device__ __forceinline__ void stsc(__amdgpu_buffer_rsrc_t rs, int off, unsigned d0, unsigned d1, unsigned tag) {
  v4u g = {d0, tag, d1, tag};
  __builtin_amdgcn_raw_buffer_store_b128(g, rs, off, 0, AUX);
}
#define BARRIER() asm volatile("s_waitcnt lgkmcnt(0)\n\ts_barrier" ::: "memory")
#define CFENCE() asm volatile("" ::: "memory")
#define VMW(n) asm volatile("s_waitcnt vmcnt(%0)" ::"i"(n) : "memory")
__device__ __forceinline__ v4u amask(unsigned x, const v4u& m) { return v4u{x & m.x, x & m.y, x & m.z, x & m.w}; }
template <bool R16> __device__ __forceinline__ f32x4 fold4(const f32x4& A, const f32x4& B) {
  f32x4 s;
#pragma unroll
  for (int r = 0; r < 4; ++r) {
    auto t = R16 ? __builtin_amdgcn_permlane16_swap(asu(A[r]), asu(B[r]), false, false) : __builtin_amdgcn_permlane32_swap(asu(A[r]), asu(B[r]), false, false);
    s[r] = asf(t[0]) + dpp<XOR8>(asf(t[1]));
  }
  return s;
}
template <bool R16> __device__ __forceinline__ void fold2(const f32x4& A, const f32x4& B, bool nlo, float& v0, float& v1) {
  float s[4];
#pragma unroll
  for (int r = 0; r < 4; ++r) {
    auto t = R16 ? __builtin_amdgcn_permlane16_swap(asu(A[r]), asu(B[r]), false, false) : __builtin_amdgcn_permlane32_swap(asu(A[r]), asu(B[r]), false, false);
    const float a = asf(t[0]), b = asf(t[1]);
    s[r] = r < 2 ? a + dpp<XOR8>(b) : dpp<XOR8>(a) + b;
  }
  v0 = nlo ? s[0] : s[2];
  v1 = nlo ? s[1] : s[3];
}
template <int N> __device__ __forceinline__ bool tags_ok(const v4u (&v)[N], unsigned tag) {
  bool ok = true;
#pragma unroll
  for (int k = 0; k < N; ++k) ok &= (v[k].y == tag) & (v[k].w == tag);
  return __builtin_amdgcn_ballot_w64(!ok) == 0ull;
}
template <int N> __device__ __forceinline__ unsigned poll(__amdgpu_buffer_rsrc_t rs, int off, int stride, unsigned tag, v4u (&v)[N]) {
  unsigned spins = 0;
#pragma unroll 1
  for (; spins < SPIN_CAP; ++spins) {
    CFENCE();
#pragma unroll
    for (int k = 0; k < N; ++k) v[k] = ldsc(rs, off, k * stride);
    if (tags_ok<N>(v, tag)) break;
  }
  return spins + 1;
}
template <bool SLEEP> __device__ __forceinline__ void lds_wait(volatile LAS unsigned* w, unsigned want) {
#pragma unroll 1
  for (unsigned spins = 0; spins < (SPIN_CAP << 6); ++spins) {
    if (__builtin_amdgcn_readfirstlane(*w) >= want) break;
    if (SLEEP) __builtin_amdgcn_s_sleep(1);
  }
  CFENCE();
}
__device__ __forceinline__ float silu(float x) { return x * __builtin_amdgcn_rcpf(1.f + __expf(-x)); }

typedef unsigned long long v2q __attribute__((ext_vector_type(2)));
typedef unsigned long long v4q __attribute__((ext_vector_type(4)));
__global__ void __launch_bounds__(NTHR) fused_decode(const void* x_, const void* kc_, const void* vc_, const void* wq_, const void* wo_, const void* wg_,
                                                     const void* wu_, const void* wd_, void* out_, void* ws_) {
  v4q ka, kb; v2q kd;
  asm volatile("s_load_dwordx8 %0, %3, 0x0\n\ts_load_dwordx8 %1, %3, 0x20\n\ts_load_dwordx4 %2, %3, 0x40"
               : "=&s"(ka), "=&s"(kb), "=&s"(kd) : "s"(__builtin_amdgcn_kernarg_segment_ptr()));
  __builtin_amdgcn_sched_barrier(0);
  __shared__ __attribute__((aligned(16))) unsigned lds_[L_TOTAL];
  LAS unsigned* L = (LAS unsigned*)lds_;
  volatile LAS unsigned* FL = (volatile LAS unsigned*)(L + L_FLAG);
  const int b = blockIdx.x;
  const int h = (b >> 3) & 7, j = (b & 7) * 4 + (b >> 6);
  const int tid = threadIdx.x, lane = tid & 63, w = __builtin_amdgcn_readfirstlane(tid >> 6);
  const int u = (b & 63) + 64 * (b >> 7);
  STAMP_DECL;
  if (w == 0) STAMP(5);
  const __amdgpu_buffer_rsrc_t rc = mkrs((const GAS char*)(__builtin_amdgcn_s_getpc() & ~255ull), (w == 4 && u >= 120) ? CODE_BYTES : 0);
  const unsigned cpf = __builtin_amdgcn_raw_buffer_load_b32(rc, lane * 128, 1024, 0), cpf2 = __builtin_amdgcn_raw_buffer_load_b32(rc, lane * 128, 9216, 0);
  const bool isE = (j & 1) == 0;
  const int ei = j >> 1;
  const bool isD = !isE && u < 56;
  const bool fin = !isE && u >= 114;
  const int fc = u - 114;
  const int rs = lane >> 3, c8 = lane & 7, g4 = lane >> 4, n16 = lane & 15;
  const bool nlo = n16 < 8;
  const int gidx = 4 * c8 + 2 * (g4 & 1) + (n16 >> 3);
  const int NI = isE ? 6 : 8;
  int r0 = (isE ? 256 : 0) + 8 * NI * (w & 3);
  int pitch = u < 64 ? 8192 : 3584;
  int sb = r0 * pitch + (u < 64 ? u : u < 92 ? u - 64 : u - 92) * 128;
  int nb1 = u < 120 ? DM * pitch : 0;
  const int sa = 2 * g4 + ((lane >> 2) & 1);
  int fo = rs * NI * pitch + c8 * 16, xo = lane * 16, xso = sa * NI * 2;
  const int wre = 512 * h + 32 * ei, wrd = u < 56 ? 32 * u : 0;
  int wrow = (wrd + ((wre - wrd) & ((j & 1) - 1))) * 896;
  int oo = rs * 896 + c8 * 16;
  asm volatile("" ::"s"(r0), "s"(pitch), "s"(sb), "s"(nb1), "s"(wrow), "v"(fo), "v"(xo), "v"(xso), "v"(oo));
  __builtin_amdgcn_sched_barrier(0);
  if (w == 0) STAMP(27);
  __builtin_amdgcn_sched_barrier(0);
  asm volatile("s_waitcnt lgkmcnt(0)" : "+s"(ka), "+s"(kb), "+s"(kd));
  __builtin_amdgcn_sched_barrier(0);
  if (w == 0) STAMP(26);
  __builtin_amdgcn_sched_barrier(0);
  const GAS char *x = (const GAS char*)ka.x, *kc = (const GAS char*)ka.y, *vc = (const GAS char*)ka.z, *wq = (const GAS char*)ka.w;
  const GAS char *wo = (const GAS char*)kb.x, *wg = (const GAS char*)kb.y, *wu = (const GAS char*)kb.z, *wd = (const GAS char*)kb.w;
  GAS char *out = (GAS char*)kd.x, *ws = (GAS char*)kd.y;
  const unsigned ep = *(const __attribute__((address_space(4))) unsigned*)(unsigned long long)ws;
  const __amdgpu_buffer_rsrc_t rw = mkrs(ws, WS_END);
  constexpr float C2 = 0.0625f * 1.4426950408889634f;

  if (w == 4) {
    STAMP(0);
    __builtin_amdgcn_s_setprio(3);
    if (lane < 20) FL[lane] = 0u;
    asm volatile("s_waitcnt lgkmcnt(0)\n\ts_barrier" ::"s"(ep) : "memory");
    const unsigned tag = ep + 1u;
    if (u < 120) {
      const v2u r0 = *(const LAS v2u*)(L + L_R1 + 2 * (lane & 31)), r1 = *(const LAS v2u*)(L + L_R1 + 64 + 2 * (lane & 31)),
                r2 = *(const LAS v2u*)(L + L_R1 + 128 + 2 * (lane & 31)), r3 = *(const LAS v2u*)(L + L_R1 + 192 + 2 * (lane & 31));
      const float v0 = (asf(r0.x) + asf(r1.x)) + (asf(r2.x) + asf(r3.x)), v1 = (asf(r0.y) + asf(r1.y)) + (asf(r2.y) + asf(r3.y));
      const int hf = isE ? 1 : 0;
      const int dst = u < 64 ? WS_Q + (u >> 3) * 8192 + hf * 4096 + (u & 7) * 512 : u < 92 ? WS_GU + (u - 64) * 2048 + hf * 512 : WS_GU + (u - 92) * 2048 + 1024 + hf * 512;
      if (lane < 32) stsc<QSAUX>(rw, dst + lane * 16, asu(v0), asu(v1), tag);
    }
    STAMP(2);
    const unsigned xw = __builtin_amdgcn_raw_buffer_load_b16(mkrs(x, 896), (4 * c8 + g4) * 2, fin ? fc * 64 : 0, 0);
    const int go = WS_GU + (u >> 1) * 2048 + g4 * 512 + (u & 1) * 256 + n16 * 16;
    v4u gu[1];
    lds_wait<true>(FL + 5, 1u);
    gu[0] = ldsc(rw, go, isD ? 0 : OOB);
    asm volatile("" ::"v"(cpf), "v"(cpf2));
    BARRIER();
    BARRIER();
    BARRIER();
    if (lane < 2) {
      const float M = fmaxf(fmaxf(asf(L[L_WM + lane]), asf(L[L_WM + 2 + lane])), fmaxf(asf(L[L_WM + 4 + lane]), asf(L[L_WM + 6 + lane])));
      const float Ls = (asf(L[L_WL + lane]) + asf(L[L_WL + 2 + lane])) + (asf(L[L_WL + 4 + lane]) + asf(L[L_WL + 6 + lane]));
      stsc(rw, WS_ML + (h * 2 + lane) * 512 + j * 16, asu(M), asu(Ls), tag);
    }
    STAMP(4);
    if (isE) {
      lds_wait<true>(FL + 8, 1u);
      v4u ml[1];
      const unsigned np1 = poll<1>(rw, WS_ML + (h * 2 + (ei >> 3)) * 512 + (lane & 31) * 16, 0, tag, ml); RAW(29, np1); (void)np1;
      const float m = asf(ml[0].x);
      const float M = x16_max(row_max(m));
      const float e = __builtin_amdgcn_exp2f((m - M) * C2);
      float Ls = row_sum(e * asf(ml[0].z)); Ls = sw16(Ls, Ls);
      if (lane < 32) L[L_W + lane] = asu(e * __builtin_amdgcn_rcpf(Ls));
      if (lane == 0) FL[7] = 1u;
      STAMP(6);
    } else if (isD) {
      if (!tags_ok<1>(gu, tag)) poll<1>(rw, go, 0, tag, gu);
      const float y = rbf(sw16(asf(gu[0].x), asf(gu[0].z)));
      auto t = __builtin_amdgcn_permlane32_swap(asu(y), asu(y), false, false);
      const float hv = rbf(rbf(silu(asf(t[0]))) * asf(t[1]));
      const int r = 2 * n16 + (g4 & 1);
      if (lane < 32) L[L_OP + (r & 7) * 4 + (r >> 3)] = asu(hv);
      STAMP(6);
      BARRIER();
    } else if (fin) {
      lds_wait<true>(FL + 8, 1u);
      v4u fv[14];
      poll<14>(rw, WS_F + g4 * F_PITCH + fc * 256 + n16 * 16, 4 * F_PITCH, tag, fv);
      float f0 = 0.f, f1 = 0.f;
#pragma unroll
      for (int i = 0; i < 14; ++i) { f0 += asf(fv[i].x); f1 += asf(fv[i].z); }
      float f = sw16(f0, f1); f = sw32(f, f);
      const float g = rbf(silu(rbf(f)));
      if (lane < 32) L[L_W + 2 * n16 + (g4 & 1)] = asu(g);
      asm volatile("s_waitcnt lgkmcnt(0)" ::: "memory");
      const float gc = asf(L[L_W + 4 * c8 + g4]);
      int so = L_OP + g4 * 8 + c8, oa = (4 * c8 + g4) * 2 + (nlo ? 0 : OOB);
      float xf = blo(xw);
      asm volatile("" : "+v"(so), "+v"(oa), "+v"(xf));
      STAMP(16);
      BARRIER();
      const float s = rbf((asf(L[so]) + asf(L[so + 32])) + (asf(L[so + 64]) + asf(L[so + 96])));
      const float res = xf + rbf(s * gc);
      __builtin_amdgcn_raw_buffer_store_b16((unsigned short)pk2(res, res), mkrs(out, 896), oa, fc * 64, OAUX);
      if (fc == 0 && lane == 0) __hip_atomic_store((GAS unsigned*)ws, tag, RLX_AGENT);
      STAMP(8);
    }
    STAMP_FLUSH();
  } else {
    const int k = w;
    const unsigned hm = (lane & 1) ? 0xffff0000u : 0x0000ffffu;
    v4u kf[16], vf[16], wf[8];
    const __amdgpu_buffer_rsrc_t rx = mkrs(x, 896);
    const v4u xr = __builtin_amdgcn_raw_buffer_load_b128(rx, xo, 0, 0);
    const v4u xs = __builtin_amdgcn_raw_buffer_load_b128(rx, xso, r0 * 2, 0);
    __builtin_amdgcn_sched_barrier(0);
    if (k == 0) STAMP(1);
    v4u fr[8];
    {
      const __amdgpu_buffer_rsrc_t ra = mkrs(u < 64 ? wq : u < 92 ? wg : wu, nb1);
#pragma unroll
      for (int i = 0; i < 8; ++i) fr[i] = ldnt(ra, fo, (i >= 6 && isE) ? OOB : sb + i * pitch);
    }
    const v2u msk2 = {(lane & 2) ? 0u : hm, (lane & 2) ? hm : 0u};
    const int cA = 2 * k, sA = cA * 128, sB = k < 3 ? sA + 128 : OOB;
    const __amdgpu_buffer_rsrc_t ro = mkrs((isE ? wo : wd) + wrow, (isE || isD) ? 32 * 896 : 0);
#define LDW(i) wf[i] = ldnt(ro, oo, ((i) >> 1) * 8 * 896 + (((i) & 1) ? sB : sA))
#pragma unroll
    for (int i = 0; i < 2; ++i) wf[i] = ldnt(ro, oo, isE ? (i >> 1) * 8 * 896 + ((i & 1) ? sB : sA) : OOB);
    const __amdgpu_buffer_rsrc_t rk = mkrs(kc + ((size_t)h * SK + (SK - WIN) + 128 * j) * 512, 128 * 512);
    const __amdgpu_buffer_rsrc_t rv = mkrs(vc + ((size_t)h * SK + (SK - WIN) + 128 * j) * 512, 128 * 512);
    const int ko = k * 2048 + (n16 >> 2) * 512 + g4 * 64 + (lane & 3) * 16;
    const int vo = rs * 512 + c8 * 16 + k * 128;
    const int rot = ROT ? ((b + (b >> 3) + (b >> 6)) & 7) * 8192 : 0;
#define LDK(i) kf[i] = ldnt(rk, ko + ((i) & 1) * 256, (((i) >> 1) * 8192 + rot) & 0xffff)
#define LDV(i) vf[i] = ldnt(rv, vo, ((i) * 4096 + rot) & 0xffff)
#define LDS_(n) do { if ((n) < 16) LDV((n) < 16 ? (n) : 0); else LDW((n) >= 16 ? (n) - 16 + WEARLY : 2); } while (0)
    if (k == 0) STAMP(18);
    {
      float xv[8];
#pragma unroll
      for (int e = 0; e < 4; ++e) { xv[2 * e] = blo(xr[e]); xv[2 * e + 1] = bhi(xr[e]); }
      const float s1 = ((xv[0] + xv[1]) + (xv[2] + xv[3])) + ((xv[4] + xv[5]) + (xv[6] + xv[7]));
      const float mean = wave_sum(s1) * (1.f / DM);
      float s2 = 0.f;
#pragma unroll
      for (int e = 0; e < 8; ++e) { const float d = xv[e] - mean; s2 += d * d; }
      s2 = lane < 56 ? s2 : 0.f;
      const float var = wave_sum(s2) * (1.f / DM);
      const float mb = rbf(mean), rden = __builtin_amdgcn_rcpf(rbf(__builtin_amdgcn_sqrtf(rbf(rbf(var) + 1e-5f))));
      unsigned xc[8];
#pragma unroll
      for (int i = 0; i < 8; ++i) {
        const float xn = rbf(((i & 1) ? bhi(xs[i >> 1]) : blo(xs[i >> 1])) - mb) * rden;
        xc[i] = pk2(xn, xn);
      }
      if (k == 0) STAMP(19);
      __builtin_amdgcn_sched_barrier(0);
      f32x4 a0 = {0.f, 0.f, 0.f, 0.f}, a1 = {0.f, 0.f, 0.f, 0.f};
#pragma unroll
      for (int i = 0; i < 8; ++i) {
        const v2u am = {xc[i] & msk2.x, xc[i] & msk2.y};
        a0 = mfma16(am, v2u{fr[i].x, fr[i].y}, a0);
        a1 = mfma16(am, v2u{fr[i].z, fr[i].w}, a1);
        __builtin_amdgcn_sched_barrier(0);
        if (i >= 8 - WINL) LDK(i - (8 - WINL));
      }
      float v0, v1;
      fold2<true>(a0, a1, nlo, v0, v1);
      if (lane < 32) *(LAS v2u*)(L + L_R1 + 64 * k + 2 * gidx) = v2u{asu(v0), asu(v1)};
      asm volatile("s_waitcnt lgkmcnt(0)\n\ts_barrier" ::"s"(ep), "v"(cpf), "v"(cpf2) : "memory");
      if (k == 0) STAMP(12);
    }
    const unsigned tag = ep + 1u;
#pragma unroll
    for (int i = WINL; i < 16; ++i) {
      VMW(WINL - 1); LDK(i);
      if (i - WINL + 2 < WEARLY) wf[i - WINL + 2] = ldnt(ro, oo, isE ? ((i - WINL + 2) >> 1) * 8 * 896 + (((i - WINL + 2) & 1) ? sB : sA) : OOB);
    }
#pragma unroll
    for (int i = 0; i < QPOS; ++i) { VMW(WINL - 1); LDV(i); }
    v4u q2[2];
    {
      const int qo = WS_Q + h * 8192 + k * 1024 + lane * 16;
      v4u qv[2];
      qv[0] = __builtin_amdgcn_raw_buffer_load_b128(rw, qo, (ABL & 1) ? OOB : 0, QLAUX);
      qv[1] = __builtin_amdgcn_raw_buffer_load_b128(rw, qo, (ABL & 3) ? OOB : 4096, QLAUX);
      __builtin_amdgcn_sched_barrier(0);
      if (lane == 0) FL[5] = 1u;
      if (k == 0) STAMP(17);
#pragma unroll
      for (int i = QPOS; i < QPOS + WINL; ++i) { VMW(WINL + 1); LDV(i); }
      VMW(WINL);
      const bool qok = (ABL & 7) ? (qv[0].y != 12345u || tag != 77u) : tags_ok<2>(qv, tag);
      q2[0] = ldsc(rw, qo, qok ? OOB : 0);
      q2[1] = ldsc(rw, qo, qok ? OOB : 4096);
      __builtin_amdgcn_sched_barrier(0);
#pragma unroll
      for (int i = QPOS + WINL; i < VB1; ++i) LDS_(i);
      if (k == 0) STAMP(13);
      unsigned np = 1;
      if (!qok) {
        qv[0] = q2[0]; qv[1] = q2[1]; ++np;
        if (!tags_ok<2>(qv, tag)) np += poll<2>(rw, qo, 4096, tag, qv);
      }
      if (k == 0) RAW(28, np);
      if (k == 0) STAMP(3);
      L[L_Q + 64 * k + lane] = pk2(asf(qv[0].x) + asf(qv[1].x), asf(qv[0].z) + asf(qv[1].z));
    }
    BARRIER();
#pragma unroll
    for (int i = VB1; i < VB2; ++i) LDS_(i);
    if (k == 0) STAMP(24);
    v4u q[4];
#pragma unroll
    for (int t = 0; t < 4; ++t) {
      const int qa = L_Q + ((lane >> 2) & 1) * 128 + (t & 1) * 64 + g4 * 16 + (lane & 3) * 4;
      q[t] = *(const LAS v4u*)(L + ((n16 >> 3) == (t >> 1) ? qa : L_Z));
    }
    float y[4];
#pragma unroll
    for (int t = 0; t < 4; ++t) {
      f32x4 d = {0.f, 0.f, 0.f, 0.f};
      d = mfma32(q[0], kf[2 * t], d); d = mfma32(q[1], kf[2 * t + 1], d);
      d = mfma32(q[2], kf[2 * t + 8], d); d = mfma32(q[3], kf[2 * t + 9], d);
      const float lo = (lane & 1) ? d[1] : d[0], hi = (lane & 1) ? d[3] : d[2];
      y[t] = (lane & 2) ? hi : lo;
    }
    {
      const float k0 = (lane & 2) ? y[2] : y[0], s0 = (lane & 2) ? y[0] : y[2], k1 = (lane & 2) ? y[3] : y[1], s1 = (lane & 2) ? y[1] : y[3];
      y[0] = k0 + dpp<XOR2>(s0); y[1] = k1 + dpp<XOR2>(s1);
    }
    const float sc = rbf(((lane & 1) ? y[1] : y[0]) + dpp<XOR1>((lane & 1) ? y[0] : y[1]));
    const float m = x32_max(row_max(sc));
    const int a = g4 & 1;
    if ((lane & 0x2f) == 0) L[L_WM + 2 * k + a] = asu(m);
    BARRIER();
#pragma unroll
    for (int i = VB2; i < VB3; ++i) LDS_(i);
    const float M = fmaxf(fmaxf(asf(L[L_WM + a]), asf(L[L_WM + 2 + a])), fmaxf(asf(L[L_WM + 4 + a]), asf(L[L_WM + 6 + a])));
    const float p = __builtin_amdgcn_exp2f((sc - M) * C2);
    L[L_P + a * 128 + (4 * (k & 1) + (n16 >> 2)) * 16 + 2 * ((lane & 3) + 4 * (g4 >> 1)) + (k >> 1)] = pk2(p, p);
    float l = row_sum(p); l = sw32(l, l);
    if ((lane & 0x2f) == 0) L[L_WL + 2 * k + a] = asu(l);
    BARRIER();
#pragma unroll
    for (int i = VB3; i < VB4; ++i) LDS_(i);
    if (k == 0) STAMP(9);
    {
      v4u pa[4];
#pragma unroll
      for (int t = 0; t < 4; ++t) pa[t] = *(const LAS v4u*)(L + L_P + (n16 >> 3) * 128 + sa * 16 + 4 * t);
      int od = WS_O + (h + 8 * j) * O_PITCH + (lane >> 5) * 1024 + k * 256 + (2 * c8 + (g4 & 1)) * 16 + (nlo ? 0 : OOB);
      asm volatile("" : "+v"(od));
      f32x4 a0 = {0.f, 0.f, 0.f, 0.f}, a1 = {0.f, 0.f, 0.f, 0.f};
#pragma unroll
      for (int i = 0; i < 16; ++i) {
        const unsigned c = pa[i >> 2][i & 3];
        const v2u am = {c & msk2.x, c & msk2.y};
        a0 = mfma16(am, v2u{vf[i].x, vf[i].y}, a0);
        a1 = mfma16(am, v2u{vf[i].z, vf[i].w}, a1);
        __builtin_amdgcn_sched_barrier(0);
        if (i == QPOS + WINL) asm volatile("" ::"v"(q2[0]), "v"(q2[1]));
        {
          const int nx = i + WOWIN;
          if (nx >= VB4 && nx < 16 + WPRE && nx < 24 - WEARLY && i < 15) LDS_(nx);
        }
      }
      const f32x4 o = fold4<true>(a0, a1);
      stsc(rw, od, pk2(o[0], o[1]), pk2(o[2], o[3]), tag);
      __builtin_amdgcn_sched_barrier(0);
    }
    if (lane == 0) FL[8] = 1u;
    if (k == 3) STAMP(14);
    if (k == 0) STAMP(15);
#pragma unroll
    for (int i = 16 + (WPRE < WOWIN - 1 ? WPRE : WOWIN - 1); i < 24 - WEARLY; ++i) LDS_(i);
    if (isE || isD) {
      v4u msk = {(c8 >> 1) == 0 ? hm : 0u, (c8 >> 1) == 1 ? hm : 0u, (c8 >> 1) == 2 ? hm : 0u, (c8 >> 1) == 3 ? hm : 0u};
      const int dead = (k < 3 || lane < 32) ? 0 : OOB;
      int sd = isE ? WS_S + (h * 16 + ei) * S_PITCH + (cA + (lane >> 5)) * 256 + (2 * c8 + (g4 & 1)) * 16 + (nlo ? dead : OOB)
                   : WS_F + u * F_PITCH + (cA + (lane >> 5)) * 512 + gidx * 16 + dead;
      int lw = L_OP + k * 32 + (4 * (c8 & 1) + g4) * 4 + (c8 >> 1) + (nlo ? 0 : 256), lr = L_OP + rs * 4;
      unsigned tv = tag;
      asm volatile("" : "+v"(msk), "+v"(sd), "+v"(lw), "+v"(lr), "+v"(tv));
      if (isE) {
        if (ODLY) __builtin_amdgcn_s_sleep(ODLY);
        v4u ov[1];
        const int po = WS_O + (h + 8 * (8 * k + rs)) * O_PITCH + ei * 128 + c8 * 16;
        ov[0] = ldsc(rw, po, 0);
        __builtin_amdgcn_sched_barrier(0);
        v4u ov2[1];
        if (ODLY2) { __builtin_amdgcn_s_sleep(ODLY2); CFENCE(); ov2[0] = ldsc(rw, po, 0); __builtin_amdgcn_sched_barrier(0); }
        lds_wait<false>(FL + 7, 1u);
        const float wt = asf(L[L_W + 8 * k + rs]);
        asm volatile("" ::"v"(wt));
        unsigned np2 = 1;
        if (!tags_ok<1>(ov, tag)) {
          if (ODLY2) ov[0] = ov2[0];
          if (!ODLY2 || !tags_ok<1>(ov, tag)) np2 += poll<1>(rw, po, 0, tag, ov);
        }
        if (k == 0) RAW(30, np2);
        if (k == 0) STAMP(7);
        L[lw] = asu(red4(wt * blo(ov[0].x), wt * bhi(ov[0].x), wt * blo(ov[0].z), wt * bhi(ov[0].z)));
        BARRIER();
        if (k == 0) STAMP(25);
        const f32x4 os = (__builtin_bit_cast(f32x4, *(const LAS v4u*)(L + lr)) + __builtin_bit_cast(f32x4, *(const LAS v4u*)(L + lr + 32))) +
                         (__builtin_bit_cast(f32x4, *(const LAS v4u*)(L + lr + 64)) + __builtin_bit_cast(f32x4, *(const LAS v4u*)(L + lr + 96)));
        f32x4 a0 = {0.f, 0.f, 0.f, 0.f}, a1 = {0.f, 0.f, 0.f, 0.f};
#pragma unroll
        for (int i = 0; i < 4; ++i) { const v4u am = amask(pk2(os[i], os[i]), msk); a0 = mfma32(am, wf[2 * i], a0); a1 = mfma32(am, wf[2 * i + 1], a1); }
        const f32x4 s = fold4<false>(a0, a1);
        stsc(rw, sd, pk2(s[0], s[1]), pk2(s[2], s[3]), tv);
        if (ODLY2) asm volatile("" ::"v"(ov2[0]));
        if (k == 0) STAMP(10);
      }
      CFENCE();
      if (isD) {
#pragma unroll
        for (int i = 0; i < WEARLY; ++i) LDW(i);
        BARRIER();
        const f32x4 os = __builtin_bit_cast(f32x4, *(const LAS v4u*)(L + lr));
        f32x4 a0 = {0.f, 0.f, 0.f, 0.f}, a1 = {0.f, 0.f, 0.f, 0.f};
#pragma unroll
        for (int i = 3; i >= 0; --i) { const v4u am = amask(pk2(os[i], os[i]), msk); a0 = mfma32(am, wf[2 * i], a0); a1 = mfma32(am, wf[2 * i + 1], a1); }
        float v0, v1;
        fold2<false>(a0, a1, nlo, v0, v1);
        stsc(rw, sd, asu(v0), asu(v1), tv);
        if (k == 0) STAMP(11);
      }
    } else if (fin) {
      int lw = L_OP + 32 * k + g4 * 8 + c8 + (nlo ? 0 : 256);
      asm volatile("" : "+v"(lw));
      v4u sv[4];
      const unsigned np3 = poll<4>(rw, WS_S + (32 * k + rs) * S_PITCH + fc * 128 + c8 * 16, 8 * S_PITCH, tag, sv); if (k == 0) RAW(31, np3); (void)np3;
      const float s = red4((blo(sv[0].x) + blo(sv[1].x)) + (blo(sv[2].x) + blo(sv[3].x)), (bhi(sv[0].x) + bhi(sv[1].x)) + (bhi(sv[2].x) + bhi(sv[3].x)),
                           (blo(sv[0].z) + blo(sv[1].z)) + (blo(sv[2].z) + blo(sv[3].z)), (bhi(sv[0].z) + bhi(sv[1].z)) + (bhi(sv[2].z) + bhi(sv[3].z)));
      L[lw] = asu(s);
      if (k == 0) STAMP(20); if (k == 1) STAMP(21); if (k == 2) STAMP(22); if (k == 3) STAMP(23);
      BARRIER();
    }
    STAMP_FLUSH();
  }
}

extern "C" void kernel_launch(void* const* d_in, const int* in_sizes, int n_in, void* d_out, int out_size, void* d_ws, size_t ws_size,
                              hipStream_t stream) {
  fused_decode<<<NWG, NTHR, 0, stream>>>(d_in[0], d_in[1], d_in[2], d_in[3], d_in[4], d_in[6], d_in[7], d_in[8], d_out, d_ws);
}
```

```cpp
#define PROBE 8
#define KFIRST 1
#define VFIRST 1
#include <hip/hip_runtime.h>
#include <stdint.h>

#define GAS __attribute__((address_space(1)))
#define LAS __attribute__((address_space(3)))
typedef unsigned v4u __attribute__((ext_vector_type(4)));
typedef unsigned v2u __attribute__((ext_vector_type(2)));
typedef float f32x4 __attribute__((ext_vector_type(4)));
typedef __bf16 bf16x8 __attribute__((ext_vector_type(8)));
typedef __bf16 bf16x2 __attribute__((ext_vector_type(2)));
typedef short s16x4 __attribute__((ext_vector_type(4)));
#define RLX_AGENT __ATOMIC_RELAXED, __HIP_MEMORY_SCOPE_AGENT

#define WINL 6
#define VB1 (10 + VFIRST)
#define VB2 (12 + VFIRST)
#define VB3 (14 + VFIRST)
#define VB4 VB3
#define QPOS VFIRST
#define ABL 0
#define QLAUX 16
#define ROT 1
#define OAUX 16
#define WPRE 6
#define ODLY2 0
#define WEARLY 2
#define PTOUCH 0
#define PFMODE 1
#define PFU 120
#define PFOFF 256
#define PFMASK 0xb8
#define TOUCH 0
#define VLATE 1
#define WSOFF 0
#define LTOT 1536
#define WOWIN 7
#define QSAUX 16
#define ODLY 12
#define CODE_BYTES 10496

constexpr int DM = 448, SK = 65536, WIN = 4096;
constexpr int NWG = 256, NTHR = 320;
constexpr int WS_Q = 4096;
constexpr int WS_GU = WS_Q + 8 * 8192;
constexpr int WS_ML = WS_GU + 28 * 2048;
constexpr int O_PITCH = 2176;
constexpr int WS_O = WS_ML + 8192;
constexpr int S_PITCH = 1792;
constexpr int WS_S = WS_O + 256 * O_PITCH;
constexpr int F_PITCH = 3584;
constexpr int WS_F = WS_S + 128 * S_PITCH;
constexpr int WS_END = WS_F + 56 * F_PITCH;
constexpr int OOB = 1 << 30;
constexpr int L_R1 = 0;
constexpr int L_Q = 256;
constexpr int L_P = 512;
constexpr int L_W = 800;
constexpr int L_WM = 832;
constexpr int L_WL = 840;
constexpr int L_FLAG = 928;
constexpr int L_Z = 944;
constexpr int L_OP = 960;
constexpr int L_TOTAL = LTOT;
constexpr unsigned SPIN_CAP = 1u << 16;

#define RAW(k, v)
#define STAMP(k) do { if ((k) == PROBE) stp = __builtin_amdgcn_s_memrealtime(); } while (0)
#define STAMP_DECL const unsigned long long st0 = __builtin_amdgcn_s_memrealtime(); unsigned long long stp = 0
#define STAMP_FLUSH() do { if (stp) { const unsigned long long tend = st0 + 3000 + 8 * (stp - st0); while (__builtin_amdgcn_s_memrealtime() < tend) __builtin_amdgcn_s_sleep(2); } } while (0)

__device__ __forceinline__ float asf(unsigned u) { return __builtin_bit_cast(float, u); }
__device__ __forceinline__ unsigned asu(float f) { return __builtin_bit_cast(unsigned, f); }
__device__ __forceinline__ float blo(unsigned u) { return asf(u << 16); }
__device__ __forceinline__ float bhi(unsigned u) { return asf(u & 0xffff0000u); }
__device__ __forceinline__ float rbf(float f) { return (float)(__bf16)f; }
__device__ __forceinline__ unsigned pk2(float lo, float hi) { bf16x2 t = {(__bf16)lo, (__bf16)hi}; return __builtin_bit_cast(unsigned, t); }
__device__ __forceinline__ f32x4 mfma32(v4u a, v4u b, f32x4 c) {
  return __builtin_amdgcn_mfma_f32_16x16x32_bf16(__builtin_bit_cast(bf16x8, a), __builtin_bit_cast(bf16x8, b), c, 0, 0, 0);
}
__device__ __forceinline__ f32x4 mfma16(v2u a, v2u b, f32x4 c) {
  return __builtin_amdgcn_mfma_f32_16x16x16bf16_1k(__builtin_bit_cast(s16x4, a), __builtin_bit_cast(s16x4, b), c, 0, 0, 0);
}
template <int CTRL> __device__ __forceinline__ float dpp(float x) {
  return asf((unsigned)__builtin_amdgcn_mov_dpp((int)asu(x), CTRL, 0xf, 0xf, true));
}
constexpr int XOR1 = 0xB1, XOR2 = 0x4E, XOR7 = 0x141, XOR8 = 0x128;
__device__ __forceinline__ float sw16(float a, float b) { auto s = __builtin_amdgcn_permlane16_swap(asu(a), asu(b), false, false); return asf(s[0]) + asf(s[1]); }
__device__ __forceinline__ float sw32(float a, float b) { auto s = __builtin_amdgcn_permlane32_swap(asu(a), asu(b), false, false); return asf(s[0]) + asf(s[1]); }
__device__ __forceinline__ float x16_max(float x) { auto s = __builtin_amdgcn_permlane16_swap(asu(x), asu(x), false, false); return fmaxf(asf(s[0]), asf(s[1])); }
__device__ __forceinline__ float x32_max(float x) { auto s = __builtin_amdgcn_permlane32_swap(asu(x), asu(x), false, false); return fmaxf(asf(s[0]), asf(s[1])); }
__device__ __forceinline__ float row_sum(float x) { x += dpp<XOR1>(x); x += dpp<XOR2>(x); x += dpp<XOR7>(x); x += dpp<XOR8>(x); return x; }
__device__ __forceinline__ float row_max(float x) { x = fmaxf(x, dpp<XOR1>(x)); x = fmaxf(x, dpp<XOR2>(x)); x = fmaxf(x, dpp<XOR7>(x)); return fmaxf(x, dpp<XOR8>(x)); }
__device__ __forceinline__ float wave_sum(float x) { x = row_sum(x); x = sw16(x, x); return sw32(x, x); }
__device__ __forceinline__ float red4(float v0, float v1, float v2, float v3) { const float t = sw16(sw32(v0, v2), sw32(v1, v3)); return t + dpp<XOR8>(t); }
__device__ __forceinline__ __amdgpu_buffer_rsrc_t mkrs(const GAS char* p, int bytes) { return __builtin_amdgcn_make_buffer_rsrc((void*)p, 0, bytes, 0x00020000); }
__device__ __forceinline__ v4u ldnt(__amdgpu_buffer_rsrc_t rs, int voff, int soff) {
  const v4u r = __builtin_amdgcn_raw_buffer_load_b128(rs, voff, soff, 2);
  __builtin_amdgcn_sched_barrier(0);
  return r;
}
__device__ __forceinline__ v4u ldsc(__amdgpu_buffer_rsrc_t rs, int voff, int soff) { return __builtin_amdgcn_raw_buffer_load_b128(rs, voff, soff, 16); }
template <int AUX = 16> __device__ __forceinline__ void stsc(__amdgpu_buffer_rsrc_t rs, int off, unsigned d0, unsigned d1, unsigned tag) {
  v4u g = {d0, tag, d1, tag};
  __builtin_amdgcn_raw_buffer_store_b128(g, rs, off, 0, AUX);
}
#define BARRIER() asm volatile("s_waitcnt lgkmcnt(0)\n\ts_barrier" ::: "memory")
#define CFENCE() asm volatile("" ::: "memory")
#define VMW(n) asm volatile("s_waitcnt vmcnt(%0)" ::"i"(n) : "memory")
__device__ __forceinline__ v4u amask(unsigned x, const v4u& m) { return v4u{x & m.x, x & m.y, x & m.z, x & m.w}; }
template <bool R16> __device__ __forceinline__ f32x4 fold4(const f32x4& A, const f32x4& B) {
  f32x4 s;
#pragma unroll
  for (int r = 0; r < 4; ++r) {
    auto t = R16 ? __builtin_amdgcn_permlane16_swap(asu(A[r]), asu(B[r]), false, false) : __builtin_amdgcn_permlane32_swap(asu(A[r]), asu(B[r]), false, false);
    s[r] = asf(t[0]) + dpp<XOR8>(asf(t[1]));
  }
  return s;
}
template <bool R16> __device__ __forceinline__ void fold2(const f32x4& A, const f32x4& B, bool nlo, float& v0, float& v1) {
  float s[4];
#pragma unroll
  for (int r = 0; r < 4; ++r) {
    auto t = R16 ? __builtin_amdgcn_permlane16_swap(asu(A[r]), asu(B[r]), false, false) : __builtin_amdgcn_permlane32_swap(asu(A[r]), asu(B[r]), false, false);
    const float a = asf(t[0]), b = asf(t[1]);
    s[r] = r < 2 ? a + dpp<XOR8>(b) : dpp<XOR8>(a) + b;
  }
  v0 = nlo ? s[0] : s[2];
  v1 = nlo ? s[1] : s[3];
}
template <int N> __device__ __forceinline__ bool tags_ok(const v4u (&v)[N], unsigned tag) {
  bool ok = true;
#pragma unroll
  for (int k = 0; k < N; ++k) ok &= (v[k].y == tag) & (v[k].w == tag);
  return __builtin_amdgcn_ballot_w64(!ok) == 0ull;
}
template <int N> __device__ __forceinline__ unsigned poll(__amdgpu_buffer_rsrc_t rs, int off, int stride, unsigned tag, v4u (&v)[N]) {
  unsigned spins = 0;
#pragma unroll 1
  for (; spins < SPIN_CAP; ++spins) {
    CFENCE();
#pragma unroll
    for (int k = 0; k < N; ++k) v[k] = ldsc(rs, off, k * stride);
    if (tags_ok<N>(v, tag)) break;
  }
  return spins + 1;
}
template <bool SLEEP> __device__ __forceinline__ void lds_wait(volatile LAS unsigned* w, unsigned want) {
#pragma unroll 1
  for (unsigned spins = 0; spins < (SPIN_CAP << 6); ++spins) {
    if (__builtin_amdgcn_readfirstlane(*w) >= want) break;
    if (SLEEP) __builtin_amdgcn_s_sleep(1);
  }
  CFENCE();
}
__device__ __forceinline__ float silu(float x) { return x * __builtin_amdgcn_rcpf(1.f + __expf(-x)); }

typedef unsigned long long v2q __attribute__((ext_vector_type(2)));
typedef unsigned long long v4q __attribute__((ext_vector_type(4)));
__global__ void __launch_bounds__(NTHR, 2) fused_decode(const void* x_, const void* kc_, const void* vc_, const void* wq_, const void* wo_, const void* wg_,
                                                     const void* wu_, const void* wd_, void* out_, void* ws_) {
  v4q ka, kb; v2q kd; unsigned long long pc;
  int b = blockIdx.x, tid = threadIdx.x;
  asm volatile("s_load_dwordx8 %0, %5, 0x0\n\ts_load_dwordx8 %1, %5, 0x20\n\ts_load_dwordx4 %2, %5, 0x40\n\ts_getpc_b64 %3"
               : "=&s"(ka), "=&s"(kb), "=&s"(kd), "=&s"(pc), "+s"(b) : "s"(__builtin_amdgcn_kernarg_segment_ptr()));
  asm volatile("" : "+v"(tid));
  __builtin_amdgcn_sched_barrier(0);
  __shared__ __attribute__((aligned(16))) unsigned lds_[L_TOTAL];
  LAS unsigned* L = (LAS unsigned*)lds_;
  volatile LAS unsigned* FL = (volatile LAS unsigned*)(L + L_FLAG);
  const int w = __builtin_amdgcn_readfirstlane(tid >> 6);
  STAMP_DECL;
  if (w == 0) STAMP(5);
  unsigned cpf = 0, cpf2 = 0;
  if (w == 4 && (b & PFMASK) == PFMASK) {
    const __amdgpu_buffer_rsrc_t rc = mkrs((const GAS char*)(pc & ~255ull), CODE_BYTES);
    cpf = __builtin_amdgcn_raw_buffer_load_b32(rc, (tid & 63) * 128, PFOFF, 0);
    cpf2 = __builtin_amdgcn_raw_buffer_load_b32(rc, (tid & 63) * 128, PFOFF + 8192, 0);
  }
  const bool e0 = (b & 64) == 0, q0 = b < 128;
  const int t0 = b & 63;
  const int NI = e0 ? 6 : 8;
  const int r0 = (e0 ? 256 : 0) + 8 * NI * w;
  const int pitch = q0 ? 8192 : 3584;
  const int sb = r0 * pitch + ((q0 || t0 < 28) ? t0 : t0 - 28) * 128;
  const int nb1 = (q0 || t0 < 56) ? DM * pitch : 0;
  const int sa = 2 * ((tid >> 4) & 3) + ((tid >> 2) & 1);
  const int fo = ((tid >> 3) & 7) * (NI * pitch) + (tid & 7) * 16, xo = (tid & 63) * 16, xso = sa * (NI * 2);
  asm volatile("" ::"s"(r0 * 2), "s"(pitch), "s"(sb), "s"(nb1), "v"(fo), "v"(xo), "v"(xso));
  __builtin_amdgcn_sched_barrier(0);
  if (w == 0) STAMP(27);
  asm volatile("s_waitcnt lgkmcnt(0)" : "+s"(ka), "+s"(kb), "+s"(kd));
  if (w == 0) STAMP(26);
  const GAS char *x = (const GAS char*)ka.x, *kc = (const GAS char*)ka.y, *vc = (const GAS char*)ka.z, *wq = (const GAS char*)ka.w;
  const GAS char *wo = (const GAS char*)kb.x, *wg = (const GAS char*)kb.y, *wu = (const GAS char*)kb.z, *wd = (const GAS char*)kb.w;
  GAS char *out = (GAS char*)kd.x, *ws = (GAS char*)kd.y;
  const __amdgpu_buffer_rsrc_t rx = mkrs(x, 896);
  typedef const __attribute__((address_space(4))) unsigned* cptr;
  unsigned ep, tk, tv_;
  asm volatile("s_cmp_lg_u32 %1, 4\n\ts_cbranch_scc1 1f\n\ts_load_dword %0, %2, 0x0\n1:" : "=&s"(ep) : "s"(w), "s"(ws) : "scc");
  tk = tv_ = 0;
  constexpr float C2 = 0.0625f * 1.4426950408889634f;
#define IDS() \
  asm volatile("" : "+s"(b)); \
  asm volatile("" : "+v"(tid)); \
  __builtin_amdgcn_sched_barrier(0); \
  const int h = (b >> 3) & 7, j = (b & 7) * 4 + (b >> 6); \
  const int lane = tid & 63; \
  const int u = (b & 63) + 64 * (b >> 7); \
  const bool isE = (j & 1) == 0; \
  const int ei = j >> 1; \
  const bool isD = !isE && u < 56; \
  const bool fin = !isE && u >= 114; \
  const int fc = u - 114; \
  const int rs = lane >> 3, c8 = lane & 7, g4 = lane >> 4, n16 = lane & 15; \
  const bool nlo = n16 < 8; \
  const int gidx = 4 * c8 + 2 * (g4 & 1) + (n16 >> 3); \
  const __amdgpu_buffer_rsrc_t rw = mkrs(ws, WS_END); \
  (void)rs; (void)gidx

  if (w == 4) {
    IDS();
    STAMP(0);
    __builtin_amdgcn_s_setprio(3);
    asm volatile("s_barrier\n\ts_waitcnt lgkmcnt(0)" : "+s"(ep) : "s"(tk), "s"(tv_) : "memory");
    const unsigned tag = ep + 1u;
    if (lane == 0) *(volatile LAS v2u*)(L + L_FLAG + 2) = v2u{tag, 1u};
    if (u < 120) {
      const v2u r0 = *(const LAS v2u*)(L + L_R1 + 2 * (lane & 31)), r1 = *(const LAS v2u*)(L + L_R1 + 64 + 2 * (lane & 31)),
                r2 = *(const LAS v2u*)(L + L_R1 + 128 + 2 * (lane & 31)), r3 = *(const LAS v2u*)(L + L_R1 + 192 + 2 * (lane & 31));
      const float v0 = (asf(r0.x) + asf(r1.x)) + (asf(r2.x) + asf(r3.x)), v1 = (asf(r0.y) + asf(r1.y)) + (asf(r2.y) + asf(r3.y));
      const int hf = isE ? 1 : 0;
      const int dst = u < 64 ? WS_Q + (u >> 3) * 8192 + hf * 4096 + (u & 7) * 512 : u < 92 ? WS_GU + (u - 64) * 2048 + hf * 512 : WS_GU + (u - 92) * 2048 + 1024 + hf * 512;
      if (lane < 32) stsc<QSAUX>(rw, dst + lane * 16, asu(v0), asu(v1), tag);
    }
    STAMP(2);
    unsigned pt0 = 0, pt1 = 0;
    if (PTOUCH) { pt0 = *(cptr)(unsigned long long)(wd + (u < 56 ? u : 0) * 32 * 896); pt1 = *(cptr)(unsigned long long)out; }
    const unsigned xw = __builtin_amdgcn_raw_buffer_load_b16(rx, (4 * c8 + g4) * 2, fin ? fc * 64 : 0, 0);
    const int go = WS_GU + (u >> 1) * 2048 + g4 * 512 + (u & 1) * 256 + n16 * 16;
    v4u gu[1];
    lds_wait<true>(FL + 5, 1u);
    gu[0] = ldsc(rw, go, isD ? 0 : OOB);
    asm volatile("" ::"s"(pt0), "s"(pt1));
    asm volatile("" ::"v"(cpf), "v"(cpf2));
    BARRIER();
    BARRIER();
    BARRIER();
    if (lane < 2) {
      const float M = fmaxf(fmaxf(asf(L[L_WM + lane]), asf(L[L_WM + 2 + lane])), fmaxf(asf(L[L_WM + 4 + lane]), asf(L[L_WM + 6 + lane])));
      const float Ls = (asf(L[L_WL + lane]) + asf(L[L_WL + 2 + lane])) + (asf(L[L_WL + 4 + lane]) + asf(L[L_WL + 6 + lane]));
      stsc(rw, WS_ML + (h * 2 + lane) * 512 + j * 16, asu(M), asu(Ls), tag);
    }
    STAMP(4);
    if (isE) {
      lds_wait<true>(FL + 8, 1u);
      v4u ml[1];
      const unsigned np1 = poll<1>(rw, WS_ML + (h * 2 + (ei >> 3)) * 512 + (lane & 31) * 16, 0, tag, ml); RAW(29, np1); (void)np1;
      const float m = asf(ml[0].x);
      const float M = x16_max(row_max(m));
      const float e = __builtin_amdgcn_exp2f((m - M) * C2);
      float Ls = row_sum(e * asf(ml[0].z)); Ls = sw16(Ls, Ls);
      if (lane < 32) L[L_W + lane] = asu(e * __builtin_amdgcn_rcpf(Ls));
      CFENCE();
      if (lane == 0) FL[7] = 1u;
      STAMP(6);
    } else if (isD) {
      if (!tags_ok<1>(gu, tag)) poll<1>(rw, go, 0, tag, gu);
      const float y = rbf(sw16(asf(gu[0].x), asf(gu[0].z)));
      auto t = __builtin_amdgcn_permlane32_swap(asu(y), asu(y), false, false);
      const float hv = rbf(rbf(silu(asf(t[0]))) * asf(t[1]));
      const int r = 2 * n16 + (g4 & 1);
      if (lane < 32) L[L_OP + (r & 7) * 4 + (r >> 3)] = asu(hv);
      STAMP(6);
      BARRIER();
    } else if (fin) {
      lds_wait<true>(FL + 8, 1u);
      v4u fv[14];
      poll<14>(rw, WS_F + g4 * F_PITCH + fc * 256 + n16 * 16, 4 * F_PITCH, tag, fv);
      float f0 = 0.f, f1 = 0.f;
#pragma unroll
      for (int i = 0; i < 14; ++i) { f0 += asf(fv[i].x); f1 += asf(fv[i].z); }
      float f = sw16(f0, f1); f = sw32(f, f);
      const float g = rbf(silu(rbf(f)));
      if (lane < 32) L[L_W + 2 * n16 + (g4 & 1)] = asu(g);
      asm volatile("s_waitcnt lgkmcnt(0)" ::: "memory");
      const float gc = asf(L[L_W + 4 * c8 + g4]);
      int so = L_OP + g4 * 8 + c8, oa = (4 * c8 + g4) * 2 + (nlo ? 0 : OOB);
      float xf = blo(xw);
      asm volatile("" : "+v"(so), "+v"(oa), "+v"(xf));
      STAMP(16);
      BARRIER();
      const float s = rbf((asf(L[so]) + asf(L[so + 32])) + (asf(L[so + 64]) + asf(L[so + 96])));
      const float res = xf + rbf(s * gc);
      __builtin_amdgcn_raw_buffer_store_b16((unsigned short)pk2(res, res), mkrs(out, 896), oa, fc * 64, OAUX);
      if (fc == 0 && lane == 0) __hip_atomic_store((GAS unsigned*)ws, tag, RLX_AGENT);
      STAMP(8);
    }
    STAMP_FLUSH();
  } else {
    const v4u xr = __builtin_amdgcn_raw_buffer_load_b128(rx, xo, 0, 0);
    const v4u xs = __builtin_amdgcn_raw_buffer_load_b128(rx, xso, r0 * 2, 0);
    __builtin_amdgcn_sched_barrier(0);
    if (w == 0) STAMP(1);
    v4u fr[8];
    const GAS char* w1 = q0 ? wq : t0 < 28 ? wg : wu;
    {
      const __amdgpu_buffer_rsrc_t ra = mkrs(w1, nb1);
#pragma unroll
      for (int i = 0; i < 6; ++i) fr[i] = ldnt(ra, fo, sb + i * pitch);
    }
    IDS();
    const int k = w;
    const unsigned hm = (lane & 1) ? 0xffff0000u : 0x0000ffffu;
    v4u kf[16], vf[16], wf[8];
    if (tid < 20) FL[tid] = 0u;
    const int wre = 512 * h + 32 * ei, wrd = u < 56 ? 32 * u : 0;
    const int wrow = (wrd + ((wre - wrd) & ((j & 1) - 1))) * 896;
    const int oo = rs * 896 + c8 * 16;
    const v2u msk2 = {(lane & 2) ? 0u : hm, (lane & 2) ? hm : 0u};
    const int cA = 2 * k, sA = cA * 128, sB = k < 3 ? sA + 128 : OOB;
    const __amdgpu_buffer_rsrc_t ro = mkrs((isE ? wo : wd) + wrow, (isE || isD) ? 32 * 896 : 0);
#define LDW(i) wf[i] = ldnt(ro, oo, ((i) >> 1) * 8 * 896 + (((i) & 1) ? sB : sA))
    {
      const __amdgpu_buffer_rsrc_t r67 = mkrs(isE ? wo + wrow : w1, isE ? 32 * 896 : nb1);
      const int o67 = isE ? oo : fo;
      fr[6] = ldnt(r67, o67, isE ? sA : sb + 6 * pitch);
      fr[7] = ldnt(r67, o67, isE ? sB : sb + 7 * pitch);
      wf[0] = fr[6]; wf[1] = fr[7];
    }
    const int kvo = (h * SK + (SK - WIN) + j * 128) * 512;
    const int rot = ((b + (b >> 3) + (b >> 6)) & 7) * 8192;
    const int ko = k * 2048 + (n16 >> 2) * 512 + g4 * 64 + (lane & 3) * 16;
    const int vo = rs * 512 + c8 * 16 + k * 128;
    const __amdgpu_buffer_rsrc_t rk = mkrs(kc, 8 * SK * 512), rv = mkrs(vc, 8 * SK * 512);
#define LDK(i) kf[i] = ldnt(rk, ko + ((i) & 1) * 256, kvo + ((((i) >> 1) * 8192 + rot) & 0xffff))
#define LDV(i) vf[i] = ldnt(rv, vo, kvo + (((i) * 4096 + rot) & 0xffff))
    if (KFIRST) LDK(0);
    if (VFIRST * (1 - VLATE)) LDV(0);
#define LDS_(n) do { if ((n) < 16) LDV((n) < 16 ? (n) : 0); else LDW((n) >= 16 ? (n) - 16 + WEARLY : 2); } while (0)
    if (k == 0) STAMP(18);
    {
      float xv[8];
#pragma unroll
      for (int e = 0; e < 4; ++e) { xv[2 * e] = blo(xr[e]); xv[2 * e + 1] = bhi(xr[e]); }
      const float s1 = ((xv[0] + xv[1]) + (xv[2] + xv[3])) + ((xv[4] + xv[5]) + (xv[6] + xv[7]));
      const float mean = wave_sum(s1) * (1.f / DM);
      float s2 = 0.f;
#pragma unroll
      for (int e = 0; e < 8; ++e) { const float d = xv[e] - mean; s2 += d * d; }
      s2 = lane < 56 ? s2 : 0.f;
      const float var = wave_sum(s2) * (1.f / DM);
      const float mb = rbf(mean), rden = __builtin_amdgcn_rcpf(rbf(__builtin_amdgcn_sqrtf(rbf(rbf(var) + 1e-5f))));
      unsigned xc[8];
#pragma unroll
      for (int i = 0; i < 8; ++i) {
        const float xn = rbf(((i & 1) ? bhi(xs[i >> 1]) : blo(xs[i >> 1])) - mb) * rden;
        xc[i] = (i >= 6 && isE) ? 0u : pk2(xn, xn);
      }
      if (k == 0) STAMP(19);
      __builtin_amdgcn_sched_barrier(0);
      f32x4 a0 = {0.f, 0.f, 0.f, 0.f}, a1 = {0.f, 0.f, 0.f, 0.f};
#pragma unroll
      for (int i = 0; i < 8; ++i) {
        const v2u am = {xc[i] & msk2.x, xc[i] & msk2.y};
        a0 = mfma16(am, v2u{fr[i].x, fr[i].y}, a0);
        a1 = mfma16(am, v2u{fr[i].z, fr[i].w}, a1);
        __builtin_amdgcn_sched_barrier(0);
        if (i >= 8 - WINL) LDK(KFIRST + i - (8 - WINL));
      }
      float v0, v1;
      fold2<true>(a0, a1, nlo, v0, v1);
      if (lane < 32) *(LAS v2u*)(L + L_R1 + 64 * k + 2 * gidx) = v2u{asu(v0), asu(v1)};
      BARRIER();
      if (k == 0) STAMP(12);
    }
    if (VFIRST * VLATE) { VMW(WINL - 1); LDV(0); }
#pragma unroll
    for (int i = KFIRST + WINL; i < 16; ++i) { VMW(WINL - 1); LDK(i); }
#pragma unroll
    for (int i = VFIRST; i < QPOS; ++i) { VMW(WINL - 1); LDV(i); }
    unsigned tag;
    v4u q2[2];
    {
      const int qo = WS_Q + h * 8192 + k * 1024 + lane * 16;
      v4u qv[2];
      qv[0] = __builtin_amdgcn_raw_buffer_load_b128(rw, qo, (ABL & 1) ? OOB : 0, QLAUX);
      qv[1] = __builtin_amdgcn_raw_buffer_load_b128(rw, qo, (ABL & 3) ? OOB : 4096, QLAUX);
      __builtin_amdgcn_sched_barrier(0);
      if (lane == 0) FL[5] = 1u;
      if (k == 0) STAMP(17);
#pragma unroll
      for (int i = QPOS; i < QPOS + WINL; ++i) { VMW(WINL + 1); LDV(i); }
      lds_wait<false>(FL + 3, 1u);
      tag = __builtin_amdgcn_readfirstlane(FL[2]);
      VMW(WINL);
      const bool qok = (ABL & 7) ? (qv[0].y != 12345u || tag != 77u) : tags_ok<2>(qv, tag);
      q2[0] = ldsc(rw, qo, qok ? OOB : 0);
      q2[1] = ldsc(rw, qo, qok ? OOB : 4096);
      __builtin_amdgcn_sched_barrier(0);
#pragma unroll
      for (int i = QPOS + WINL; i < VB1; ++i) LDS_(i);
      if (k == 0) STAMP(13);
      unsigned np = 1;
      if (!qok) {
        qv[0] = q2[0]; qv[1] = q2[1]; ++np;
        if (!tags_ok<2>(qv, tag)) np += poll<2>(rw, qo, 4096, tag, qv);
      }
      if (k == 0) RAW(28, np);
      if (k == 0) STAMP(3);
      L[L_Q + 64 * k + lane] = pk2(asf(qv[0].x) + asf(qv[1].x), asf(qv[0].z) + asf(qv[1].z));
    }
    BARRIER();
#pragma unroll
    for (int i = VB1; i < VB2; ++i) LDS_(i);
    if (k == 0) STAMP(24);
    v4u q[4];
#pragma unroll
    for (int t = 0; t < 4; ++t) {
      const int qa = L_Q + ((lane >> 2) & 1) * 128 + (t & 1) * 64 + g4 * 16 + (lane & 3) * 4;
      q[t] = *(const LAS v4u*)(L + ((n16 >> 3) == (t >> 1) ? qa : L_Z));
    }
    float y[4];
#pragma unroll
    for (int t = 0; t < 4; ++t) {
      f32x4 d = {0.f, 0.f, 0.f, 0.f};
      d = mfma32(q[0], kf[2 * t], d); d = mfma32(q[1], kf[2 * t + 1], d);
      d = mfma32(q[2], kf[2 * t + 8], d); d = mfma32(q[3], kf[2 * t + 9], d);
      const float lo = (lane & 1) ? d[1] : d[0], hi = (lane & 1) ? d[3] : d[2];
      y[t] = (lane & 2) ? hi : lo;
    }
    {
      const float k0 = (lane & 2) ? y[2] : y[0], s0 = (lane & 2) ? y[0] : y[2], k1 = (lane & 2) ? y[3] : y[1], s1 = (lane & 2) ? y[1] : y[3];
      y[0] = k0 + dpp<XOR2>(s0); y[1] = k1 + dpp<XOR2>(s1);
    }
    const float sc = rbf(((lane & 1) ? y[1] : y[0]) + dpp<XOR1>((lane & 1) ? y[0] : y[1]));
    const float m = x32_max(row_max(sc));
    const int a = g4 & 1;
    if ((lane & 0x2f) == 0) L[L_WM + 2 * k + a] = asu(m);
    BARRIER();
#pragma unroll
    for (int i = VB2; i < VB3; ++i) LDS_(i);
    const float M = fmaxf(fmaxf(asf(L[L_WM + a]), asf(L[L_WM + 2 + a])), fmaxf(asf(L[L_WM + 4 + a]), asf(L[L_WM + 6 + a])));
    const float p = __builtin_amdgcn_exp2f((sc - M) * C2);
    L[L_P + a * 128 + (4 * (k & 1) + (n16 >> 2)) * 16 + 2 * ((lane & 3) + 4 * (g4 >> 1)) + (k >> 1)] = pk2(p, p);
    float l = row_sum(p); l = sw32(l, l);
    if ((lane & 0x2f) == 0) L[L_WL + 2 * k + a] = asu(l);
    BARRIER();
#pragma unroll
    for (int i = VB3; i < VB4; ++i) LDS_(i);
    if (k == 0) STAMP(9);
    {
      v4u pa[4];
#pragma unroll
      for (int t = 0; t < 4; ++t) pa[t] = *(const LAS v4u*)(L + L_P + (n16 >> 3) * 128 + sa * 16 + 4 * t);
      int od = WS_O + (h + 8 * j) * O_PITCH + (lane >> 5) * 1024 + k * 256 + (2 * c8 + (g4 & 1)) * 16 + (nlo ? 0 : OOB);
      asm volatile("" : "+v"(od));
      f32x4 a0 = {0.f, 0.f, 0.f, 0.f}, a1 = {0.f, 0.f, 0.f, 0.f};
#pragma unroll
      for (int i = 0; i < 16; ++i) {
        const unsigned c = pa[i >> 2][i & 3];
        const v2u am = {c & msk2.x, c & msk2.y};
        a0 = mfma16(am, v2u{vf[i].x, vf[i].y}, a0);
        a1 = mfma16(am, v2u{vf[i].z, vf[i].w}, a1);
        __builtin_amdgcn_sched_barrier(0);
        if (i == QPOS + WINL) asm volatile("" ::"v"(q2[0]), "v"(q2[1]));
        {
          const int nx = i + WOWIN;
          if (nx >= VB4 && nx < 16 + WPRE && nx < 24 - WEARLY && i < 15) LDS_(nx);
        }
      }
      const f32x4 o = fold4<true>(a0, a1);
      stsc(rw, od, pk2(o[0], o[1]), pk2(o[2], o[3]), tag);
      __builtin_amdgcn_sched_barrier(0);
    }
    if (lane == 0) FL[8] = 1u;
    if (k == 3) STAMP(14);
    if (k == 0) STAMP(15);
#pragma unroll
    for (int i = 16 + (WPRE < WOWIN - 1 ? WPRE : WOWIN - 1); i < 24 - WEARLY; ++i) LDS_(i);
    if (isE || isD) {
      v4u msk = {(c8 >> 1) == 0 ? hm : 0u, (c8 >> 1) == 1 ? hm : 0u, (c8 >> 1) == 2 ? hm : 0u, (c8 >> 1) == 3 ? hm : 0u};
      const int dead = (k < 3 || lane < 32) ? 0 : OOB;
      int sd = isE ? WS_S + (h * 16 + ei) * S_PITCH + (cA + (lane >> 5)) * 256 + (2 * c8 + (g4 & 1)) * 16 + (nlo ? dead : OOB)
                   : WS_F + u * F_PITCH + (cA + (lane >> 5)) * 512 + gidx * 16 + dead;
      int lw = L_OP + k * 32 + (4 * (c8 & 1) + g4) * 4 + (c8 >> 1) + (nlo ? 0 : 256), lr = L_OP + rs * 4;
      unsigned tv = tag;
      asm volatile("" : "+v"(msk), "+v"(sd), "+v"(lw), "+v"(lr), "+v"(tv));
      if (isE) {
        if (ODLY) __builtin_amdgcn_s_sleep(ODLY);
        v4u ov[1];
        const int po = WS_O + (h + 8 * (8 * k + rs)) * O_PITCH + ei * 128 + c8 * 16;
        ov[0] = ldsc(rw, po, 0);
        __builtin_amdgcn_sched_barrier(0);
        v4u ov2[1];
        if (ODLY2) { __builtin_amdgcn_s_sleep(ODLY2); CFENCE(); ov2[0] = ldsc(rw, po, 0); __builtin_amdgcn_sched_barrier(0); }
        lds_wait<false>(FL + 7, 1u);
        const float wt = asf(L[L_W + 8 * k + rs]);
        asm volatile("" ::"v"(wt));
        unsigned np2 = 1;
        if (!tags_ok<1>(ov, tag)) {
          if (ODLY2) ov[0] = ov2[0];
          if (!ODLY2 || !tags_ok<1>(ov, tag)) np2 += poll<1>(rw, po, 0, tag, ov);
        }
        if (k == 0) RAW(30, np2);
        if (k == 0) STAMP(7);
        L[lw] = asu(red4(wt * blo(ov[0].x), wt * bhi(ov[0].x), wt * blo(ov[0].z), wt * bhi(ov[0].z)));
        BARRIER();
        if (k == 0) STAMP(25);
        const f32x4 os = (__builtin_bit_cast(f32x4, *(const LAS v4u*)(L + lr)) + __builtin_bit_cast(f32x4, *(const LAS v4u*)(L + lr + 32))) +
                         (__builtin_bit_cast(f32x4, *(const LAS v4u*)(L + lr + 64)) + __builtin_bit_cast(f32x4, *(const LAS v4u*)(L + lr + 96)));
        f32x4 a0 = {0.f, 0.f, 0.f, 0.f}, a1 = {0.f, 0.f, 0.f, 0.f};
#pragma unroll
        for (int i = 0; i < 4; ++i) { const v4u am = amask(pk2(os[i], os[i]), msk); a0 = mfma32(am, wf[2 * i], a0); a1 = mfma32(am, wf[2 * i + 1], a1); }
        const f32x4 s = fold4<false>(a0, a1);
        stsc(rw, sd, pk2(s[0], s[1]), pk2(s[2], s[3]), tv);
        if (ODLY2) asm volatile("" ::"v"(ov2[0]));
        if (k == 0) STAMP(10);
      }
      CFENCE();
      if (isD) {
#pragma unroll
        for (int i = 0; i < WEARLY; ++i) LDW(i);
        BARRIER();
        const f32x4 os = __builtin_bit_cast(f32x4, *(const LAS v4u*)(L + lr));
        f32x4 a0 = {0.f, 0.f, 0.f, 0.f}, a1 = {0.f, 0.f, 0.f, 0.f};
#pragma unroll
        for (int i = 3; i >= 0; --i) { const v4u am = amask(pk2(os[i], os[i]), msk); a0 = mfma32(am, wf[2 * i], a0); a1 = mfma32(am, wf[2 * i + 1], a1); }
        float v0, v1;
        fold2<false>(a0, a1, nlo, v0, v1);
        stsc(rw, sd, asu(v0), asu(v1), tv);
        if (k == 0) STAMP(11);
      }
    } else if (fin) {
      int lw = L_OP + 32 * k + g4 * 8 + c8 + (nlo ? 0 : 256);
      asm volatile("" : "+v"(lw));
      v4u sv[4];
      const unsigned np3 = poll<4>(rw, WS_S + (32 * k + rs) * S_PITCH + fc * 128 + c8 * 16, 8 * S_PITCH, tag, sv); if (k == 0) RAW(31, np3); (void)np3;
      const float s = red4((blo(sv[0].x) + blo(sv[1].x)) + (blo(sv[2].x) + blo(sv[3].x)), (bhi(sv[0].x) + bhi(sv[1].x)) + (bhi(sv[2].x) + bhi(sv[3].x)),
                           (blo(sv[0].z) + blo(sv[1].z)) + (blo(sv[2].z) + blo(sv[3].z)), (bhi(sv[0].z) + bhi(sv[1].z)) + (bhi(sv[2].z) + bhi(sv[3].z)));
      L[lw] = asu(s);
      if (k == 0) STAMP(20); if (k == 1) STAMP(21); if (k == 2) STAMP(22); if (k == 3) STAMP(23);
      BARRIER();
    }
    STAMP_FLUSH();
  }
}

extern "C" void kernel_launch(void* const* d_in, const int* in_sizes, int n_in, void* d_out, int out_size, void* d_ws, size_t ws_size,
                              hipStream_t stream) {
  fused_decode<<<NWG, NTHR, 0, stream>>>(d_in[0], d_in[1], d_in[2], d_in[3], d_in[4], d_in[6], d_in[7], d_in[8], d_out, (char*)d_ws + WSOFF);
}
```

```cpp
#define PROBE 8
#define WOWIN 9
#include <hip/hip_runtime.h>
#include <stdint.h>

#define GAS __attribute__((address_space(1)))
#define LAS __attribute__((address_space(3)))
typedef unsigned v4u __attribute__((ext_vector_type(4)));
typedef unsigned v2u __attribute__((ext_vector_type(2)));
typedef float f32x4 __attribute__((ext_vector_type(4)));
typedef __bf16 bf16x8 __attribute__((ext_vector_type(8)));
typedef __bf16 bf16x2 __attribute__((ext_vector_type(2)));
typedef short s16x4 __attribute__((ext_vector_type(4)));
#define RLX_AGENT __ATOMIC_RELAXED, __HIP_MEMORY_SCOPE_AGENT

#define WINL 6
#define VB1 (10 + VFIRST)
#define VB2 (12 + VFIRST)
#define VB3 (14 + VFIRST)
#define VB4 VB3
#define QPOS VFIRST
#define ABL 0
#define QLAUX 16
#define ROT 1
#define OAUX 16
#define WPRE 6
#define ODLY2 0
#define WEARLY 2
#define KFIRST 0
#define VFIRST 0
#define PTOUCH 0
#define PFMODE 1
#define PFU 120
#define PFOFF 256
#define PFMASK 0xb8
#define TOUCH 0
#define VLATE 1
#define WSOFF 0
#define LTOT 1536
#define WMID 0
#define QSAUX 16
#define ODLY 12
#define CODE_BYTES 10496

constexpr int DM = 448, SK = 65536, WIN = 4096;
constexpr int NWG = 256, NTHR = 320;
constexpr int WS_Q = 4096;
constexpr int WS_GU = WS_Q + 8 * 8192;
constexpr int WS_ML = WS_GU + 28 * 2048;
constexpr int O_PITCH = 2176;
constexpr int WS_O = WS_ML + 8192;
constexpr int S_PITCH = 1792;
constexpr int WS_S = WS_O + 256 * O_PITCH;
constexpr int F_PITCH = 3584;
constexpr int WS_F = WS_S + 128 * S_PITCH;
constexpr int WS_END = WS_F + 56 * F_PITCH;
constexpr int OOB = 1 << 30;
constexpr int L_R1 = 0;
constexpr int L_Q = 256;
constexpr int L_P = 512;
constexpr int L_W = 800;
constexpr int L_WM = 832;
constexpr int L_WL = 840;
constexpr int L_FLAG = 928;
constexpr int L_Z = 944;
constexpr int L_OP = 960;
constexpr int L_TOTAL = LTOT;
constexpr unsigned SPIN_CAP = 1u << 16;

#define RAW(k, v)
#define STAMP(k) do { if ((k) == PROBE) stp = __builtin_amdgcn_s_memrealtime(); } while (0)
#define STAMP_DECL const unsigned long long st0 = __builtin_amdgcn_s_memrealtime(); unsigned long long stp = 0
#define STAMP_FLUSH() do { if (stp) { const unsigned long long tend = st0 + 3000 + 8 * (stp - st0); while (__builtin_amdgcn_s_memrealtime() < tend) __builtin_amdgcn_s_sleep(2); } } while (0)

__device__ __forceinline__ float asf(unsigned u) { return __builtin_bit_cast(float, u); }
__device__ __forceinline__ unsigned asu(float f) { return __builtin_bit_cast(unsigned, f); }
__device__ __forceinline__ float blo(unsigned u) { return asf(u << 16); }
__device__ __forceinline__ float bhi(unsigned u) { return asf(u & 0xffff0000u); }
__device__ __forceinline__ float rbf(float f) { return (float)(__bf16)f; }
__device__ __forceinline__ unsigned pk2(float lo, float hi) { bf16x2 t = {(__bf16)lo, (__bf16)hi}; return __builtin_bit_cast(unsigned, t); }
__device__ __forceinline__ f32x4 mfma32(v4u a, v4u b, f32x4 c) {
  return __builtin_amdgcn_mfma_f32_16x16x32_bf16(__builtin_bit_cast(bf16x8, a), __builtin_bit_cast(bf16x8, b), c, 0, 0, 0);
}
__device__ __forceinline__ f32x4 mfma16(v2u a, v2u b, f32x4 c) {
  return __builtin_amdgcn_mfma_f32_16x16x16bf16_1k(__builtin_bit_cast(s16x4, a), __builtin_bit_cast(s16x4, b), c, 0, 0, 0);
}
template <int CTRL> __device__ __forceinline__ float dpp(float x) {
  return asf((unsigned)__builtin_amdgcn_mov_dpp((int)asu(x), CTRL, 0xf, 0xf, true));
}
constexpr int XOR1 = 0xB1, XOR2 = 0x4E, XOR7 = 0x141, XOR8 = 0x128;
__device__ __forceinline__ float sw16(float a, float b) { auto s = __builtin_amdgcn_permlane16_swap(asu(a), asu(b), false, false); return asf(s[0]) + asf(s[1]); }
__device__ __forceinline__ float sw32(float a, float b) { auto s = __builtin_amdgcn_permlane32_swap(asu(a), asu(b), false, false); return asf(s[0]) + asf(s[1]); }
__device__ __forceinline__ float x16_max(float x) { auto s = __builtin_amdgcn_permlane16_swap(asu(x), asu(x), false, false); return fmaxf(asf(s[0]), asf(s[1])); }
__device__ __forceinline__ float x32_max(float x) { auto s = __builtin_amdgcn_permlane32_swap(asu(x), asu(x), false, false); return fmaxf(asf(s[0]), asf(s[1])); }
__device__ __forceinline__ float row_sum(float x) { x += dpp<XOR1>(x); x += dpp<XOR2>(x); x += dpp<XOR7>(x); x += dpp<XOR8>(x); return x; }
__device__ __forceinline__ float row_max(float x) { x = fmaxf(x, dpp<XOR1>(x)); x = fmaxf(x, dpp<XOR2>(x)); x = fmaxf(x, dpp<XOR7>(x)); return fmaxf(x, dpp<XOR8>(x)); }
__device__ __forceinline__ float wave_sum(float x) { x = row_sum(x); x = sw16(x, x); return sw32(x, x); }
__device__ __forceinline__ float red4(float v0, float v1, float v2, float v3) { const float t = sw16(sw32(v0, v2), sw32(v1, v3)); return t + dpp<XOR8>(t); }
__device__ __forceinline__ __amdgpu_buffer_rsrc_t mkrs(const GAS char* p, int bytes) { return __builtin_amdgcn_make_buffer_rsrc((void*)p, 0, bytes, 0x00020000); }
__device__ __forceinline__ v4u ldnt(__amdgpu_buffer_rsrc_t rs, int voff, int soff) {
  const v4u r = __builtin_amdgcn_raw_buffer_load_b128(rs, voff, soff, 2);
  __builtin_amdgcn_sched_barrier(0);
  return r;
}
__device__ __forceinline__ v4u ldsc(__amdgpu_buffer_rsrc_t rs, int voff, int soff) { return __builtin_amdgcn_raw_buffer_load_b128(rs, voff, soff, 16); }
template <int AUX = 16> __device__ __forceinline__ void stsc(__amdgpu_buffer_rsrc_t rs, int off, unsigned d0, unsigned d1, unsigned tag) {
  v4u g = {d0, tag, d1, tag};
  __builtin_amdgcn_raw_buffer_store_b128(g, rs, off, 0, AUX);
}
#define BARRIER() asm volatile("s_waitcnt lgkmcnt(0)\n\ts_barrier" ::: "memory")
#define CFENCE() asm volatile("" ::: "memory")
#define VMW(n) asm volatile("s_waitcnt vmcnt(%0)" ::"i"(n) : "memory")
__device__ __forceinline__ v4u amask(unsigned x, const v4u& m) { return v4u{x & m.x, x & m.y, x & m.z, x & m.w}; }
template <bool R16> __device__ __forceinline__ f32x4 fold4(const f32x4& A, const f32x4& B) {
  f32x4 s;
#pragma unroll
  for (int r = 0; r < 4; ++r) {
    auto t = R16 ? __builtin_amdgcn_permlane16_swap(asu(A[r]), asu(B[r]), false, false) : __builtin_amdgcn_permlane32_swap(asu(A[r]), asu(B[r]), false, false);
    s[r] = asf(t[0]) + dpp<XOR8>(asf(t[1]));
  }
  return s;
}
template <bool R16> __device__ __forceinline__ void fold2(const f32x4& A, const f32x4& B, bool nlo, float& v0, float& v1) {
  float s[4];
#pragma unroll
  for (int r = 0; r < 4; ++r) {
    auto t = R16 ? __builtin_amdgcn_permlane16_swap(asu(A[r]), asu(B[r]), false, false) : __builtin_amdgcn_permlane32_swap(asu(A[r]), asu(B[r]), false, false);
    const float a = asf(t[0]), b = asf(t[1]);
    s[r] = r < 2 ? a + dpp<XOR8>(b) : dpp<XOR8>(a) + b;
  }
  v0 = nlo ? s[0] : s[2];
  v1 = nlo ? s[1] : s[3];
}
template <int N> __device__ __forceinline__ bool tags_ok(const v4u (&v)[N], unsigned tag) {
  bool ok = true;
#pragma unroll
  for (int k = 0; k < N; ++k) ok &= (v[k].y == tag) & (v[k].w == tag);
  return __builtin_amdgcn_ballot_w64(!ok) == 0ull;
}
template <int N> __device__ __forceinline__ unsigned poll(__amdgpu_buffer_rsrc_t rs, int off, int stride, unsigned tag, v4u (&v)[N]) {
  unsigned spins = 0;
#pragma unroll 1
  for (; spins < SPIN_CAP; ++spins) {
    CFENCE();
#pragma unroll
    for (int k = 0; k < N; ++k) v[k] = ldsc(rs, off, k * stride);
    if (tags_ok<N>(v, tag)) break;
  }
  return spins + 1;
}
template <bool SLEEP> __device__ __forceinline__ void lds_wait(volatile LAS unsigned* w, unsigned want) {
#pragma unroll 1
  for (unsigned spins = 0; spins < (SPIN_CAP << 6); ++spins) {
    if (__builtin_amdgcn_readfirstlane(*w) >= want) break;
    if (SLEEP) __builtin_amdgcn_s_sleep(1);
  }
  CFENCE();
}
__device__ __forceinline__ float silu(float x) { return x * __builtin_amdgcn_rcpf(1.f + __expf(-x)); }

typedef unsigned long long v2q __attribute__((ext_vector_type(2)));
typedef unsigned long long v4q __attribute__((ext_vector_type(4)));
__global__ void __launch_bounds__(NTHR, 2) fused_decode(const void* x_, const void* kc_, const void* vc_, const void* wq_, const void* wo_, const void* wg_,
                                                     const void* wu_, const void* wd_, void* out_, void* ws_) {
  v4q ka, kb; v2q kd; unsigned long long pc;
  int b = blockIdx.x, tid = threadIdx.x;
  asm volatile("s_load_dwordx8 %0, %5, 0x0\n\ts_load_dwordx8 %1, %5, 0x20\n\ts_load_dwordx4 %2, %5, 0x40\n\ts_getpc_b64 %3"
               : "=&s"(ka), "=&s"(kb), "=&s"(kd), "=&s"(pc), "+s"(b) : "s"(__builtin_amdgcn_kernarg_segment_ptr()));
  asm volatile("" : "+v"(tid));
  __builtin_amdgcn_sched_barrier(0);
  __shared__ __attribute__((aligned(16))) unsigned lds_[L_TOTAL];
  LAS unsigned* L = (LAS unsigned*)lds_;
  volatile LAS unsigned* FL = (volatile LAS unsigned*)(L + L_FLAG);
  const int w = __builtin_amdgcn_readfirstlane(tid >> 6);
  STAMP_DECL;
  if (w == 0) STAMP(5);
  unsigned cpf = 0, cpf2 = 0;
  if (w == 4 && (b & PFMASK) == PFMASK) {
    const __amdgpu_buffer_rsrc_t rc = mkrs((const GAS char*)(pc & ~255ull), CODE_BYTES);
    cpf = __builtin_amdgcn_raw_buffer_load_b32(rc, (tid & 63) * 128, PFOFF, 0);
    cpf2 = __builtin_amdgcn_raw_buffer_load_b32(rc, (tid & 63) * 128, PFOFF + 8192, 0);
  }
  const bool e0 = (b & 64) == 0, q0 = b < 128;
  const int t0 = b & 63;
  const int NI = e0 ? 6 : 8;
  const int r0 = (e0 ? 256 : 0) + 8 * NI * w;
  const int pitch = q0 ? 8192 : 3584;
  const int sb = r0 * pitch + ((q0 || t0 < 28) ? t0 : t0 - 28) * 128;
  const int nb1 = (q0 || t0 < 56) ? DM * pitch : 0;
  const int sa = 2 * ((tid >> 4) & 3) + ((tid >> 2) & 1);
  const int fo = ((tid >> 3) & 7) * (NI * pitch) + (tid & 7) * 16, xo = (tid & 63) * 16, xso = sa * (NI * 2);
  asm volatile("" ::"s"(r0 * 2), "s"(pitch), "s"(sb), "s"(nb1), "v"(fo), "v"(xo), "v"(xso));
  __builtin_amdgcn_sched_barrier(0);
  if (w == 0) STAMP(27);
  asm volatile("s_waitcnt lgkmcnt(0)" : "+s"(ka), "+s"(kb), "+s"(kd));
  if (w == 0) STAMP(26);
  const GAS char *x = (const GAS char*)ka.x, *kc = (const GAS char*)ka.y, *vc = (const GAS char*)ka.z, *wq = (const GAS char*)ka.w;
  const GAS char *wo = (const GAS char*)kb.x, *wg = (const GAS char*)kb.y, *wu = (const GAS char*)kb.z, *wd = (const GAS char*)kb.w;
  GAS char *out = (GAS char*)kd.x, *ws = (GAS char*)kd.y;
  const __amdgpu_buffer_rsrc_t rx = mkrs(x, 896);
  typedef const __attribute__((address_space(4))) unsigned* cptr;
  unsigned ep, tk, tv_;
  asm volatile("s_cmp_lg_u32 %1, 4\n\ts_cbranch_scc1 1f\n\ts_load_dword %0, %2, 0x0\n1:" : "=&s"(ep) : "s"(w), "s"(ws) : "scc");
  tk = tv_ = 0;
  constexpr float C2 = 0.0625f * 1.4426950408889634f;
#define IDS() \
  asm volatile("" : "+s"(b)); \
  asm volatile("" : "+v"(tid)); \
  __builtin_amdgcn_sched_barrier(0); \
  const int h = (b >> 3) & 7, j = (b & 7) * 4 + (b >> 6); \
  const int lane = tid & 63; \
  const int u = (b & 63) + 64 * (b >> 7); \
  const bool isE = (j & 1) == 0; \
  const int ei = j >> 1; \
  const bool isD = !isE && u < 56; \
  const bool fin = !isE && u >= 114; \
  const int fc = u - 114; \
  const int rs = lane >> 3, c8 = lane & 7, g4 = lane >> 4, n16 = lane & 15; \
  const bool nlo = n16 < 8; \
  const int gidx = 4 * c8 + 2 * (g4 & 1) + (n16 >> 3); \
  const __amdgpu_buffer_rsrc_t rw = mkrs(ws, WS_END); \
  (void)rs; (void)gidx

  if (w == 4) {
    IDS();
    STAMP(0);
    __builtin_amdgcn_s_setprio(3);
    asm volatile("s_barrier\n\ts_waitcnt lgkmcnt(0)" : "+s"(ep) : "s"(tk), "s"(tv_) : "memory");
    const unsigned tag = ep + 1u;
    if (lane == 0) *(volatile LAS v2u*)(L + L_FLAG + 2) = v2u{tag, 1u};
    if (u < 120) {
      const v2u r0 = *(const LAS v2u*)(L + L_R1 + 2 * (lane & 31)), r1 = *(const LAS v2u*)(L + L_R1 + 64 + 2 * (lane & 31)),
                r2 = *(const LAS v2u*)(L + L_R1 + 128 + 2 * (lane & 31)), r3 = *(const LAS v2u*)(L + L_R1 + 192 + 2 * (lane & 31));
      const float v0 = (asf(r0.x) + asf(r1.x)) + (asf(r2.x) + asf(r3.x)), v1 = (asf(r0.y) + asf(r1.y)) + (asf(r2.y) + asf(r3.y));
      const int hf = isE ? 1 : 0;
      const int dst = u < 64 ? WS_Q + (u >> 3) * 8192 + hf * 4096 + (u & 7) * 512 : u < 92 ? WS_GU + (u - 64) * 2048 + hf * 512 : WS_GU + (u - 92) * 2048 + 1024 + hf * 512;
      if (lane < 32) stsc<QSAUX>(rw, dst + lane * 16, asu(v0), asu(v1), tag);
    }
    STAMP(2);
    unsigned pt0 = 0, pt1 = 0;
    if (PTOUCH) { pt0 = *(cptr)(unsigned long long)(wd + (u < 56 ? u : 0) * 32 * 896); pt1 = *(cptr)(unsigned long long)out; }
    const unsigned xw = __builtin_amdgcn_raw_buffer_load_b16(rx, (4 * c8 + g4) * 2, fin ? fc * 64 : 0, 0);
    const int go = WS_GU + (u >> 1) * 2048 + g4 * 512 + (u & 1) * 256 + n16 * 16;
    v4u gu[1];
    lds_wait<true>(FL + 5, 1u);
    gu[0] = ldsc(rw, go, isD ? 0 : OOB);
    asm volatile("" ::"s"(pt0), "s"(pt1));
    asm volatile("" ::"v"(cpf), "v"(cpf2));
    BARRIER();
    BARRIER();
    BARRIER();
    if (lane < 2) {
      const float M = fmaxf(fmaxf(asf(L[L_WM + lane]), asf(L[L_WM + 2 + lane])), fmaxf(asf(L[L_WM + 4 + lane]), asf(L[L_WM + 6 + lane])));
      const float Ls = (asf(L[L_WL + lane]) + asf(L[L_WL + 2 + lane])) + (asf(L[L_WL + 4 + lane]) + asf(L[L_WL + 6 + lane]));
      stsc(rw, WS_ML + (h * 2 + lane) * 512 + j * 16, asu(M), asu(Ls), tag);
    }
    STAMP(4);
    if (isE) {
      lds_wait<true>(FL + 8, 1u);
      v4u ml[1];
      const unsigned np1 = poll<1>(rw, WS_ML + (h * 2 + (ei >> 3)) * 512 + (lane & 31) * 16, 0, tag, ml); RAW(29, np1); (void)np1;
      const float m = asf(ml[0].x);
      const float M = x16_max(row_max(m));
      const float e = __builtin_amdgcn_exp2f((m - M) * C2);
      float Ls = row_sum(e * asf(ml[0].z)); Ls = sw16(Ls, Ls);
      if (lane < 32) L[L_W + lane] = asu(e * __builtin_amdgcn_rcpf(Ls));
      CFENCE();
      if (lane == 0) FL[7] = 1u;
      STAMP(6);
    } else if (isD) {
      if (!tags_ok<1>(gu, tag)) poll<1>(rw, go, 0, tag, gu);
      const float y = rbf(sw16(asf(gu[0].x), asf(gu[0].z)));
      auto t = __builtin_amdgcn_permlane32_swap(asu(y), asu(y), false, false);
      const float hv = rbf(rbf(silu(asf(t[0]))) * asf(t[1]));
      const int r = 2 * n16 + (g4 & 1);
      if (lane < 32) L[L_OP + (r & 7) * 4 + (r >> 3)] = asu(hv);
      STAMP(6);
      BARRIER();
    } else if (fin) {
      lds_wait<true>(FL + 8, 1u);
      v4u fv[14];
      poll<14>(rw, WS_F + g4 * F_PITCH + fc * 256 + n16 * 16, 4 * F_PITCH, tag, fv);
      float f0 = 0.f, f1 = 0.f;
#pragma unroll
      for (int i = 0; i < 14; ++i) { f0 += asf(fv[i].x); f1 += asf(fv[i].z); }
      float f = sw16(f0, f1); f = sw32(f, f);
      const float g = rbf(silu(rbf(f)));
      if (lane < 32) L[L_W + 2 * n16 + (g4 & 1)] = asu(g);
      asm volatile("s_waitcnt lgkmcnt(0)" ::: "memory");
      const float gc = asf(L[L_W + 4 * c8 + g4]);
      int so = L_OP + g4 * 8 + c8, oa = (4 * c8 + g4) * 2 + (nlo ? 0 : OOB);
      float xf = blo(xw);
      asm volatile("" : "+v"(so), "+v"(oa), "+v"(xf));
      STAMP(16);
      BARRIER();
      const float s = rbf((asf(L[so]) + asf(L[so + 32])) + (asf(L[so + 64]) + asf(L[so + 96])));
      const float res = xf + rbf(s * gc);
      __builtin_amdgcn_raw_buffer_store_b16((unsigned short)pk2(res, res), mkrs(out, 896), oa, fc * 64, OAUX);
      if (fc == 0 && lane == 0) __hip_atomic_store((GAS unsigned*)ws, tag, RLX_AGENT);
      STAMP(8);
    }
    STAMP_FLUSH();
  } else {
    const v4u xr = __builtin_amdgcn_raw_buffer_load_b128(rx, xo, 0, 0);
    const v4u xs = __builtin_amdgcn_raw_buffer_load_b128(rx, xso, r0 * 2, 0);
    __builtin_amdgcn_sched_barrier(0);
    if (w == 0) STAMP(1);
    v4u fr[8];
    const GAS char* w1 = q0 ? wq : t0 < 28 ? wg : wu;
    {
      const __amdgpu_buffer_rsrc_t ra = mkrs(w1, nb1);
#pragma unroll
      for (int i = 0; i < 6; ++i) fr[i] = ldnt(ra, fo, sb + i * pitch);
    }
    IDS();
    const int k = w;
    const unsigned hm = (lane & 1) ? 0xffff0000u : 0x0000ffffu;
    v4u kf[16], vf[16], wf[8];
    if (tid < 20) FL[tid] = 0u;
    const int wre = 512 * h + 32 * ei, wrd = u < 56 ? 32 * u : 0;
    const int wrow = (wrd + ((wre - wrd) & ((j & 1) - 1))) * 896;
    const int oo = rs * 896 + c8 * 16;
    const v2u msk2 = {(lane & 2) ? 0u : hm, (lane & 2) ? hm : 0u};
    const int cA = 2 * k, sA = cA * 128, sB = k < 3 ? sA + 128 : OOB;
    const __amdgpu_buffer_rsrc_t ro = mkrs((isE ? wo : wd) + wrow, (isE || isD) ? 32 * 896 : 0);
#define LDW(i) wf[i] = ldnt(ro, oo, ((i) >> 1) * 8 * 896 + (((i) & 1) ? sB : sA))
    {
      const __amdgpu_buffer_rsrc_t r67 = mkrs(isE ? wo + wrow : w1, isE ? 32 * 896 : nb1);
      const int o67 = isE ? oo : fo;
      fr[6] = ldnt(r67, o67, isE ? sA : sb + 6 * pitch);
      fr[7] = ldnt(r67, o67, isE ? sB : sb + 7 * pitch);
      wf[0] = fr[6]; wf[1] = fr[7];
    }
    const int kvo = (h * SK + (SK - WIN) + j * 128) * 512;
    const int rot = ((b + (b >> 3) + (b >> 6)) & 7) * 8192;
    const int ko = k * 2048 + (n16 >> 2) * 512 + g4 * 64 + (lane & 3) * 16;
    const int vo = rs * 512 + c8 * 16 + k * 128;
    const __amdgpu_buffer_rsrc_t rk = mkrs(kc, 8 * SK * 512), rv = mkrs(vc, 8 * SK * 512);
#define LDK(i) kf[i] = ldnt(rk, ko + ((i) & 1) * 256, kvo + ((((i) >> 1) * 8192 + rot) & 0xffff))
#define LDV(i) vf[i] = ldnt(rv, vo, kvo + (((i) * 4096 + rot) & 0xffff))
    if (KFIRST) LDK(0);
    if (VFIRST * (1 - VLATE)) LDV(0);
#define LDS_(n) do { if ((n) < 16) LDV((n) < 16 ? (n) : 0); else LDW((n) >= 16 ? (n) - 16 + WEARLY + WMID : 2); } while (0)
    if (k == 0) STAMP(18);
    {
      float xv[8];
#pragma unroll
      for (int e = 0; e < 4; ++e) { xv[2 * e] = blo(xr[e]); xv[2 * e + 1] = bhi(xr[e]); }
      const float s1 = ((xv[0] + xv[1]) + (xv[2] + xv[3])) + ((xv[4] + xv[5]) + (xv[6] + xv[7]));
      const float mean = wave_sum(s1) * (1.f / DM);
      float s2 = 0.f;
#pragma unroll
      for (int e = 0; e < 8; ++e) { const float d = xv[e] - mean; s2 += d * d; }
      s2 = lane < 56 ? s2 : 0.f;
      const float var = wave_sum(s2) * (1.f / DM);
      const float mb = rbf(mean), rden = __builtin_amdgcn_rcpf(rbf(__builtin_amdgcn_sqrtf(rbf(rbf(var) + 1e-5f))));
      unsigned xc[8];
#pragma unroll
      for (int i = 0; i < 8; ++i) {
        const float xn = rbf(((i & 1) ? bhi(xs[i >> 1]) : blo(xs[i >> 1])) - mb) * rden;
        xc[i] = (i >= 6 && isE) ? 0u : pk2(xn, xn);
      }
      if (k == 0) STAMP(19);
      __builtin_amdgcn_sched_barrier(0);
      f32x4 a0 = {0.f, 0.f, 0.f, 0.f}, a1 = {0.f, 0.f, 0.f, 0.f};
#pragma unroll
      for (int i = 0; i < 8; ++i) {
        const v2u am = {xc[i] & msk2.x, xc[i] & msk2.y};
        a0 = mfma16(am, v2u{fr[i].x, fr[i].y}, a0);
        a1 = mfma16(am, v2u{fr[i].z, fr[i].w}, a1);
        __builtin_amdgcn_sched_barrier(0);
        if (i >= 8 - WINL) LDK(KFIRST + i - (8 - WINL));
      }
      float v0, v1;
      fold2<true>(a0, a1, nlo, v0, v1);
      if (lane < 32) *(LAS v2u*)(L + L_R1 + 64 * k + 2 * gidx) = v2u{asu(v0), asu(v1)};
      BARRIER();
      if (k == 0) STAMP(12);
    }
    if (VFIRST * VLATE) { VMW(WINL - 1); LDV(0); }
#pragma unroll
    for (int i = KFIRST + WINL; i < 16; ++i) { VMW(WINL - 1); LDK(i); }
#pragma unroll
    for (int i = VFIRST; i < QPOS; ++i) { VMW(WINL - 1); LDV(i); }
    unsigned tag;
    v4u q2[2];
    {
      const int qo = WS_Q + h * 8192 + k * 1024 + lane * 16;
      v4u qv[2];
      qv[0] = __builtin_amdgcn_raw_buffer_load_b128(rw, qo, (ABL & 1) ? OOB : 0, QLAUX);
      qv[1] = __builtin_amdgcn_raw_buffer_load_b128(rw, qo, (ABL & 3) ? OOB : 4096, QLAUX);
      __builtin_amdgcn_sched_barrier(0);
      if (lane == 0) FL[5] = 1u;
      if (k == 0) STAMP(17);
#pragma unroll
      for (int i = QPOS; i < QPOS + WINL; ++i) { VMW(WINL + 1); LDV(i); }
      lds_wait<false>(FL + 3, 1u);
      tag = __builtin_amdgcn_readfirstlane(FL[2]);
      VMW(WINL);
      const bool qok = (ABL & 7) ? (qv[0].y != 12345u || tag != 77u) : tags_ok<2>(qv, tag);
      q2[0] = ldsc(rw, qo, qok ? OOB : 0);
      q2[1] = ldsc(rw, qo, qok ? OOB : 4096);
      __builtin_amdgcn_sched_barrier(0);
#pragma unroll
      for (int i = QPOS + WINL; i < VB1; ++i) LDS_(i);
      if (k == 0) STAMP(13);
      unsigned np = 1;
      if (!qok) {
        qv[0] = q2[0]; qv[1] = q2[1]; ++np;
        if (!tags_ok<2>(qv, tag)) np += poll<2>(rw, qo, 4096, tag, qv);
      }
      if (k == 0) RAW(28, np);
      if (k == 0) STAMP(3);
      L[L_Q + 64 * k + lane] = pk2(asf(qv[0].x) + asf(qv[1].x), asf(qv[0].z) + asf(qv[1].z));
    }
    BARRIER();
#pragma unroll
    for (int i = VB1; i < VB2; ++i) LDS_(i);
    if (k == 0) STAMP(24);
    v4u q[4];
#pragma unroll
    for (int t = 0; t < 4; ++t) {
      const int qa = L_Q + ((lane >> 2) & 1) * 128 + (t & 1) * 64 + g4 * 16 + (lane & 3) * 4;
      q[t] = *(const LAS v4u*)(L + ((n16 >> 3) == (t >> 1) ? qa : L_Z));
    }
    float y[4];
#pragma unroll
    for (int t = 0; t < 4; ++t) {
      f32x4 d = {0.f, 0.f, 0.f, 0.f};
      d = mfma32(q[0], kf[2 * t], d); d = mfma32(q[1], kf[2 * t + 1], d);
      d = mfma32(q[2], kf[2 * t + 8], d); d = mfma32(q[3], kf[2 * t + 9], d);
      const float lo = (lane & 1) ? d[1] : d[0], hi = (lane & 1) ? d[3] : d[2];
      y[t] = (lane & 2) ? hi : lo;
    }
    {
      const float k0 = (lane & 2) ? y[2] : y[0], s0 = (lane & 2) ? y[0] : y[2], k1 = (lane & 2) ? y[3] : y[1], s1 = (lane & 2) ? y[1] : y[3];
      y[0] = k0 + dpp<XOR2>(s0); y[1] = k1 + dpp<XOR2>(s1);
    }
    const float sc = rbf(((lane & 1) ? y[1] : y[0]) + dpp<XOR1>((lane & 1) ? y[0] : y[1]));
    const float m = x32_max(row_max(sc));
    const int a = g4 & 1;
    if ((lane & 0x2f) == 0) L[L_WM + 2 * k + a] = asu(m);
    BARRIER();
#pragma unroll
    for (int i = VB2; i < VB3; ++i) LDS_(i);
    const float M = fmaxf(fmaxf(asf(L[L_WM + a]), asf(L[L_WM + 2 + a])), fmaxf(asf(L[L_WM + 4 + a]), asf(L[L_WM + 6 + a])));
    const float p = __builtin_amdgcn_exp2f((sc - M) * C2);
    L[L_P + a * 128 + (4 * (k & 1) + (n16 >> 2)) * 16 + 2 * ((lane & 3) + 4 * (g4 >> 1)) + (k >> 1)] = pk2(p, p);
    float l = row_sum(p); l = sw32(l, l);
    if ((lane & 0x2f) == 0) L[L_WL + 2 * k + a] = asu(l);
    BARRIER();
#pragma unroll
    for (int i = VB3; i < VB4; ++i) LDS_(i);
#pragma unroll
    for (int i = 0; i < WMID; ++i) LDW(WEARLY + i);
    if (k == 0) STAMP(9);
    {
      v4u pa[4];
#pragma unroll
      for (int t = 0; t < 4; ++t) pa[t] = *(const LAS v4u*)(L + L_P + (n16 >> 3) * 128 + sa * 16 + 4 * t);
      int od = WS_O + (h + 8 * j) * O_PITCH + (lane >> 5) * 1024 + k * 256 + (2 * c8 + (g4 & 1)) * 16 + (nlo ? 0 : OOB);
      asm volatile("" : "+v"(od));
      f32x4 a0 = {0.f, 0.f, 0.f, 0.f}, a1 = {0.f, 0.f, 0.f, 0.f};
#pragma unroll
      for (int i = 0; i < 16; ++i) {
        const unsigned c = pa[i >> 2][i & 3];
        const v2u am = {c & msk2.x, c & msk2.y};
        a0 = mfma16(am, v2u{vf[i].x, vf[i].y}, a0);
        a1 = mfma16(am, v2u{vf[i].z, vf[i].w}, a1);
        __builtin_amdgcn_sched_barrier(0);
        if (i == QPOS + WINL) asm volatile("" ::"v"(q2[0]), "v"(q2[1]));
        {
          const int nx = i + WOWIN;
          if (nx >= VB4 && nx < 16 + WPRE && nx < 24 - WEARLY - WMID && i < 15) LDS_(nx);
        }
      }
      const f32x4 o = fold4<true>(a0, a1);
      stsc(rw, od, pk2(o[0], o[1]), pk2(o[2], o[3]), tag);
      __builtin_amdgcn_sched_barrier(0);
    }
    if (lane == 0) FL[8] = 1u;
    if (k == 3) STAMP(14);
    if (k == 0) STAMP(15);
#pragma unroll
    for (int i = 16 + (WPRE < WOWIN - 1 ? WPRE : WOWIN - 1); i < 24 - WEARLY - WMID; ++i) LDS_(i);
    if (isE || isD) {
      v4u msk = {(c8 >> 1) == 0 ? hm : 0u, (c8 >> 1) == 1 ? hm : 0u, (c8 >> 1) == 2 ? hm : 0u, (c8 >> 1) == 3 ? hm : 0u};
      const int dead = (k < 3 || lane < 32) ? 0 : OOB;
      int sd = isE ? WS_S + (h * 16 + ei) * S_PITCH + (cA + (lane >> 5)) * 256 + (2 * c8 + (g4 & 1)) * 16 + (nlo ? dead : OOB)
                   : WS_F + u * F_PITCH + (cA + (lane >> 5)) * 512 + gidx * 16 + dead;
      int lw = L_OP + k * 32 + (4 * (c8 & 1) + g4) * 4 + (c8 >> 1) + (nlo ? 0 : 256), lr = L_OP + rs * 4;
      unsigned tv = tag;
      asm volatile("" : "+v"(msk), "+v"(sd), "+v"(lw), "+v"(lr), "+v"(tv));
      if (isE) {
        if (ODLY) __builtin_amdgcn_s_sleep(ODLY);
        v4u ov[1];
        const int po = WS_O + (h + 8 * (8 * k + rs)) * O_PITCH + ei * 128 + c8 * 16;
        ov[0] = ldsc(rw, po, 0);
        __builtin_amdgcn_sched_barrier(0);
        v4u ov2[1];
        if (ODLY2) { __builtin_amdgcn_s_sleep(ODLY2); CFENCE(); ov2[0] = ldsc(rw, po, 0); __builtin_amdgcn_sched_barrier(0); }
        lds_wait<false>(FL + 7, 1u);
        const float wt = asf(L[L_W + 8 * k + rs]);
        asm volatile("" ::"v"(wt));
        unsigned np2 = 1;
        if (!tags_ok<1>(ov, tag)) {
          if (ODLY2) ov[0] = ov2[0];
          if (!ODLY2 || !tags_ok<1>(ov, tag)) np2 += poll<1>(rw, po, 0, tag, ov);
        }
        if (k == 0) RAW(30, np2);
        if (k == 0) STAMP(7);
        L[lw] = asu(red4(wt * blo(ov[0].x), wt * bhi(ov[0].x), wt * blo(ov[0].z), wt * bhi(ov[0].z)));
        BARRIER();
        if (k == 0) STAMP(25);
        const f32x4 os = (__builtin_bit_cast(f32x4, *(const LAS v4u*)(L + lr)) + __builtin_bit_cast(f32x4, *(const LAS v4u*)(L + lr + 32))) +
                         (__builtin_bit_cast(f32x4, *(const LAS v4u*)(L + lr + 64)) + __builtin_bit_cast(f32x4, *(const LAS v4u*)(L + lr + 96)));
        f32x4 a0 = {0.f, 0.f, 0.f, 0.f}, a1 = {0.f, 0.f, 0.f, 0.f};
#pragma unroll
        for (int i = 0; i < 4; ++i) { const v4u am = amask(pk2(os[i], os[i]), msk); a0 = mfma32(am, wf[2 * i], a0); a1 = mfma32(am, wf[2 * i + 1], a1); }
        const f32x4 s = fold4<false>(a0, a1);
        stsc(rw, sd, pk2(s[0], s[1]), pk2(s[2], s[3]), tv);
        if (ODLY2) asm volatile("" ::"v"(ov2[0]));
        if (k == 0) STAMP(10);
      }
      CFENCE();
      if (isD) {
#pragma unroll
        for (int i = 0; i < WEARLY; ++i) LDW(i);
        BARRIER();
        const f32x4 os = __builtin_bit_cast(f32x4, *(const LAS v4u*)(L + lr));
        f32x4 a0 = {0.f, 0.f, 0.f, 0.f}, a1 = {0.f, 0.f, 0.f, 0.f};
#pragma unroll
        for (int i = 3; i >= 0; --i) { const v4u am = amask(pk2(os[i], os[i]), msk); a0 = mfma32(am, wf[2 * i], a0); a1 = mfma32(am, wf[2 * i + 1], a1); }
        float v0, v1;
        fold2<false>(a0, a1, nlo, v0, v1);
        stsc(rw, sd, asu(v0), asu(v1), tv);
        if (k == 0) STAMP(11);
      }
    } else if (fin) {
      int lw = L_OP + 32 * k + g4 * 8 + c8 + (nlo ? 0 : 256);
      asm volatile("" : "+v"(lw));
      v4u sv[4];
      const unsigned np3 = poll<4>(rw, WS_S + (32 * k + rs) * S_PITCH + fc * 128 + c8 * 16, 8 * S_PITCH, tag, sv); if (k == 0) RAW(31, np3); (void)np3;
      const float s = red4((blo(sv[0].x) + blo(sv[1].x)) + (blo(sv[2].x) + blo(sv[3].x)), (bhi(sv[0].x) + bhi(sv[1].x)) + (bhi(sv[2].x) + bhi(sv[3].x)),
                           (blo(sv[0].z) + blo(sv[1].z)) + (blo(sv[2].z) + blo(sv[3].z)), (bhi(sv[0].z) + bhi(sv[1].z)) + (bhi(sv[2].z) + bhi(sv[3].z)));
      L[lw] = asu(s);
      if (k == 0) STAMP(20); if (k == 1) STAMP(21); if (k == 2) STAMP(22); if (k == 3) STAMP(23);
      BARRIER();
    }
    STAMP_FLUSH();
  }
}

extern "C" void kernel_launch(void* const* d_in, const int* in_sizes, int n_in, void* d_out, int out_size, void* d_ws, size_t ws_size,
                              hipStream_t stream) {
  fused_decode<<<NWG, NTHR, 0, stream>>>(d_in[0], d_in[1], d_in[2], d_in[3], d_in[4], d_in[6], d_in[7], d_in[8], d_out, (char*)d_ws + WSOFF);
}
```

```cpp
#define PROBE 8
#define VB1 16
#define VB2 16
#define VB3 16
#include <hip/hip_runtime.h>
#include <stdint.h>

#define GAS __attribute__((address_space(1)))
#define LAS __attribute__((address_space(3)))
typedef unsigned v4u __attribute__((ext_vector_type(4)));
typedef unsigned v2u __attribute__((ext_vector_type(2)));
typedef float f32x4 __attribute__((ext_vector_type(4)));
typedef __bf16 bf16x8 __attribute__((ext_vector_type(8)));
typedef __bf16 bf16x2 __attribute__((ext_vector_type(2)));
typedef short s16x4 __attribute__((ext_vector_type(4)));
#define RLX_AGENT __ATOMIC_RELAXED, __HIP_MEMORY_SCOPE_AGENT

#define WINL 6
#define VB4 VB3
#define QPOS VFIRST
#define ABL 0
#define QLAUX 16
#define ROT 1
#define OAUX 16
#define WPRE 6
#define ODLY2 0
#define WEARLY 2
#define KFIRST 0
#define VFIRST 0
#define PTOUCH 0
#define PFMODE 1
#define PFU 120
#define PFOFF 256
#define PFMASK 0xb8
#define TOUCH 0
#define VLATE 1
#define WSOFF 0
#define LTOT 1536
#define WMID 0
#define WOWIN 7
#define QSAUX 16
#define ODLY 12
#define CODE_BYTES 10496

constexpr int DM = 448, SK = 65536, WIN = 4096;
constexpr int NWG = 256, NTHR = 320;
constexpr int WS_Q = 4096;
constexpr int WS_GU = WS_Q + 8 * 8192;
constexpr int WS_ML = WS_GU + 28 * 2048;
constexpr int O_PITCH = 2176;
constexpr int WS_O = WS_ML + 8192;
constexpr int S_PITCH = 1792;
constexpr int WS_S = WS_O + 256 * O_PITCH;
constexpr int F_PITCH = 3584;
constexpr int WS_F = WS_S + 128 * S_PITCH;
constexpr int WS_END = WS_F + 56 * F_PITCH;
constexpr int OOB = 1 << 30;
constexpr int L_R1 = 0;
constexpr int L_Q = 256;
constexpr int L_P = 512;
constexpr int L_W = 800;
constexpr int L_WM = 832;
constexpr int L_WL = 840;
constexpr int L_FLAG = 928;
constexpr int L_Z = 944;
constexpr int L_OP = 960;
constexpr int L_TOTAL = LTOT;
constexpr unsigned SPIN_CAP = 1u << 16;

#define RAW(k, v)
#define STAMP(k) do { if ((k) == PROBE) stp = __builtin_amdgcn_s_memrealtime(); } while (0)
#define STAMP_DECL const unsigned long long st0 = __builtin_amdgcn_s_memrealtime(); unsigned long long stp = 0
#define STAMP_FLUSH() do { if (stp) { const unsigned long long tend = st0 + 3000 + 8 * (stp - st0); while (__builtin_amdgcn_s_memrealtime() < tend) __builtin_amdgcn_s_sleep(2); } } while (0)

__device__ __forceinline__ float asf(unsigned u) { return __builtin_bit_cast(float, u); }
__device__ __forceinline__ unsigned asu(float f) { return __builtin_bit_cast(unsigned, f); }
__device__ __forceinline__ float blo(unsigned u) { return asf(u << 16); }
__device__ __forceinline__ float bhi(unsigned u) { return asf(u & 0xffff0000u); }
__device__ __forceinline__ float rbf(float f) { return (float)(__bf16)f; }
__device__ __forceinline__ unsigned pk2(float lo, float hi) { bf16x2 t = {(__bf16)lo, (__bf16)hi}; return __builtin_bit_cast(unsigned, t); }
__device__ __forceinline__ f32x4 mfma32(v4u a, v4u b, f32x4 c) {
  return __builtin_amdgcn_mfma_f32_16x16x32_bf16(__builtin_bit_cast(bf16x8, a), __builtin_bit_cast(bf16x8, b), c, 0, 0, 0);
}
__device__ __forceinline__ f32x4 mfma16(v2u a, v2u b, f32x4 c) {
  return __builtin_amdgcn_mfma_f32_16x16x16bf16_1k(__builtin_bit_cast(s16x4, a), __builtin_bit_cast(s16x4, b), c, 0, 0, 0);
}
template <int CTRL> __device__ __forceinline__ float dpp(float x) {
  return asf((unsigned)__builtin_amdgcn_mov_dpp((int)asu(x), CTRL, 0xf, 0xf, true));
}
constexpr int XOR1 = 0xB1, XOR2 = 0x4E, XOR7 = 0x141, XOR8 = 0x128;
__device__ __forceinline__ float sw16(float a, float b) { auto s = __builtin_amdgcn_permlane16_swap(asu(a), asu(b), false, false); return asf(s[0]) + asf(s[1]); }
__device__ __forceinline__ float sw32(float a, float b) { auto s = __builtin_amdgcn_permlane32_swap(asu(a), asu(b), false, false); return asf(s[0]) + asf(s[1]); }
__device__ __forceinline__ float x16_max(float x) { auto s = __builtin_amdgcn_permlane16_swap(asu(x), asu(x), false, false); return fmaxf(asf(s[0]), asf(s[1])); }
__device__ __forceinline__ float x32_max(float x) { auto s = __builtin_amdgcn_permlane32_swap(asu(x), asu(x), false, false); return fmaxf(asf(s[0]), asf(s[1])); }
__device__ __forceinline__ float row_sum(float x) { x += dpp<XOR1>(x); x += dpp<XOR2>(x); x += dpp<XOR7>(x); x += dpp<XOR8>(x); return x; }
__device__ __forceinline__ float row_max(float x) { x = fmaxf(x, dpp<XOR1>(x)); x = fmaxf(x, dpp<XOR2>(x)); x = fmaxf(x, dpp<XOR7>(x)); return fmaxf(x, dpp<XOR8>(x)); }
__device__ __forceinline__ float wave_sum(float x) { x = row_sum(x); x = sw16(x, x); return sw32(x, x); }
__device__ __forceinline__ float red4(float v0, float v1, float v2, float v3) { const float t = sw16(sw32(v0, v2), sw32(v1, v3)); return t + dpp<XOR8>(t); }
__device__ __forceinline__ __amdgpu_buffer_rsrc_t mkrs(const GAS char* p, int bytes) { return __builtin_amdgcn_make_buffer_rsrc((void*)p, 0, bytes, 0x00020000); }
__device__ __forceinline__ v4u ldnt(__amdgpu_buffer_rsrc_t rs, int voff, int soff) {
  const v4u r = __builtin_amdgcn_raw_buffer_load_b128(rs, voff, soff, 2);
  __builtin_amdgcn_sched_barrier(0);
  return r;
}
__device__ __forceinline__ v4u ldsc(__amdgpu_buffer_rsrc_t rs, int voff, int soff) { return __builtin_amdgcn_raw_buffer_load_b128(rs, voff, soff, 16); }
template <int AUX = 16> __device__ __forceinline__ void stsc(__amdgpu_buffer_rsrc_t rs, int off, unsigned d0, unsigned d1, unsigned tag) {
  v4u g = {d0, tag, d1, tag};
  __builtin_amdgcn_raw_buffer_store_b128(g, rs, off, 0, AUX);
}
#define BARRIER() asm volatile("s_waitcnt lgkmcnt(0)\n\ts_barrier" ::: "memory")
#define CFENCE() asm volatile("" ::: "memory")
#define VMW(n) asm volatile("s_waitcnt vmcnt(%0)" ::"i"(n) : "memory")
__device__ __forceinline__ v4u amask(unsigned x, const v4u& m) { return v4u{x & m.x, x & m.y, x & m.z, x & m.w}; }
template <bool R16> __device__ __forceinline__ f32x4 fold4(const f32x4& A, const f32x4& B) {
  f32x4 s;
#pragma unroll
  for (int r = 0; r < 4; ++r) {
    auto t = R16 ? __builtin_amdgcn_permlane16_swap(asu(A[r]), asu(B[r]), false, false) : __builtin_amdgcn_permlane32_swap(asu(A[r]), asu(B[r]), false, false);
    s[r] = asf(t[0]) + dpp<XOR8>(asf(t[1]));
  }
  return s;
}
template <bool R16> __device__ __forceinline__ void fold2(const f32x4& A, const f32x4& B, bool nlo, float& v0, float& v1) {
  float s[4];
#pragma unroll
  for (int r = 0; r < 4; ++r) {
    auto t = R16 ? __builtin_amdgcn_permlane16_swap(asu(A[r]), asu(B[r]), false, false) : __builtin_amdgcn_permlane32_swap(asu(A[r]), asu(B[r]), false, false);
    const float a = asf(t[0]), b = asf(t[1]);
    s[r] = r < 2 ? a + dpp<XOR8>(b) : dpp<XOR8>(a) + b;
  }
  v0 = nlo ? s[0] : s[2];
  v1 = nlo ? s[1] : s[3];
}
template <int N> __device__ __forceinline__ bool tags_ok(const v4u (&v)[N], unsigned tag) {
  bool ok = true;
#pragma unroll
  for (int k = 0; k < N; ++k) ok &= (v[k].y == tag) & (v[k].w == tag);
  return __builtin_amdgcn_ballot_w64(!ok) == 0ull;
}
template <int N> __device__ __forceinline__ unsigned poll(__amdgpu_buffer_rsrc_t rs, int off, int stride, unsigned tag, v4u (&v)[N]) {
  unsigned spins = 0;
#pragma unroll 1
  for (; spins < SPIN_CAP; ++spins) {
    CFENCE();
#pragma unroll
    for (int k = 0; k < N; ++k) v[k] = ldsc(rs, off, k * stride);
    if (tags_ok<N>(v, tag)) break;
  }
  return spins + 1;
}
template <bool SLEEP> __device__ __forceinline__ void lds_wait(volatile LAS unsigned* w, unsigned want) {
#pragma unroll 1
  for (unsigned spins = 0; spins < (SPIN_CAP << 6); ++spins) {
    if (__builtin_amdgcn_readfirstlane(*w) >= want) break;
    if (SLEEP) __builtin_amdgcn_s_sleep(1);
  }
  CFENCE();
}
__device__ __forceinline__ float silu(float x) { return x * __builtin_amdgcn_rcpf(1.f + __expf(-x)); }

typedef unsigned long long v2q __attribute__((ext_vector_type(2)));
typedef unsigned long long v4q __attribute__((ext_vector_type(4)));
__global__ void __launch_bounds__(NTHR, 2) fused_decode(const void* x_, const void* kc_, const void* vc_, const void* wq_, const void* wo_, const void* wg_,
                                                     const void* wu_, const void* wd_, void* out_, void* ws_) {
  v4q ka, kb; v2q kd; unsigned long long pc;
  int b = blockIdx.x, tid = threadIdx.x;
  asm volatile("s_load_dwordx8 %0, %5, 0x0\n\ts_load_dwordx8 %1, %5, 0x20\n\ts_load_dwordx4 %2, %5, 0x40\n\ts_getpc_b64 %3"
               : "=&s"(ka), "=&s"(kb), "=&s"(kd), "=&s"(pc), "+s"(b) : "s"(__builtin_amdgcn_kernarg_segment_ptr()));
  asm volatile("" : "+v"(tid));
  __builtin_amdgcn_sched_barrier(0);
  __shared__ __attribute__((aligned(16))) unsigned lds_[L_TOTAL];
  LAS unsigned* L = (LAS unsigned*)lds_;
  volatile LAS unsigned* FL = (volatile LAS unsigned*)(L + L_FLAG);
  const int w = __builtin_amdgcn_readfirstlane(tid >> 6);
  STAMP_DECL;
  if (w == 0) STAMP(5);
  unsigned cpf = 0, cpf2 = 0;
  if (w == 4 && (b & PFMASK) == PFMASK) {
    const __amdgpu_buffer_rsrc_t rc = mkrs((const GAS char*)(pc & ~255ull), CODE_BYTES);
    cpf = __builtin_amdgcn_raw_buffer_load_b32(rc, (tid & 63) * 128, PFOFF, 0);
    cpf2 = __builtin_amdgcn_raw_buffer_load_b32(rc, (tid & 63) * 128, PFOFF + 8192, 0);
  }
  const bool e0 = (b & 64) == 0, q0 = b < 128;
  const int t0 = b & 63;
  const int NI = e0 ? 6 : 8;
  const int r0 = (e0 ? 256 : 0) + 8 * NI * w;
  const int pitch = q0 ? 8192 : 3584;
  const int sb = r0 * pitch + ((q0 || t0 < 28) ? t0 : t0 - 28) * 128;
  const int nb1 = (q0 || t0 < 56) ? DM * pitch : 0;
  const int sa = 2 * ((tid >> 4) & 3) + ((tid >> 2) & 1);
  const int fo = ((tid >> 3) & 7) * (NI * pitch) + (tid & 7) * 16, xo = (tid & 63) * 16, xso = sa * (NI * 2);
  asm volatile("" ::"s"(r0 * 2), "s"(pitch), "s"(sb), "s"(nb1), "v"(fo), "v"(xo), "v"(xso));
  __builtin_amdgcn_sched_barrier(0);
  if (w == 0) STAMP(27);
  asm volatile("s_waitcnt lgkmcnt(0)" : "+s"(ka), "+s"(kb), "+s"(kd));
  if (w == 0) STAMP(26);
  const GAS char *x = (const GAS char*)ka.x, *kc = (const GAS char*)ka.y, *vc = (const GAS char*)ka.z, *wq = (const GAS char*)ka.w;
  const GAS char *wo = (const GAS char*)kb.x, *wg = (const GAS char*)kb.y, *wu = (const GAS char*)kb.z, *wd = (const GAS char*)kb.w;
  GAS char *out = (GAS char*)kd.x, *ws = (GAS char*)kd.y;
  const __amdgpu_buffer_rsrc_t rx = mkrs(x, 896);
  typedef const __attribute__((address_space(4))) unsigned* cptr;
  unsigned ep, tk, tv_;
  asm volatile("s_cmp_lg_u32 %1, 4\n\ts_cbranch_scc1 1f\n\ts_load_dword %0, %2, 0x0\n1:" : "=&s"(ep) : "s"(w), "s"(ws) : "scc");
  tk = tv_ = 0;
  constexpr float C2 = 0.0625f * 1.4426950408889634f;
#define IDS() \
  asm volatile("" : "+s"(b)); \
  asm volatile("" : "+v"(tid)); \
  __builtin_amdgcn_sched_barrier(0); \
  const int h = (b >> 3) & 7, j = (b & 7) * 4 + (b >> 6); \
  const int lane = tid & 63; \
  const int u = (b & 63) + 64 * (b >> 7); \
  const bool isE = (j & 1) == 0; \
  const int ei = j >> 1; \
  const bool isD = !isE && u < 56; \
  const bool fin = !isE && u >= 114; \
  const int fc = u - 114; \
  const int rs = lane >> 3, c8 = lane & 7, g4 = lane >> 4, n16 = lane & 15; \
  const bool nlo = n16 < 8; \
  const int gidx = 4 * c8 + 2 * (g4 & 1) + (n16 >> 3); \
  const __amdgpu_buffer_rsrc_t rw = mkrs(ws, WS_END); \
  (void)rs; (void)gidx

  if (w == 4) {
    IDS();
    STAMP(0);
    __builtin_amdgcn_s_setprio(3);
    asm volatile("s_barrier\n\ts_waitcnt lgkmcnt(0)" : "+s"(ep) : "s"(tk), "s"(tv_) : "memory");
    const unsigned tag = ep + 1u;
    if (lane == 0) *(volatile LAS v2u*)(L + L_FLAG + 2) = v2u{tag, 1u};
    if (u < 120) {
      const v2u r0 = *(const LAS v2u*)(L + L_R1 + 2 * (lane & 31)), r1 = *(const LAS v2u*)(L + L_R1 + 64 + 2 * (lane & 31)),
                r2 = *(const LAS v2u*)(L + L_R1 + 128 + 2 * (lane & 31)), r3 = *(const LAS v2u*)(L + L_R1 + 192 + 2 * (lane & 31));
      const float v0 = (asf(r0.x) + asf(r1.x)) + (asf(r2.x) + asf(r3.x)), v1 = (asf(r0.y) + asf(r1.y)) + (asf(r2.y) + asf(r3.y));
      const int hf = isE ? 1 : 0;
      const int dst = u < 64 ? WS_Q + (u >> 3) * 8192 + hf * 4096 + (u & 7) * 512 : u < 92 ? WS_GU + (u - 64) * 2048 + hf * 512 : WS_GU + (u - 92) * 2048 + 1024 + hf * 512;
      if (lane < 32) stsc<QSAUX>(rw, dst + lane * 16, asu(v0), asu(v1), tag);
    }
    STAMP(2);
    unsigned pt0 = 0, pt1 = 0;
    if (PTOUCH) { pt0 = *(cptr)(unsigned long long)(wd + (u < 56 ? u : 0) * 32 * 896); pt1 = *(cptr)(unsigned long long)out; }
    const unsigned xw = __builtin_amdgcn_raw_buffer_load_b16(rx, (4 * c8 + g4) * 2, fin ? fc * 64 : 0, 0);
    const int go = WS_GU + (u >> 1) * 2048 + g4 * 512 + (u & 1) * 256 + n16 * 16;
    v4u gu[1];
    lds_wait<true>(FL + 5, 1u);
    gu[0] = ldsc(rw, go, isD ? 0 : OOB);
    asm volatile("" ::"s"(pt0), "s"(pt1));
    asm volatile("" ::"v"(cpf), "v"(cpf2));
    BARRIER();
    BARRIER();
    BARRIER();
    if (lane < 2) {
      const float M = fmaxf(fmaxf(asf(L[L_WM + lane]), asf(L[L_WM + 2 + lane])), fmaxf(asf(L[L_WM + 4 + lane]), asf(L[L_WM + 6 + lane])));
      const float Ls = (asf(L[L_WL + lane]) + asf(L[L_WL + 2 + lane])) + (asf(L[L_WL + 4 + lane]) + asf(L[L_WL + 6 + lane]));
      stsc(rw, WS_ML + (h * 2 + lane) * 512 + j * 16, asu(M), asu(Ls), tag);
    }
    STAMP(4);
    if (isE) {
      lds_wait<true>(FL + 8, 1u);
      v4u ml[1];
      const unsigned np1 = poll<1>(rw, WS_ML + (h * 2 + (ei >> 3)) * 512 + (lane & 31) * 16, 0, tag, ml); RAW(29, np1); (void)np1;
      const float m = asf(ml[0].x);
      const float M = x16_max(row_max(m));
      const float e = __builtin_amdgcn_exp2f((m - M) * C2);
      float Ls = row_sum(e * asf(ml[0].z)); Ls = sw16(Ls, Ls);
      if (lane < 32) L[L_W + lane] = asu(e * __builtin_amdgcn_rcpf(Ls));
      CFENCE();
      if (lane == 0) FL[7] = 1u;
      STAMP(6);
    } else if (isD) {
      if (!tags_ok<1>(gu, tag)) poll<1>(rw, go, 0, tag, gu);
      const float y = rbf(sw16(asf(gu[0].x), asf(gu[0].z)));
      auto t = __builtin_amdgcn_permlane32_swap(asu(y), asu(y), false, false);
      const float hv = rbf(rbf(silu(asf(t[0]))) * asf(t[1]));
      const int r = 2 * n16 + (g4 & 1);
      if (lane < 32) L[L_OP + (r & 7) * 4 + (r >> 3)] = asu(hv);
      STAMP(6);
      BARRIER();
    } else if (fin) {
      lds_wait<true>(FL + 8, 1u);
      v4u fv[14];
      poll<14>(rw, WS_F + g4 * F_PITCH + fc * 256 + n16 * 16, 4 * F_PITCH, tag, fv);
      float f0 = 0.f, f1 = 0.f;
#pragma unroll
      for (int i = 0; i < 14; ++i) { f0 += asf(fv[i].x); f1 += asf(fv[i].z); }
      float f = sw16(f0, f1); f = sw32(f, f);
      const float g = rbf(silu(rbf(f)));
      if (lane < 32) L[L_W + 2 * n16 + (g4 & 1)] = asu(g);
      asm volatile("s_waitcnt lgkmcnt(0)" ::: "memory");
      const float gc = asf(L[L_W + 4 * c8 + g4]);
      int so = L_OP + g4 * 8 + c8, oa = (4 * c8 + g4) * 2 + (nlo ? 0 : OOB);
      float xf = blo(xw);
      asm volatile("" : "+v"(so), "+v"(oa), "+v"(xf));
      STAMP(16);
      BARRIER();
      const float s = rbf((asf(L[so]) + asf(L[so + 32])) + (asf(L[so + 64]) + asf(L[so + 96])));
      const float res = xf + rbf(s * gc);
      __builtin_amdgcn_raw_buffer_store_b16((unsigned short)pk2(res, res), mkrs(out, 896), oa, fc * 64, OAUX);
      if (fc == 0 && lane == 0) __hip_atomic_store((GAS unsigned*)ws, tag, RLX_AGENT);
      STAMP(8);
    }
    STAMP_FLUSH();
  } else {
    const v4u xr = __builtin_amdgcn_raw_buffer_load_b128(rx, xo, 0, 0);
    const v4u xs = __builtin_amdgcn_raw_buffer_load_b128(rx, xso, r0 * 2, 0);
    __builtin_amdgcn_sched_barrier(0);
    if (w == 0) STAMP(1);
    v4u fr[8];
    const GAS char* w1 = q0 ? wq : t0 < 28 ? wg : wu;
    {
      const __amdgpu_buffer_rsrc_t ra = mkrs(w1, nb1);
#pragma unroll
      for (int i = 0; i < 6; ++i) fr[i] = ldnt(ra, fo, sb + i * pitch);
    }
    IDS();
    const int k = w;
    const unsigned hm = (lane & 1) ? 0xffff0000u : 0x0000ffffu;
    v4u kf[16], vf[16], wf[8];
    if (tid < 20) FL[tid] = 0u;
    const int wre = 512 * h + 32 * ei, wrd = u < 56 ? 32 * u : 0;
    const int wrow = (wrd + ((wre - wrd) & ((j & 1) - 1))) * 896;
    const int oo = rs * 896 + c8 * 16;
    const v2u msk2 = {(lane & 2) ? 0u : hm, (lane & 2) ? hm : 0u};
    const int cA = 2 * k, sA = cA * 128, sB = k < 3 ? sA + 128 : OOB;
    const __amdgpu_buffer_rsrc_t ro = mkrs((isE ? wo : wd) + wrow, (isE || isD) ? 32 * 896 : 0);
#define LDW(i) wf[i] = ldnt(ro, oo, ((i) >> 1) * 8 * 896 + (((i) & 1) ? sB : sA))
    {
      const __amdgpu_buffer_rsrc_t r67 = mkrs(isE ? wo + wrow : w1, isE ? 32 * 896 : nb1);
      const int o67 = isE ? oo : fo;
      fr[6] = ldnt(r67, o67, isE ? sA : sb + 6 * pitch);
      fr[7] = ldnt(r67, o67, isE ? sB : sb + 7 * pitch);
      wf[0] = fr[6]; wf[1] = fr[7];
    }
    const int kvo = (h * SK + (SK - WIN) + j * 128) * 512;
    const int rot = ((b + (b >> 3) + (b >> 6)) & 7) * 8192;
    const int ko = k * 2048 + (n16 >> 2) * 512 + g4 * 64 + (lane & 3) * 16;
    const int vo = rs * 512 + c8 * 16 + k * 128;
    const __amdgpu_buffer_rsrc_t rk = mkrs(kc, 8 * SK * 512), rv = mkrs(vc, 8 * SK * 512);
#define LDK(i) kf[i] = ldnt(rk, ko + ((i) & 1) * 256, kvo + ((((i) >> 1) * 8192 + rot) & 0xffff))
#define LDV(i) vf[i] = ldnt(rv, vo, kvo + (((i) * 4096 + rot) & 0xffff))
    if (KFIRST) LDK(0);
    if (VFIRST * (1 - VLATE)) LDV(0);
#define LDS_(n) do { if ((n) < 16) LDV((n) < 16 ? (n) : 0); else LDW((n) >= 16 ? (n) - 16 + WEARLY + WMID : 2); } while (0)
    if (k == 0) STAMP(18);
    {
      float xv[8];
#pragma unroll
      for (int e = 0; e < 4; ++e) { xv[2 * e] = blo(xr[e]); xv[2 * e + 1] = bhi(xr[e]); }
      const float s1 = ((xv[0] + xv[1]) + (xv[2] + xv[3])) + ((xv[4] + xv[5]) + (xv[6] + xv[7]));
      const float mean = wave_sum(s1) * (1.f / DM);
      float s2 = 0.f;
#pragma unroll
      for (int e = 0; e < 8; ++e) { const float d = xv[e] - mean; s2 += d * d; }
      s2 = lane < 56 ? s2 : 0.f;
      const float var = wave_sum(s2) * (1.f / DM);
      const float mb = rbf(mean), rden = __builtin_amdgcn_rcpf(rbf(__builtin_amdgcn_sqrtf(rbf(rbf(var) + 1e-5f))));
      unsigned xc[8];
#pragma unroll
      for (int i = 0; i < 8; ++i) {
        const float xn = rbf(((i & 1) ? bhi(xs[i >> 1]) : blo(xs[i >> 1])) - mb) * rden;
        xc[i] = (i >= 6 && isE) ? 0u : pk2(xn, xn);
      }
      if (k == 0) STAMP(19);
      __builtin_amdgcn_sched_barrier(0);
      f32x4 a0 = {0.f, 0.f, 0.f, 0.f}, a1 = {0.f, 0.f, 0.f, 0.f};
#pragma unroll
      for (int i = 0; i < 8; ++i) {
        const v2u am = {xc[i] & msk2.x, xc[i] & msk2.y};
        a0 = mfma16(am, v2u{fr[i].x, fr[i].y}, a0);
        a1 = mfma16(am, v2u{fr[i].z, fr[i].w}, a1);
        __builtin_amdgcn_sched_barrier(0);
        if (i >= 8 - WINL) LDK(KFIRST + i - (8 - WINL));
      }
      float v0, v1;
      fold2<true>(a0, a1, nlo, v0, v1);
      if (lane < 32) *(LAS v2u*)(L + L_R1 + 64 * k + 2 * gidx) = v2u{asu(v0), asu(v1)};
      BARRIER();
      if (k == 0) STAMP(12);
    }
    if (VFIRST * VLATE) { VMW(WINL - 1); LDV(0); }
#pragma unroll
    for (int i = KFIRST + WINL; i < 16; ++i) { VMW(WINL - 1); LDK(i); }
#pragma unroll
    for (int i = VFIRST; i < QPOS; ++i) { VMW(WINL - 1); LDV(i); }
    unsigned tag;
    v4u q2[2];
    {
      const int qo = WS_Q + h * 8192 + k * 1024 + lane * 16;
      v4u qv[2];
      qv[0] = __builtin_amdgcn_raw_buffer_load_b128(rw, qo, (ABL & 1) ? OOB : 0, QLAUX);
      qv[1] = __builtin_amdgcn_raw_buffer_load_b128(rw, qo, (ABL & 3) ? OOB : 4096, QLAUX);
      __builtin_amdgcn_sched_barrier(0);
      if (lane == 0) FL[5] = 1u;
      if (k == 0) STAMP(17);
#pragma unroll
      for (int i = QPOS; i < QPOS + WINL; ++i) { VMW(WINL + 1); LDV(i); }
      lds_wait<false>(FL + 3, 1u);
      tag = __builtin_amdgcn_readfirstlane(FL[2]);
      VMW(WINL);
      const bool qok = (ABL & 7) ? (qv[0].y != 12345u || tag != 77u) : tags_ok<2>(qv, tag);
      q2[0] = ldsc(rw, qo, qok ? OOB : 0);
      q2[1] = ldsc(rw, qo, qok ? OOB : 4096);
      __builtin_amdgcn_sched_barrier(0);
#pragma unroll
      for (int i = QPOS + WINL; i < VB1; ++i) LDS_(i);
      if (k == 0) STAMP(13);
      unsigned np = 1;
      if (!qok) {
        qv[0] = q2[0]; qv[1] = q2[1]; ++np;
        if (!tags_ok<2>(qv, tag)) np += poll<2>(rw, qo, 4096, tag, qv);
      }
      if (k == 0) RAW(28, np);
      if (k == 0) STAMP(3);
      L[L_Q + 64 * k + lane] = pk2(asf(qv[0].x) + asf(qv[1].x), asf(qv[0].z) + asf(qv[1].z));
    }
    BARRIER();
#pragma unroll
    for (int i = VB1; i < VB2; ++i) LDS_(i);
    if (k == 0) STAMP(24);
    v4u q[4];
#pragma unroll
    for (int t = 0; t < 4; ++t) {
      const int qa = L_Q + ((lane >> 2) & 1) * 128 + (t & 1) * 64 + g4 * 16 + (lane & 3) * 4;
      q[t] = *(const LAS v4u*)(L + ((n16 >> 3) == (t >> 1) ? qa : L_Z));
    }
    float y[4];
#pragma unroll
    for (int t = 0; t < 4; ++t) {
      f32x4 d = {0.f, 0.f, 0.f, 0.f};
      d = mfma32(q[0], kf[2 * t], d); d = mfma32(q[1], kf[2 * t + 1], d);
      d = mfma32(q[2], kf[2 * t + 8], d); d = mfma32(q[3], kf[2 * t + 9], d);
      const float lo = (lane & 1) ? d[1] : d[0], hi = (lane & 1) ? d[3] : d[2];
      y[t] = (lane & 2) ? hi : lo;
    }
    {
      const float k0 = (lane & 2) ? y[2] : y[0], s0 = (lane & 2) ? y[0] : y[2], k1 = (lane & 2) ? y[3] : y[1], s1 = (lane & 2) ? y[1] : y[3];
      y[0] = k0 + dpp<XOR2>(s0); y[1] = k1 + dpp<XOR2>(s1);
    }
    const float sc = rbf(((lane & 1) ? y[1] : y[0]) + dpp<XOR1>((lane & 1) ? y[0] : y[1]));
    const float m = x32_max(row_max(sc));
    const int a = g4 & 1;
    if ((lane & 0x2f) == 0) L[L_WM + 2 * k + a] = asu(m);
    BARRIER();
#pragma unroll
    for (int i = VB2; i < VB3; ++i) LDS_(i);
    const float M = fmaxf(fmaxf(asf(L[L_WM + a]), asf(L[L_WM + 2 + a])), fmaxf(asf(L[L_WM + 4 + a]), asf(L[L_WM + 6 + a])));
    const float p = __builtin_amdgcn_exp2f((sc - M) * C2);
    L[L_P + a * 128 + (4 * (k & 1) + (n16 >> 2)) * 16 + 2 * ((lane & 3) + 4 * (g4 >> 1)) + (k >> 1)] = pk2(p, p);
    float l = row_sum(p); l = sw32(l, l);
    if ((lane & 0x2f) == 0) L[L_WL + 2 * k + a] = asu(l);
    BARRIER();
#pragma unroll
    for (int i = VB3; i < VB4; ++i) LDS_(i);
#pragma unroll
    for (int i = 0; i < WMID; ++i) LDW(WEARLY + i);
    if (k == 0) STAMP(9);
    {
      v4u pa[4];
#pragma unroll
      for (int t = 0; t < 4; ++t) pa[t] = *(const LAS v4u*)(L + L_P + (n16 >> 3) * 128 + sa * 16 + 4 * t);
      int od = WS_O + (h + 8 * j) * O_PITCH + (lane >> 5) * 1024 + k * 256 + (2 * c8 + (g4 & 1)) * 16 + (nlo ? 0 : OOB);
      asm volatile("" : "+v"(od));
      f32x4 a0 = {0.f, 0.f, 0.f, 0.f}, a1 = {0.f, 0.f, 0.f, 0.f};
#pragma unroll
      for (int i = 0; i < 16; ++i) {
        const unsigned c = pa[i >> 2][i & 3];
        const v2u am = {c & msk2.x, c & msk2.y};
        a0 = mfma16(am, v2u{vf[i].x, vf[i].y}, a0);
        a1 = mfma16(am, v2u{vf[i].z, vf[i].w}, a1);
        __builtin_amdgcn_sched_barrier(0);
        if (i == QPOS + WINL) asm volatile("" ::"v"(q2[0]), "v"(q2[1]));
        {
          const int nx = i + WOWIN;
          if (nx >= VB4 && nx < 16 + WPRE && nx < 24 - WEARLY - WMID && i < 15) LDS_(nx);
        }
      }
      const f32x4 o = fold4<true>(a0, a1);
      stsc(rw, od, pk2(o[0], o[1]), pk2(o[2], o[3]), tag);
      __builtin_amdgcn_sched_barrier(0);
    }
    if (lane == 0) FL[8] = 1u;
    if (k == 3) STAMP(14);
    if (k == 0) STAMP(15);
#pragma unroll
    for (int i = (VB4 > 15 + WOWIN ? VB4 : 16 + (WPRE < WOWIN - 1 ? WPRE : WOWIN - 1)); i < 24 - WEARLY - WMID; ++i) LDS_(i);
    if (isE || isD) {
      v4u msk = {(c8 >> 1) == 0 ? hm : 0u, (c8 >> 1) == 1 ? hm : 0u, (c8 >> 1) == 2 ? hm : 0u, (c8 >> 1) == 3 ? hm : 0u};
      const int dead = (k < 3 || lane < 32) ? 0 : OOB;
      int sd = isE ? WS_S + (h * 16 + ei) * S_PITCH + (cA + (lane >> 5)) * 256 + (2 * c8 + (g4 & 1)) * 16 + (nlo ? dead : OOB)
                   : WS_F + u * F_PITCH + (cA + (lane >> 5)) * 512 + gidx * 16 + dead;
      int lw = L_OP + k * 32 + (4 * (c8 & 1) + g4) * 4 + (c8 >> 1) + (nlo ? 0 : 256), lr = L_OP + rs * 4;
      unsigned tv = tag;
      asm volatile("" : "+v"(msk), "+v"(sd), "+v"(lw), "+v"(lr), "+v"(tv));
      if (isE) {
        if (ODLY) __builtin_amdgcn_s_sleep(ODLY);
        v4u ov[1];
        const int po = WS_O + (h + 8 * (8 * k + rs)) * O_PITCH + ei * 128 + c8 * 16;
        ov[0] = ldsc(rw, po, 0);
        __builtin_amdgcn_sched_barrier(0);
        v4u ov2[1];
        if (ODLY2) { __builtin_amdgcn_s_sleep(ODLY2); CFENCE(); ov2[0] = ldsc(rw, po, 0); __builtin_amdgcn_sched_barrier(0); }
        lds_wait<false>(FL + 7, 1u);
        const float wt = asf(L[L_W + 8 * k + rs]);
        asm volatile("" ::"v"(wt));
        unsigned np2 = 1;
        if (!tags_ok<1>(ov, tag)) {
          if (ODLY2) ov[0] = ov2[0];
          if (!ODLY2 || !tags_ok<1>(ov, tag)) np2 += poll<1>(rw, po, 0, tag, ov);
        }
        if (k == 0) RAW(30, np2);
        if (k == 0) STAMP(7);
        L[lw] = asu(red4(wt * blo(ov[0].x), wt * bhi(ov[0].x), wt * blo(ov[0].z), wt * bhi(ov[0].z)));
        BARRIER();
        if (k == 0) STAMP(25);
        const f32x4 os = (__builtin_bit_cast(f32x4, *(const LAS v4u*)(L + lr)) + __builtin_bit_cast(f32x4, *(const LAS v4u*)(L + lr + 32))) +
                         (__builtin_bit_cast(f32x4, *(const LAS v4u*)(L + lr + 64)) + __builtin_bit_cast(f32x4, *(const LAS v4u*)(L + lr + 96)));
        f32x4 a0 = {0.f, 0.f, 0.f, 0.f}, a1 = {0.f, 0.f, 0.f, 0.f};
#pragma unroll
        for (int i = 0; i < 4; ++i) { const v4u am = amask(pk2(os[i], os[i]), msk); a0 = mfma32(am, wf[2 * i], a0); a1 = mfma32(am, wf[2 * i + 1], a1); }
        const f32x4 s = fold4<false>(a0, a1);
        stsc(rw, sd, pk2(s[0], s[1]), pk2(s[2], s[3]), tv);
        if (ODLY2) asm volatile("" ::"v"(ov2[0]));
        if (k == 0) STAMP(10);
      }
      CFENCE();
      if (isD) {
#pragma unroll
        for (int i = 0; i < WEARLY; ++i) LDW(i);
        BARRIER();
        const f32x4 os = __builtin_bit_cast(f32x4, *(const LAS v4u*)(L + lr));
        f32x4 a0 = {0.f, 0.f, 0.f, 0.f}, a1 = {0.f, 0.f, 0.f, 0.f};
#pragma unroll
        for (int i = 3; i >= 0; --i) { const v4u am = amask(pk2(os[i], os[i]), msk); a0 = mfma32(am, wf[2 * i], a0); a1 = mfma32(am, wf[2 * i + 1], a1); }
        float v0, v1;
        fold2<false>(a0, a1, nlo, v0, v1);
        stsc(rw, sd, asu(v0), asu(v1), tv);
        if (k == 0) STAMP(11);
      }
    } else if (fin) {
      int lw = L_OP + 32 * k + g4 * 8 + c8 + (nlo ? 0 : 256);
      asm volatile("" : "+v"(lw));
      v4u sv[4];
      const unsigned np3 = poll<4>(rw, WS_S + (32 * k + rs) * S_PITCH + fc * 128 + c8 * 16, 8 * S_PITCH, tag, sv); if (k == 0) RAW(31, np3); (void)np3;
      const float s = red4((blo(sv[0].x) + blo(sv[1].x)) + (blo(sv[2].x) + blo(sv[3].x)), (bhi(sv[0].x) + bhi(sv[1].x)) + (bhi(sv[2].x) + bhi(sv[3].x)),
                           (blo(sv[0].z) + blo(sv[1].z)) + (blo(sv[2].z) + blo(sv[3].z)), (bhi(sv[0].z) + bhi(sv[1].z)) + (bhi(sv[2].z) + bhi(sv[3].z)));
      L[lw] = asu(s);
      if (k == 0) STAMP(20); if (k == 1) STAMP(21); if (k == 2) STAMP(22); if (k == 3) STAMP(23);
      BARRIER();
    }
    STAMP_FLUSH();
  }
}

extern "C" void kernel_launch(void* const* d_in, const int* in_sizes, int n_in, void* d_out, int out_size, void* d_ws, size_t ws_size,
                              hipStream_t stream) {
  fused_decode<<<NWG, NTHR, 0, stream>>>(d_in[0], d_in[1], d_in[2], d_in[3], d_in[4], d_in[6], d_in[7], d_in[8], d_out, (char*)d_ws + WSOFF);
}
```

```cpp
#include <hip/hip_runtime.h>
#include <stdint.h>

#define GAS __attribute__((address_space(1)))
#define LAS __attribute__((address_space(3)))
typedef unsigned v4u __attribute__((ext_vector_type(4)));
typedef unsigned v2u __attribute__((ext_vector_type(2)));
typedef float f32x4 __attribute__((ext_vector_type(4)));
typedef __bf16 bf16x8 __attribute__((ext_vector_type(8)));
typedef __bf16 bf16x2 __attribute__((ext_vector_type(2)));
typedef short s16x4 __attribute__((ext_vector_type(4)));
typedef unsigned long long v2q __attribute__((ext_vector_type(2)));
typedef unsigned long long v4q __attribute__((ext_vector_type(4)));

constexpr int DM = 448, SK = 65536, WIN = 4096;
constexpr int NWG = 256, NTHR = 320;
constexpr int WINL = 6;
constexpr int VB1 = 10, VB2 = 12, VB3 = 14;
constexpr int WOWIN = 7;
constexpr int CODE_BYTES = 10496;
constexpr int WS_Q = 4096;
constexpr int WS_GU = WS_Q + 8 * 8192;
constexpr int WS_ML = WS_GU + 28 * 2048;
constexpr int O_PITCH = 2176;
constexpr int WS_O = WS_ML + 8192;
constexpr int S_PITCH = 1792;
constexpr int WS_S = WS_O + 256 * O_PITCH;
constexpr int F_PITCH = 3584;
constexpr int WS_F = WS_S + 128 * S_PITCH;
constexpr int WS_END = WS_F + 56 * F_PITCH;
constexpr int OOB = 1 << 30;
constexpr int L_R1 = 0;
constexpr int L_Q = 256;
constexpr int L_P = 512;
constexpr int L_W = 800;
constexpr int L_WM = 832;
constexpr int L_WL = 840;
constexpr int L_FLAG = 928;
constexpr int L_Z = 944;
constexpr int L_OP = 960;
constexpr int L_TOTAL = 1536;
constexpr unsigned SPIN_CAP = 1u << 18;

__device__ __forceinline__ float asf(unsigned u) { return __builtin_bit_cast(float, u); }
__device__ __forceinline__ unsigned asu(float f) { return __builtin_bit_cast(unsigned, f); }
__device__ __forceinline__ float blo(unsigned u) { return asf(u << 16); }
__device__ __forceinline__ float bhi(unsigned u) { return asf(u & 0xffff0000u); }
__device__ __forceinline__ float rbf(float f) { return (float)(__bf16)f; }
__device__ __forceinline__ unsigned pk2(float lo, float hi) { bf16x2 t = {(__bf16)lo, (__bf16)hi}; return __builtin_bit_cast(unsigned, t); }
__device__ __forceinline__ f32x4 mfma32(v4u a, v4u b, f32x4 c) {
  return __builtin_amdgcn_mfma_f32_16x16x32_bf16(__builtin_bit_cast(bf16x8, a), __builtin_bit_cast(bf16x8, b), c, 0, 0, 0);
}
__device__ __forceinline__ f32x4 mfma16(v2u a, v2u b, f32x4 c) {
  return __builtin_amdgcn_mfma_f32_16x16x16bf16_1k(__builtin_bit_cast(s16x4, a), __builtin_bit_cast(s16x4, b), c, 0, 0, 0);
}
template <int CTRL> __device__ __forceinline__ float dpp(float x) {
  return asf((unsigned)__builtin_amdgcn_mov_dpp((int)asu(x), CTRL, 0xf, 0xf, true));
}
constexpr int XOR1 = 0xB1, XOR2 = 0x4E, XOR7 = 0x141, XOR8 = 0x128;
__device__ __forceinline__ float sw16(float a, float b) { auto s = __builtin_amdgcn_permlane16_swap(asu(a), asu(b), false, false); return asf(s[0]) + asf(s[1]); }
__device__ __forceinline__ float sw32(float a, float b) { auto s = __builtin_amdgcn_permlane32_swap(asu(a), asu(b), false, false); return asf(s[0]) + asf(s[1]); }
__device__ __forceinline__ float x16_max(float x) { auto s = __builtin_amdgcn_permlane16_swap(asu(x), asu(x), false, false); return fmaxf(asf(s[0]), asf(s[1])); }
__device__ __forceinline__ float x32_max(float x) { auto s = __builtin_amdgcn_permlane32_swap(asu(x), asu(x), false, false); return fmaxf(asf(s[0]), asf(s[1])); }
__device__ __forceinline__ float row_sum(float x) { x += dpp<XOR1>(x); x += dpp<XOR2>(x); x += dpp<XOR7>(x); x += dpp<XOR8>(x); return x; }
__device__ __forceinline__ float row_max(float x) { x = fmaxf(x, dpp<XOR1>(x)); x = fmaxf(x, dpp<XOR2>(x)); x = fmaxf(x, dpp<XOR7>(x)); return fmaxf(x, dpp<XOR8>(x)); }
__device__ __forceinline__ float wave_sum(float x) { x = row_sum(x); x = sw16(x, x); return sw32(x, x); }
__device__ __forceinline__ float red4(float v0, float v1, float v2, float v3) { const float t = sw16(sw32(v0, v2), sw32(v1, v3)); return t + dpp<XOR8>(t); }
__device__ __forceinline__ __amdgpu_buffer_rsrc_t mkrs(const GAS char* p, int bytes) { return __builtin_amdgcn_make_buffer_rsrc((void*)p, 0, bytes, 0x00020000); }
__device__ __forceinline__ v4u ldnt(__amdgpu_buffer_rsrc_t rs, int voff, int soff) {
  const v4u r = __builtin_amdgcn_raw_buffer_load_b128(rs, voff, soff, 2);
  __builtin_amdgcn_sched_barrier(0);
  return r;
}
__device__ __forceinline__ v4u ldsc(__amdgpu_buffer_rsrc_t rs, int voff, int soff) { return __builtin_amdgcn_raw_buffer_load_b128(rs, voff, soff, 16); }
__device__ __forceinline__ void stsc(__amdgpu_buffer_rsrc_t rs, int off, unsigned d0, unsigned d1, unsigned tag) {
  v4u g = {d0, tag, d1, tag};
  __builtin_amdgcn_raw_buffer_store_b128(g, rs, off, 0, 16);
}
#define BARRIER() asm volatile("s_waitcnt lgkmcnt(0)\n\ts_barrier" ::: "memory")
#define CFENCE() asm volatile("" ::: "memory")
#define VMW(n) asm volatile("s_waitcnt vmcnt(%0)" ::"i"(n) : "memory")
__device__ __forceinline__ v4u amask(unsigned x, const v4u& m) { return v4u{x & m.x, x & m.y, x & m.z, x & m.w}; }
template <bool R16> __device__ __forceinline__ f32x4 fold4(const f32x4& A, const f32x4& B) {
  f32x4 s;
#pragma unroll
  for (int r = 0; r < 4; ++r) {
    auto t = R16 ? __builtin_amdgcn_permlane16_swap(asu(A[r]), asu(B[r]), false, false) : __builtin_amdgcn_permlane32_swap(asu(A[r]), asu(B[r]), false, false);
    s[r] = asf(t[0]) + dpp<XOR8>(asf(t[1]));
  }
  return s;
}
template <bool R16> __device__ __forceinline__ void fold2(const f32x4& A, const f32x4& B, bool nlo, float& v0, float& v1) {
  float s[4];
#pragma unroll
  for (int r = 0; r < 4; ++r) {
    auto t = R16 ? __builtin_amdgcn_permlane16_swap(asu(A[r]), asu(B[r]), false, false) : __builtin_amdgcn_permlane32_swap(asu(A[r]), asu(B[r]), false, false);
    const float a = asf(t[0]), b = asf(t[1]);
    s[r] = r < 2 ? a + dpp<XOR8>(b) : dpp<XOR8>(a) + b;
  }
  v0 = nlo ? s[0] : s[2];
  v1 = nlo ? s[1] : s[3];
}
template <int N> __device__ __forceinline__ bool tags_ok(const v4u (&v)[N], unsigned tag) {
  bool ok = true;
#pragma unroll
  for (int k = 0; k < N; ++k) ok &= (v[k].y == tag) & (v[k].w == tag);
  return __builtin_amdgcn_ballot_w64(!ok) == 0ull;
}
template <int N> __device__ __forceinline__ void poll(__amdgpu_buffer_rsrc_t rs, int off, int stride, unsigned tag, v4u (&v)[N]) {
#pragma unroll 1
  for (unsigned spins = 0; spins < SPIN_CAP; ++spins) {
    CFENCE();
#pragma unroll
    for (int k = 0; k < N; ++k) v[k] = ldsc(rs, off, k * stride);
    if (tags_ok<N>(v, tag)) break;
  }
}
template <bool SLEEP> __device__ __forceinline__ void lds_wait(volatile LAS unsigned* w) {
#pragma unroll 1
  for (unsigned spins = 0; spins < (SPIN_CAP << 5); ++spins) {
    if (__builtin_amdgcn_readfirstlane(*w) != 0u) break;
    if (SLEEP) __builtin_amdgcn_s_sleep(1);
  }
  CFENCE();
}
__device__ __forceinline__ float silu(float x) { return x * __builtin_amdgcn_rcpf(1.f + __expf(-x)); }

__global__ void __launch_bounds__(NTHR, 2) fused_decode(const void* x_, const void* kc_, const void* vc_, const void* wq_, const void* wo_, const void* wg_,
                                                        const void* wu_, const void* wd_, void* out_, void* ws_) {
  v4q ka, kb; v2q kd; unsigned long long pc;
  int b = blockIdx.x, tid = threadIdx.x;
  asm volatile("s_load_dwordx8 %0, %5, 0x0\n\ts_load_dwordx8 %1, %5, 0x20\n\ts_load_dwordx4 %2, %5, 0x40\n\ts_getpc_b64 %3"
               : "=&s"(ka), "=&s"(kb), "=&s"(kd), "=&s"(pc), "+s"(b) : "s"(__builtin_amdgcn_kernarg_segment_ptr()));
  asm volatile("" : "+v"(tid));
  __builtin_amdgcn_sched_barrier(0);
  __shared__ __attribute__((aligned(16))) unsigned lds_[L_TOTAL];
  LAS unsigned* L = (LAS unsigned*)lds_;
  volatile LAS unsigned* FL = (volatile LAS unsigned*)(L + L_FLAG);
  const int w = __builtin_amdgcn_readfirstlane(tid >> 6);
  unsigned cpf = 0, cpf2 = 0;
  if (w == 4 && (b & 0xb8) == 0xb8) {
    const __amdgpu_buffer_rsrc_t rc = mkrs((const GAS char*)(pc & ~255ull), CODE_BYTES);
    cpf = __builtin_amdgcn_raw_buffer_load_b32(rc, (tid & 63) * 128, 256, 0);
    cpf2 = __builtin_amdgcn_raw_buffer_load_b32(rc, (tid & 63) * 128, 256 + 8192, 0);
  }
  const bool e0 = (b & 64) == 0, q0 = b < 128;
  const int t0 = b & 63;
  const int NI = e0 ? 6 : 8;
  const int r0 = (e0 ? 256 : 0) + 8 * NI * w;
  const int pitch = q0 ? 8192 : 3584;
  const int sb = r0 * pitch + (q0 ? t0 : t0 < 28 ? t0 : t0 - 28) * 128;
  const int nb1 = (q0 || t0 < 56) ? DM * pitch : 0;
  const int sa = 2 * ((tid >> 4) & 3) + ((tid >> 2) & 1);
  const int fo = ((tid >> 3) & 7) * (NI * pitch) + (tid & 7) * 16, xo = (tid & 63) * 16, xso = sa * (NI * 2);
  asm volatile("" ::"s"(r0 * 2), "s"(pitch), "s"(sb), "s"(nb1), "v"(fo), "v"(xo), "v"(xso));
  __builtin_amdgcn_sched_barrier(0);
  asm volatile("s_waitcnt lgkmcnt(0)" : "+s"(ka), "+s"(kb), "+s"(kd));
  const GAS char *x = (const GAS char*)ka.x, *kc = (const GAS char*)ka.y, *vc = (const GAS char*)ka.z, *wq = (const GAS char*)ka.w;
  const GAS char *wo = (const GAS char*)kb.x, *wg = (const GAS char*)kb.y, *wu = (const GAS char*)kb.z, *wd = (const GAS char*)kb.w;
  GAS char *out = (GAS char*)kd.x, *ws = (GAS char*)kd.y;
  const __amdgpu_buffer_rsrc_t rx = mkrs(x, 896);
  unsigned ep;
  asm volatile("s_cmp_lg_u32 %1, 4\n\ts_cbranch_scc1 1f\n\ts_load_dword %0, %2, 0x0\n1:" : "=&s"(ep) : "s"(w), "s"(ws) : "scc");
  constexpr float C2 = 0.0625f * 1.4426950408889634f;
#define IDS() \
  asm volatile("" : "+s"(b)); \
  asm volatile("" : "+v"(tid)); \
  __builtin_amdgcn_sched_barrier(0); \
  const int h = (b >> 3) & 7, j = (b & 7) * 4 + (b >> 6); \
  const int lane = tid & 63; \
  const int u = (b & 63) + 64 * (b >> 7); \
  const bool isE = (j & 1) == 0; \
  const int ei = j >> 1; \
  const bool isD = !isE && u < 56; \
  const bool fin = !isE && u >= 114; \
  const int fc = u - 114; \
  const int c8 = lane & 7, g4 = lane >> 4, n16 = lane & 15; \
  const bool nlo = n16 < 8; \
  const __amdgpu_buffer_rsrc_t rw = mkrs(ws, WS_END)

  if (w == 4) {
    IDS();
    __builtin_amdgcn_s_setprio(3);
    asm volatile("s_barrier\n\ts_waitcnt lgkmcnt(0)" : "+s"(ep)::"memory");
    const unsigned tag = ep + 1u;
    if (lane == 0) *(volatile LAS v2u*)(L + L_FLAG + 2) = v2u{tag, 1u};
    if (u < 120) {
      const v2u p0 = *(const LAS v2u*)(L + L_R1 + 2 * (lane & 31)), p1 = *(const LAS v2u*)(L + L_R1 + 64 + 2 * (lane & 31)),
                p2 = *(const LAS v2u*)(L + L_R1 + 128 + 2 * (lane & 31)), p3 = *(const LAS v2u*)(L + L_R1 + 192 + 2 * (lane & 31));
      const float v0 = (asf(p0.x) + asf(p1.x)) + (asf(p2.x) + asf(p3.x)), v1 = (asf(p0.y) + asf(p1.y)) + (asf(p2.y) + asf(p3.y));
      const int hf = isE ? 1 : 0;
      const int dst = u < 64 ? WS_Q + (u >> 3) * 8192 + hf * 4096 + (u & 7) * 512 : u < 92 ? WS_GU + (u - 64) * 2048 + hf * 512 : WS_GU + (u - 92) * 2048 + 1024 + hf * 512;
      if (lane < 32) stsc(rw, dst + lane * 16, asu(v0), asu(v1), tag);
    }
    const unsigned xw = __builtin_amdgcn_raw_buffer_load_b16(rx, (4 * c8 + g4) * 2, fin ? fc * 64 : 0, 0);
    const int go = WS_GU + (u >> 1) * 2048 + g4 * 512 + (u & 1) * 256 + n16 * 16;
    v4u gu[1];
    lds_wait<true>(FL + 5);
    gu[0] = ldsc(rw, go, isD ? 0 : OOB);
    asm volatile("" ::"v"(cpf), "v"(cpf2));
    BARRIER();
    BARRIER();
    BARRIER();
    if (lane < 2) {
      const float M = fmaxf(fmaxf(asf(L[L_WM + lane]), asf(L[L_WM + 2 + lane])), fmaxf(asf(L[L_WM + 4 + lane]), asf(L[L_WM + 6 + lane])));
      const float Ls = (asf(L[L_WL + lane]) + asf(L[L_WL + 2 + lane])) + (asf(L[L_WL + 4 + lane]) + asf(L[L_WL + 6 + lane]));
      stsc(rw, WS_ML + (h * 2 + lane) * 512 + j * 16, asu(M), asu(Ls), tag);
    }
    if (isE) {
      lds_wait<true>(FL + 8);
      v4u ml[1];
      poll<1>(rw, WS_ML + (h * 2 + (ei >> 3)) * 512 + (lane & 31) * 16, 0, tag, ml);
      const float m = asf(ml[0].x);
      const float M = x16_max(row_max(m));
      const float e = __builtin_amdgcn_exp2f((m - M) * C2);
      float Ls = row_sum(e * asf(ml[0].z)); Ls = sw16(Ls, Ls);
      if (lane < 32) L[L_W + lane] = asu(e * __builtin_amdgcn_rcpf(Ls));
      CFENCE();
      if (lane == 0) FL[7] = 1u;
    } else if (isD) {
      if (!tags_ok<1>(gu, tag)) poll<1>(rw, go, 0, tag, gu);
      const float y = rbf(sw16(asf(gu[0].x), asf(gu[0].z)));
      auto t = __builtin_amdgcn_permlane32_swap(asu(y), asu(y), false, false);
      const float hv = rbf(rbf(silu(asf(t[0]))) * asf(t[1]));
      const int r = 2 * n16 + (g4 & 1);
      if (lane < 32) L[L_OP + (r & 7) * 4 + (r >> 3)] = asu(hv);
      BARRIER();
    } else if (fin) {
      lds_wait<true>(FL + 8);
      v4u fv[14];
      poll<14>(rw, WS_F + g4 * F_PITCH + fc * 256 + n16 * 16, 4 * F_PITCH, tag, fv);
      float f0 = 0.f, f1 = 0.f;
#pragma unroll
      for (int i = 0; i < 14; ++i) { f0 += asf(fv[i].x); f1 += asf(fv[i].z); }
      float f = sw16(f0, f1); f = sw32(f, f);
      const float g = rbf(silu(rbf(f)));
      if (lane < 32) L[L_W + 2 * n16 + (g4 & 1)] = asu(g);
      asm volatile("s_waitcnt lgkmcnt(0)" ::: "memory");
      const float gc = asf(L[L_W + 4 * c8 + g4]);
      int so = L_OP + g4 * 8 + c8, oa = (4 * c8 + g4) * 2 + (nlo ? 0 : OOB);
      float xf = blo(xw);
      asm volatile("" : "+v"(so), "+v"(oa), "+v"(xf));
      BARRIER();
      const float s = rbf((asf(L[so]) + asf(L[so + 32])) + (asf(L[so + 64]) + asf(L[so + 96])));
      const float res = xf + rbf(s * gc);
      __builtin_amdgcn_raw_buffer_store_b16((unsigned short)pk2(res, res), mkrs(out, 896), oa, fc * 64, 16);
      if (fc == 0 && lane == 0) __hip_atomic_store((GAS unsigned*)ws, tag, __ATOMIC_RELAXED, __HIP_MEMORY_SCOPE_AGENT);
    }
  } else {
    const v4u xr = __builtin_amdgcn_raw_buffer_load_b128(rx, xo, 0, 0);
    const v4u xs = __builtin_amdgcn_raw_buffer_load_b128(rx, xso, r0 * 2, 0);
    __builtin_amdgcn_sched_barrier(0);
    v4u fr[8];
    const GAS char* w1 = q0 ? wq : t0 < 28 ? wg : wu;
    {
      const __amdgpu_buffer_rsrc_t ra = mkrs(w1, nb1);
#pragma unroll
      for (int i = 0; i < 6; ++i) fr[i] = ldnt(ra, fo, sb + i * pitch);
    }
    IDS();
    const int k = w, rs = lane >> 3;
    const int gidx = 4 * c8 + 2 * (g4 & 1) + (n16 >> 3);
    const unsigned hm = (lane & 1) ? 0xffff0000u : 0x0000ffffu;
    v4u kf[16], vf[16], wf[8];
    if (tid < 20) FL[tid] = 0u;
    const int wre = 512 * h + 32 * ei, wrd = u < 56 ? 32 * u : 0;
    const int wrow = (wrd + ((wre - wrd) & ((j & 1) - 1))) * 896;
    const int oo = rs * 896 + c8 * 16;
    const v2u msk2 = {(lane & 2) ? 0u : hm, (lane & 2) ? hm : 0u};
    const int cA = 2 * k, sA = cA * 128, sB = k < 3 ? sA + 128 : OOB;
    const __amdgpu_buffer_rsrc_t ro = mkrs((isE ? wo : wd) + wrow, (isE || isD) ? 32 * 896 : 0);
#define LDW(i) wf[i] = ldnt(ro, oo, ((i) >> 1) * 8 * 896 + (((i) & 1) ? sB : sA))
    {
      const __amdgpu_buffer_rsrc_t r67 = mkrs(isE ? wo + wrow : w1, isE ? 32 * 896 : nb1);
      const int o67 = isE ? oo : fo;
      fr[6] = ldnt(r67, o67, isE ? sA : sb + 6 * pitch);
      fr[7] = ldnt(r67, o67, isE ? sB : sb + 7 * pitch);
      wf[0] = fr[6]; wf[1] = fr[7];
    }
    const int kvo = (h * SK + (SK - WIN) + j * 128) * 512;
    const int rot = ((b + (b >> 3) + (b >> 6)) & 7) * 8192;
    const int ko = k * 2048 + (n16 >> 2) * 512 + g4 * 64 + (lane & 3) * 16;
    const int vo = rs * 512 + c8 * 16 + k * 128;
    const __amdgpu_buffer_rsrc_t rk = mkrs(kc, 8 * SK * 512), rv = mkrs(vc, 8 * SK * 512);
#define LDK(i) kf[i] = ldnt(rk, ko + ((i) & 1) * 256, kvo + ((((i) >> 1) * 8192 + rot) & 0xffff))
#define LDV(i) vf[i] = ldnt(rv, vo, kvo + (((i) * 4096 + rot) & 0xffff))
    {
      float xv[8];
#pragma unroll
      for (int e = 0; e < 4; ++e) { xv[2 * e] = blo(xr[e]); xv[2 * e + 1] = bhi(xr[e]); }
      const float s1 = ((xv[0] + xv[1]) + (xv[2] + xv[3])) + ((xv[4] + xv[5]) + (xv[6] + xv[7]));
      const float mean = wave_sum(s1) * (1.f / DM);
      float s2 = 0.f;
#pragma unroll
      for (int e = 0; e < 8; ++e) { const float d = xv[e] - mean; s2 += d * d; }
      s2 = lane < 56 ? s2 : 0.f;
      const float var = wave_sum(s2) * (1.f / DM);
      const float mb = rbf(mean), rden = __builtin_amdgcn_rcpf(rbf(__builtin_amdgcn_sqrtf(rbf(rbf(var) + 1e-5f))));
      unsigned xc[8];
#pragma unroll
      for (int i = 0; i < 8; ++i) {
        const float xn = rbf(((i & 1) ? bhi(xs[i >> 1]) : blo(xs[i >> 1])) - mb) * rden;
        xc[i] = (i >= 6 && isE) ? 0u : pk2(xn, xn);
      }
      __builtin_amdgcn_sched_barrier(0);
      f32x4 a0 = {0.f, 0.f, 0.f, 0.f}, a1 = {0.f, 0.f, 0.f, 0.f};
#pragma unroll
      for (int i = 0; i < 8; ++i) {
        const v2u am = {xc[i] & msk2.x, xc[i] & msk2.y};
        a0 = mfma16(am, v2u{fr[i].x, fr[i].y}, a0);
        a1 = mfma16(am, v2u{fr[i].z, fr[i].w}, a1);
        __builtin_amdgcn_sched_barrier(0);
        if (i >= 8 - WINL) LDK(i - (8 - WINL));
      }
      float v0, v1;
      fold2<true>(a0, a1, nlo, v0, v1);
      if (lane < 32) *(LAS v2u*)(L + L_R1 + 64 * k + 2 * gidx) = v2u{asu(v0), asu(v1)};
      BARRIER();
    }
#pragma unroll
    for (int i = WINL; i < 16; ++i) { VMW(WINL - 1); LDK(i); }
    unsigned tag;
    v4u q2[2];
    {
      const int qo = WS_Q + h * 8192 + k * 1024 + lane * 16;
      v4u qv[2];
      qv[0] = ldsc(rw, qo, 0);
      qv[1] = ldsc(rw, qo, 4096);
      __builtin_amdgcn_sched_barrier(0);
      if (lane == 0) FL[5] = 1u;
#pragma unroll
      for (int i = 0; i < WINL; ++i) { VMW(WINL + 1); LDV(i); }
      lds_wait<false>(FL + 3);
      tag = __builtin_amdgcn_readfirstlane(FL[2]);
      VMW(WINL);
      const bool qok = tags_ok<2>(qv, tag);
      q2[0] = ldsc(rw, qo, qok ? OOB : 0);
      q2[1] = ldsc(rw, qo, qok ? OOB : 4096);
      __builtin_amdgcn_sched_barrier(0);
#pragma unroll
      for (int i = WINL; i < VB1; ++i) LDV(i);
      if (!qok) {
        qv[0] = q2[0]; qv[1] = q2[1];
        if (!tags_ok<2>(qv, tag)) poll<2>(rw, qo, 4096, tag, qv);
      }
      L[L_Q + 64 * k + lane] = pk2(asf(qv[0].x) + asf(qv[1].x), asf(qv[0].z) + asf(qv[1].z));
    }
    BARRIER();
#pragma unroll
    for (int i = VB1; i < VB2; ++i) LDV(i);
    v4u q[4];
#pragma unroll
    for (int t = 0; t < 4; ++t) {
      const int qa = L_Q + ((lane >> 2) & 1) * 128 + (t & 1) * 64 + g4 * 16 + (lane & 3) * 4;
      q[t] = *(const LAS v4u*)(L + ((n16 >> 3) == (t >> 1) ? qa : L_Z));
    }
    float y[4];
#pragma unroll
    for (int t = 0; t < 4; ++t) {
      f32x4 d = {0.f, 0.f, 0.f, 0.f};
      d = mfma32(q[0], kf[2 * t], d); d = mfma32(q[1], kf[2 * t + 1], d);
      d = mfma32(q[2], kf[2 * t + 8], d); d = mfma32(q[3], kf[2 * t + 9], d);
      const float lo = (lane & 1) ? d[1] : d[0], hi = (lane & 1) ? d[3] : d[2];
      y[t] = (lane & 2) ? hi : lo;
    }
    {
      const float k0 = (lane & 2) ? y[2] : y[0], s0 = (lane & 2) ? y[0] : y[2], k1 = (lane & 2) ? y[3] : y[1], s1 = (lane & 2) ? y[1] : y[3];
      y[0] = k0 + dpp<XOR2>(s0); y[1] = k1 + dpp<XOR2>(s1);
    }
    const float sc = rbf(((lane & 1) ? y[1] : y[0]) + dpp<XOR1>((lane & 1) ? y[0] : y[1]));
    const float m = x32_max(row_max(sc));
    const int a = g4 & 1;
    if ((lane & 0x2f) == 0) L[L_WM + 2 * k + a] = asu(m);
    BARRIER();
#pragma unroll
    for (int i = VB2; i < VB3; ++i) LDV(i);
    const float M = fmaxf(fmaxf(asf(L[L_WM + a]), asf(L[L_WM + 2 + a])), fmaxf(asf(L[L_WM + 4 + a]), asf(L[L_WM + 6 + a])));
    const float p = __builtin_amdgcn_exp2f((sc - M) * C2);
    L[L_P + a * 128 + (4 * (k & 1) + (n16 >> 2)) * 16 + 2 * ((lane & 3) + 4 * (g4 >> 1)) + (k >> 1)] = pk2(p, p);
    float l = row_sum(p); l = sw32(l, l);
    if ((lane & 0x2f) == 0) L[L_WL + 2 * k + a] = asu(l);
    BARRIER();
    {
      v4u pa[4];
#pragma unroll
      for (int t = 0; t < 4; ++t) pa[t] = *(const LAS v4u*)(L + L_P + (n16 >> 3) * 128 + sa * 16 + 4 * t);
      int od = WS_O + (h + 8 * j) * O_PITCH + (lane >> 5) * 1024 + k * 256 + (2 * c8 + (g4 & 1)) * 16 + (nlo ? 0 : OOB);
      asm volatile("" : "+v"(od));
      f32x4 a0 = {0.f, 0.f, 0.f, 0.f}, a1 = {0.f, 0.f, 0.f, 0.f};
#pragma unroll
      for (int i = 0; i < 16; ++i) {
        const unsigned c = pa[i >> 2][i & 3];
        const v2u am = {c & msk2.x, c & msk2.y};
        a0 = mfma16(am, v2u{vf[i].x, vf[i].y}, a0);
        a1 = mfma16(am, v2u{vf[i].z, vf[i].w}, a1);
        __builtin_amdgcn_sched_barrier(0);
        if (i == WINL) asm volatile("" ::"v"(q2[0]), "v"(q2[1]));
        const int nx = i + WOWIN;
        if (nx >= VB3 && nx < 16) LDV(nx < 16 ? nx : 0);
        if (nx >= 16 && nx < 22) LDW(nx >= 16 && nx < 22 ? nx - 14 : 2);
      }
      const f32x4 o = fold4<true>(a0, a1);
      stsc(rw, od, pk2(o[0], o[1]), pk2(o[2], o[3]), tag);
      __builtin_amdgcn_sched_barrier(0);
    }
    if (lane == 0) FL[8] = 1u;
    if (isE || isD) {
      v4u msk = {(c8 >> 1) == 0 ? hm : 0u, (c8 >> 1) == 1 ? hm : 0u, (c8 >> 1) == 2 ? hm : 0u, (c8 >> 1) == 3 ? hm : 0u};
      const int dead = (k < 3 || lane < 32) ? 0 : OOB;
      int sd = isE ? WS_S + (h * 16 + ei) * S_PITCH + (cA + (lane >> 5)) * 256 + (2 * c8 + (g4 & 1)) * 16 + (nlo ? dead : OOB)
                   : WS_F + u * F_PITCH + (cA + (lane >> 5)) * 512 + gidx * 16 + dead;
      int lw = L_OP + k * 32 + (4 * (c8 & 1) + g4) * 4 + (c8 >> 1) + (nlo ? 0 : 256), lr = L_OP + rs * 4;
      unsigned tv = tag;
      asm volatile("" : "+v"(msk), "+v"(sd), "+v"(lw), "+v"(lr), "+v"(tv));
      if (isE) {
        __builtin_amdgcn_s_sleep(12);
        v4u ov[1];
        const int po = WS_O + (h + 8 * (8 * k + rs)) * O_PITCH + ei * 128 + c8 * 16;
        ov[0] = ldsc(rw, po, 0);
        __builtin_amdgcn_sched_barrier(0);
        lds_wait<false>(FL + 7);
        const float wt = asf(L[L_W + 8 * k + rs]);
        asm volatile("" ::"v"(wt));
        if (!tags_ok<1>(ov, tag)) poll<1>(rw, po, 0, tag, ov);
        L[lw] = asu(red4(wt * blo(ov[0].x), wt * bhi(ov[0].x), wt * blo(ov[0].z), wt * bhi(ov[0].z)));
        BARRIER();
        const f32x4 os = (__builtin_bit_cast(f32x4, *(const LAS v4u*)(L + lr)) + __builtin_bit_cast(f32x4, *(const LAS v4u*)(L + lr + 32))) +
                         (__builtin_bit_cast(f32x4, *(const LAS v4u*)(L + lr + 64)) + __builtin_bit_cast(f32x4, *(const LAS v4u*)(L + lr + 96)));
        f32x4 a0 = {0.f, 0.f, 0.f, 0.f}, a1 = {0.f, 0.f, 0.f, 0.f};
#pragma unroll
        for (int i = 0; i < 4; ++i) { const v4u am = amask(pk2(os[i], os[i]), msk); a0 = mfma32(am, wf[2 * i], a0); a1 = mfma32(am, wf[2 * i + 1], a1); }
        const f32x4 s = fold4<false>(a0, a1);
        stsc(rw, sd, pk2(s[0], s[1]), pk2(s[2], s[3]), tv);
      }
      CFENCE();
      if (isD) {
        LDW(0); LDW(1);
        BARRIER();
        const f32x4 os = __builtin_bit_cast(f32x4, *(const LAS v4u*)(L + lr));
        f32x4 a0 = {0.f, 0.f, 0.f, 0.f}, a1 = {0.f, 0.f, 0.f, 0.f};
#pragma unroll
        for (int i = 3; i >= 0; --i) { const v4u am = amask(pk2(os[i], os[i]), msk); a0 = mfma32(am, wf[2 * i], a0); a1 = mfma32(am, wf[2 * i + 1], a1); }
        float v0, v1;
        fold2<false>(a0, a1, nlo, v0, v1);
        stsc(rw, sd, asu(v0), asu(v1), tv);
      }
    } else if (fin) {
      int lw = L_OP + 32 * k + g4 * 8 + c8 + (nlo ? 0 : 256);
      asm volatile("" : "+v"(lw));
      v4u sv[4];
      poll<4>(rw, WS_S + (32 * k + rs) * S_PITCH + fc * 128 + c8 * 16, 8 * S_PITCH, tag, sv);
      const float s = red4((blo(sv[0].x) + blo(sv[1].x)) + (blo(sv[2].x) + blo(sv[3].x)), (bhi(sv[0].x) + bhi(sv[1].x)) + (bhi(sv[2].x) + bhi(sv[3].x)),
                           (blo(sv[0].z) + blo(sv[1].z)) + (blo(sv[2].z) + blo(sv[3].z)), (bhi(sv[0].z) + bhi(sv[1].z)) + (bhi(sv[2].z) + bhi(sv[3].z)));
      L[lw] = asu(s);
      BARRIER();
    }
  }
}

extern "C" void kernel_launch(void* const* d_in, const int* in_sizes, int n_in, void* d_out, int out_size, void* d_ws, size_t ws_size,
                              hipStream_t stream) {
  fused_decode<<<NWG, NTHR, 0, stream>>>(d_in[0], d_in[1], d_in[2], d_in[3], d_in[4], d_in[6], d_in[7], d_in[8], d_out, d_ws);
}
```

```cpp
#define PROBE 5
#include <hip/hip_runtime.h>
#include <stdint.h>

#define GAS __attribute__((address_space(1)))
#define LAS __attribute__((address_space(3)))
typedef unsigned v4u __attribute__((ext_vector_type(4)));
typedef unsigned v2u __attribute__((ext_vector_type(2)));
typedef float f32x4 __attribute__((ext_vector_type(4)));
typedef __bf16 bf16x8 __attribute__((ext_vector_type(8)));
typedef __bf16 bf16x2 __attribute__((ext_vector_type(2)));
typedef short s16x4 __attribute__((ext_vector_type(4)));
#define RLX_AGENT __ATOMIC_RELAXED, __HIP_MEMORY_SCOPE_AGENT

#define WINL 6
#define VB1 (10 + VFIRST)
#define VB2 (12 + VFIRST)
#define VB3 (14 + VFIRST)
#define VB4 VB3
#define QPOS VFIRST
#define ABL 0
#define QLAUX 16
#define ROT 1
#define OAUX 16
#define WPRE 6
#define ODLY2 0
#define WEARLY 2
#define KFIRST 0
#define VFIRST 0
#define PTOUCH 0
#define PFMODE 1
#define PFU 120
#define PFOFF 256
#define PFMASK 0xb8
#define TOUCH 0
#define VLATE 1
#define WSOFF 0
#define LTOT 1536
#define WMID 0
#define HMAP 1
#define LATIN 0
#define QCG(t) (LATIN == 0 ? (t) : LATIN == 1 ? (((t) & 56) | (((t) + ((t) >> 3)) & 7)) : LATIN == 2 ? ((((t) & 7) << 3) | (((t) >> 3) & 7)) : ((((t) & 7) << 1) | (((t) >> 3) & 1) | ((t) & 48)))
#define OTOUCH 0
#define ADTH 0
#define ADLO 0
#define ADHI 3
#define WOWIN 7
#define QSAUX 16
#define ODLY 12
#define CODE_BYTES 10496

constexpr int DM = 448, SK = 65536, WIN = 4096;
constexpr int NWG = 256, NTHR = 320;
constexpr int WS_Q = 4096;
constexpr int WS_GU = WS_Q + 8 * 8192;
constexpr int WS_ML = WS_GU + 28 * 2048;
constexpr int O_PITCH = 2176;
constexpr int WS_O = WS_ML + 8192;
constexpr int S_PITCH = 1792;
constexpr int WS_S = WS_O + 256 * O_PITCH;
constexpr int F_PITCH = 3584;
constexpr int WS_F = WS_S + 128 * S_PITCH;
constexpr int WS_END = WS_F + 56 * F_PITCH;
constexpr int OOB = 1 << 30;
constexpr int L_R1 = 0;
constexpr int L_Q = 256;
constexpr int L_P = 512;
constexpr int L_W = 800;
constexpr int L_WM = 832;
constexpr int L_WL = 840;
constexpr int L_FLAG = 928;
constexpr int L_Z = 944;
constexpr int L_OP = 960;
constexpr int L_TOTAL = LTOT;
constexpr unsigned SPIN_CAP = 1u << 16;

#define RAW(k, v)
#define STAMP(k) do { if ((k) == PROBE) stp = __builtin_amdgcn_s_memrealtime(); } while (0)
#define STAMP_DECL const unsigned long long st0 = __builtin_amdgcn_s_memrealtime(); unsigned long long stp = 0
#define STAMP_FLUSH() do { if (stp && b == 0) { const unsigned long long tend = st0 + 3000 + 500 * ((unsigned)__builtin_amdgcn_s_getreg(20 | (3 << 11)) & 0xfu); while (__builtin_amdgcn_s_memrealtime() < tend) __builtin_amdgcn_s_sleep(2); } } while (0)

__device__ __forceinline__ float asf(unsigned u) { return __builtin_bit_cast(float, u); }
__device__ __forceinline__ unsigned asu(float f) { return __builtin_bit_cast(unsigned, f); }
__device__ __forceinline__ float blo(unsigned u) { return asf(u << 16); }
__device__ __forceinline__ float bhi(unsigned u) { return asf(u & 0xffff0000u); }
__device__ __forceinline__ float rbf(float f) { return (float)(__bf16)f; }
__device__ __forceinline__ unsigned pk2(float lo, float hi) { bf16x2 t = {(__bf16)lo, (__bf16)hi}; return __builtin_bit_cast(unsigned, t); }
__device__ __forceinline__ f32x4 mfma32(v4u a, v4u b, f32x4 c) {
  return __builtin_amdgcn_mfma_f32_16x16x32_bf16(__builtin_bit_cast(bf16x8, a), __builtin_bit_cast(bf16x8, b), c, 0, 0, 0);
}
__device__ __forceinline__ f32x4 mfma16(v2u a, v2u b, f32x4 c) {
  return __builtin_amdgcn_mfma_f32_16x16x16bf16_1k(__builtin_bit_cast(s16x4, a), __builtin_bit_cast(s16x4, b), c, 0, 0, 0);
}
template <int CTRL> __device__ __forceinline__ float dpp(float x) {
  return asf((unsigned)__builtin_amdgcn_mov_dpp((int)asu(x), CTRL, 0xf, 0xf, true));
}
constexpr int XOR1 = 0xB1, XOR2 = 0x4E, XOR7 = 0x141, XOR8 = 0x128;
__device__ __forceinline__ float sw16(float a, float b) { auto s = __builtin_amdgcn_permlane16_swap(asu(a), asu(b), false, false); return asf(s[0]) + asf(s[1]); }
__device__ __forceinline__ float sw32(float a, float b) { auto s = __builtin_amdgcn_permlane32_swap(asu(a), asu(b), false, false); return asf(s[0]) + asf(s[1]); }
__device__ __forceinline__ float x16_max(float x) { auto s = __builtin_amdgcn_permlane16_swap(asu(x), asu(x), false, false); return fmaxf(asf(s[0]), asf(s[1])); }
__device__ __forceinline__ float x32_max(float x) { auto s = __builtin_amdgcn_permlane32_swap(asu(x), asu(x), false, false); return fmaxf(asf(s[0]), asf(s[1])); }
__device__ __forceinline__ float row_sum(float x) { x += dpp<XOR1>(x); x += dpp<XOR2>(x); x += dpp<XOR7>(x); x += dpp<XOR8>(x); return x; }
__device__ __forceinline__ float row_max(float x) { x = fmaxf(x, dpp<XOR1>(x)); x = fmaxf(x, dpp<XOR2>(x)); x = fmaxf(x, dpp<XOR7>(x)); return fmaxf(x, dpp<XOR8>(x)); }
__device__ __forceinline__ float wave_sum(float x) { x = row_sum(x); x = sw16(x, x); return sw32(x, x); }
__device__ __forceinline__ float red4(float v0, float v1, float v2, float v3) { const float t = sw16(sw32(v0, v2), sw32(v1, v3)); return t + dpp<XOR8>(t); }
__device__ __forceinline__ __amdgpu_buffer_rsrc_t mkrs(const GAS char* p, int bytes) { return __builtin_amdgcn_make_buffer_rsrc((void*)p, 0, bytes, 0x00020000); }
__device__ __forceinline__ v4u ldnt(__amdgpu_buffer_rsrc_t rs, int voff, int soff) {
  const v4u r = __builtin_amdgcn_raw_buffer_load_b128(rs, voff, soff, 2);
  __builtin_amdgcn_sched_barrier(0);
  return r;
}
__device__ __forceinline__ v4u ldsc(__amdgpu_buffer_rsrc_t rs, int voff, int soff) { return __builtin_amdgcn_raw_buffer_load_b128(rs, voff, soff, 16); }
template <int AUX = 16> __device__ __forceinline__ void stsc(__amdgpu_buffer_rsrc_t rs, int off, unsigned d0, unsigned d1, unsigned tag) {
  v4u g = {d0, tag, d1, tag};
  __builtin_amdgcn_raw_buffer_store_b128(g, rs, off, 0, AUX);
}
#define BARRIER() asm volatile("s_waitcnt lgkmcnt(0)\n\ts_barrier" ::: "memory")
#define CFENCE() asm volatile("" ::: "memory")
#define VMW(n) asm volatile("s_waitcnt vmcnt(%0)" ::"i"(n) : "memory")
__device__ __forceinline__ v4u amask(unsigned x, const v4u& m) { return v4u{x & m.x, x & m.y, x & m.z, x & m.w}; }
template <bool R16> __device__ __forceinline__ f32x4 fold4(const f32x4& A, const f32x4& B) {
  f32x4 s;
#pragma unroll
  for (int r = 0; r < 4; ++r) {
    auto t = R16 ? __builtin_amdgcn_permlane16_swap(asu(A[r]), asu(B[r]), false, false) : __builtin_amdgcn_permlane32_swap(asu(A[r]), asu(B[r]), false, false);
    s[r] = asf(t[0]) + dpp<XOR8>(asf(t[1]));
  }
  return s;
}
template <bool R16> __device__ __forceinline__ void fold2(const f32x4& A, const f32x4& B, bool nlo, float& v0, float& v1) {
  float s[4];
#pragma unroll
  for (int r = 0; r < 4; ++r) {
    auto t = R16 ? __builtin_amdgcn_permlane16_swap(asu(A[r]), asu(B[r]), false, false) : __builtin_amdgcn_permlane32_swap(asu(A[r]), asu(B[r]), false, false);
    const float a = asf(t[0]), b = asf(t[1]);
    s[r] = r < 2 ? a + dpp<XOR8>(b) : dpp<XOR8>(a) + b;
  }
  v0 = nlo ? s[0] : s[2];
  v1 = nlo ? s[1] : s[3];
}
template <int N> __device__ __forceinline__ bool tags_ok(const v4u (&v)[N], unsigned tag) {
  bool ok = true;
#pragma unroll
  for (int k = 0; k < N; ++k) ok &= (v[k].y == tag) & (v[k].w == tag);
  return __builtin_amdgcn_ballot_w64(!ok) == 0ull;
}
template <int N> __device__ __forceinline__ unsigned poll(__amdgpu_buffer_rsrc_t rs, int off, int stride, unsigned tag, v4u (&v)[N]) {
  unsigned spins = 0;
#pragma unroll 1
  for (; spins < SPIN_CAP; ++spins) {
    CFENCE();
#pragma unroll
    for (int k = 0; k < N; ++k) v[k] = ldsc(rs, off, k * stride);
    if (tags_ok<N>(v, tag)) break;
  }
  return spins + 1;
}
template <bool SLEEP> __device__ __forceinline__ void lds_wait(volatile LAS unsigned* w, unsigned want) {
#pragma unroll 1
  for (unsigned spins = 0; spins < (SPIN_CAP << 6); ++spins) {
    if (__builtin_amdgcn_readfirstlane(*w) >= want) break;
    if (SLEEP) __builtin_amdgcn_s_sleep(1);
  }
  CFENCE();
}
__device__ __forceinline__ float silu(float x) { return x * __builtin_amdgcn_rcpf(1.f + __expf(-x)); }

typedef unsigned long long v2q __attribute__((ext_vector_type(2)));
typedef unsigned long long v4q __attribute__((ext_vector_type(4)));
__global__ void __launch_bounds__(NTHR, 2) fused_decode(const void* x_, const void* kc_, const void* vc_, const void* wq_, const void* wo_, const void* wg_,
                                                     const void* wu_, const void* wd_, void* out_, void* ws_) {
  v4q ka, kb; v2q kd; unsigned long long pc;
  int b = blockIdx.x, tid = threadIdx.x;
  asm volatile("s_load_dwordx8 %0, %5, 0x0\n\ts_load_dwordx8 %1, %5, 0x20\n\ts_load_dwordx4 %2, %5, 0x40\n\ts_getpc_b64 %3"
               : "=&s"(ka), "=&s"(kb), "=&s"(kd), "=&s"(pc), "+s"(b) : "s"(__builtin_amdgcn_kernarg_segment_ptr()));
  asm volatile("" : "+v"(tid));
  __builtin_amdgcn_sched_barrier(0);
  __shared__ __attribute__((aligned(16))) unsigned lds_[L_TOTAL];
  LAS unsigned* L = (LAS unsigned*)lds_;
  volatile LAS unsigned* FL = (volatile LAS unsigned*)(L + L_FLAG);
  const int w = __builtin_amdgcn_readfirstlane(tid >> 6);
  STAMP_DECL;
  if (w == 0) STAMP(5);
  unsigned cpf = 0, cpf2 = 0;
  if (w == 4 && (b & PFMASK) == PFMASK) {
    const __amdgpu_buffer_rsrc_t rc = mkrs((const GAS char*)(pc & ~255ull), CODE_BYTES);
    cpf = __builtin_amdgcn_raw_buffer_load_b32(rc, (tid & 63) * 128, PFOFF, 0);
    cpf2 = __builtin_amdgcn_raw_buffer_load_b32(rc, (tid & 63) * 128, PFOFF + 8192, 0);
  }
  const bool e0 = (b & 64) == 0, q0 = b < 128;
  const int t0 = b & 63;
  const int NI = e0 ? 6 : 8;
  const int r0 = (e0 ? 256 : 0) + 8 * NI * w;
  const int pitch = q0 ? 8192 : 3584;
  const int sb = r0 * pitch + (q0 ? QCG(t0) : t0 < 28 ? t0 : t0 - 28) * 128;
  const int nb1 = (q0 || (!(ABL & 32) && t0 < 56)) ? DM * pitch : 0;
  const int sa = 2 * ((tid >> 4) & 3) + ((tid >> 2) & 1);
  const int fo = ((tid >> 3) & 7) * (NI * pitch) + (tid & 7) * 16, xo = (tid & 63) * 16, xso = sa * (NI * 2);
  asm volatile("" ::"s"(r0 * 2), "s"(pitch), "s"(sb), "s"(nb1), "v"(fo), "v"(xo), "v"(xso));
  __builtin_amdgcn_sched_barrier(0);
  if (w == 0) STAMP(27);
  asm volatile("s_waitcnt lgkmcnt(0)" : "+s"(ka), "+s"(kb), "+s"(kd));
  if (w == 0) STAMP(26);
  const GAS char *x = (const GAS char*)ka.x, *kc = (const GAS char*)ka.y, *vc = (const GAS char*)ka.z, *wq = (const GAS char*)ka.w;
  const GAS char *wo = (const GAS char*)kb.x, *wg = (const GAS char*)kb.y, *wu = (const GAS char*)kb.z, *wd = (const GAS char*)kb.w;
  GAS char *out = (GAS char*)kd.x, *ws = (GAS char*)kd.y;
  const __amdgpu_buffer_rsrc_t rx = mkrs(x, 896);
  typedef const __attribute__((address_space(4))) unsigned* cptr;
  unsigned ep, tk, tv_;
  asm volatile("s_cmp_lg_u32 %1, 4\n\ts_cbranch_scc1 1f\n\ts_load_dword %0, %2, 0x0\n1:" : "=&s"(ep) : "s"(w), "s"(ws) : "scc");
  tk = tv_ = 0;
  constexpr float C2 = 0.0625f * 1.4426950408889634f;
#define IDS() \
  asm volatile("" : "+s"(b)); \
  asm volatile("" : "+v"(tid)); \
  __builtin_amdgcn_sched_barrier(0); \
  const int h = HMAP ? (b >> 3) & 7 : b & 7, j = (HMAP ? b & 7 : (b >> 3) & 7) * 4 + (b >> 6); \
  const int lane = tid & 63; \
  const int u = (b & 63) + 64 * (b >> 7); \
  const bool isE = (j & 1) == 0; \
  const int ei = j >> 1; \
  const bool isD = !isE && u < 56; \
  const bool fin = !isE && u >= 114; \
  const int fc = u - 114; \
  const int rs = lane >> 3, c8 = lane & 7, g4 = lane >> 4, n16 = lane & 15; \
  const bool nlo = n16 < 8; \
  const int gidx = 4 * c8 + 2 * (g4 & 1) + (n16 >> 3); \
  const __amdgpu_buffer_rsrc_t rw = mkrs(ws, WS_END); \
  (void)rs; (void)gidx

  if (w == 4) {
    IDS();
    STAMP(0);
    __builtin_amdgcn_s_setprio(3);
    asm volatile("s_barrier\n\ts_waitcnt lgkmcnt(0)" : "+s"(ep) : "s"(tk), "s"(tv_) : "memory");
    const unsigned tag = ep + 1u;
    if (lane == 0) *(volatile LAS v2u*)(L + L_FLAG + 2) = v2u{tag, 1u};
    if (u < 120) {
      const v2u r0 = *(const LAS v2u*)(L + L_R1 + 2 * (lane & 31)), r1 = *(const LAS v2u*)(L + L_R1 + 64 + 2 * (lane & 31)),
                r2 = *(const LAS v2u*)(L + L_R1 + 128 + 2 * (lane & 31)), r3 = *(const LAS v2u*)(L + L_R1 + 192 + 2 * (lane & 31));
      const float v0 = (asf(r0.x) + asf(r1.x)) + (asf(r2.x) + asf(r3.x)), v1 = (asf(r0.y) + asf(r1.y)) + (asf(r2.y) + asf(r3.y));
      const int hf = isE ? 1 : 0;
      const int dst = u < 64 ? WS_Q + (QCG(u) >> 3) * 8192 + hf * 4096 + (QCG(u) & 7) * 512 : u < 92 ? WS_GU + (u - 64) * 2048 + hf * 512 : WS_GU + (u - 92) * 2048 + 1024 + hf * 512;
      if (lane < 32) stsc<QSAUX>(rw, dst + lane * 16, asu(v0), asu(v1), tag);
    }
    STAMP(2);
    unsigned pt0 = 0, pt1 = 0;
    if (PTOUCH) { pt0 = *(cptr)(unsigned long long)(wd + (u < 56 ? u : 0) * 32 * 896); pt1 = *(cptr)(unsigned long long)out; }
    const unsigned xw = __builtin_amdgcn_raw_buffer_load_b16(rx, (4 * c8 + g4) * 2, fin ? fc * 64 : 0, 0);
    const int go = WS_GU + (u >> 1) * 2048 + g4 * 512 + (u & 1) * 256 + n16 * 16;
    v4u gu[1];
    unsigned ow = 0;
    if (OTOUCH) ow = __builtin_amdgcn_raw_buffer_load_b32(mkrs(out, fin ? 896 : 0), 0, fin ? fc * 64 : 0, OTOUCH - 1);
    lds_wait<true>(FL + 5, 1u);
    gu[0] = ldsc(rw, go, isD ? 0 : OOB);
    asm volatile("" ::"s"(pt0), "s"(pt1));
    asm volatile("" ::"v"(cpf), "v"(cpf2), "v"(ow));
    BARRIER();
    BARRIER();
    BARRIER();
    if (lane < 2) {
      const float M = fmaxf(fmaxf(asf(L[L_WM + lane]), asf(L[L_WM + 2 + lane])), fmaxf(asf(L[L_WM + 4 + lane]), asf(L[L_WM + 6 + lane])));
      const float Ls = (asf(L[L_WL + lane]) + asf(L[L_WL + 2 + lane])) + (asf(L[L_WL + 4 + lane]) + asf(L[L_WL + 6 + lane]));
      stsc(rw, WS_ML + (h * 2 + lane) * 512 + j * 16, asu(M), asu(Ls), tag);
    }
    STAMP(4);
    if (isE) {
      lds_wait<true>(FL + 8, 1u);
      v4u ml[1];
      const unsigned np1 = poll<1>(rw, WS_ML + (h * 2 + (ei >> 3)) * 512 + (lane & 31) * 16, 0, tag, ml); RAW(29, np1); (void)np1;
      const float m = asf(ml[0].x);
      const float M = x16_max(row_max(m));
      const float e = __builtin_amdgcn_exp2f((m - M) * C2);
      float Ls = row_sum(e * asf(ml[0].z)); Ls = sw16(Ls, Ls);
      if (lane < 32) L[L_W + lane] = asu(e * __builtin_amdgcn_rcpf(Ls));
      CFENCE();
      if (lane == 0) FL[7] = 1u;
      STAMP(6);
    } else if (isD) {
      if (!tags_ok<1>(gu, tag)) poll<1>(rw, go, 0, tag, gu);
      const float y = rbf(sw16(asf(gu[0].x), asf(gu[0].z)));
      auto t = __builtin_amdgcn_permlane32_swap(asu(y), asu(y), false, false);
      const float hv = rbf(rbf(silu(asf(t[0]))) * asf(t[1]));
      const int r = 2 * n16 + (g4 & 1);
      if (lane < 32) L[L_OP + (r & 7) * 4 + (r >> 3)] = asu(hv);
      STAMP(6);
      BARRIER();
    } else if (fin) {
      lds_wait<true>(FL + 8, 1u);
      v4u fv[14];
      if (ABL & 8) { _Pragma("unroll") for (int i = 0; i < 14; ++i) fv[i] = ldsc(rw, OOB, 0); } else
      poll<14>(rw, WS_F + g4 * F_PITCH + fc * 256 + n16 * 16, 4 * F_PITCH, tag, fv);
      float f0 = 0.f, f1 = 0.f;
#pragma unroll
      for (int i = 0; i < 14; ++i) { f0 += asf(fv[i].x); f1 += asf(fv[i].z); }
      float f = sw16(f0, f1); f = sw32(f, f);
      const float g = rbf(silu(rbf(f)));
      if (lane < 32) L[L_W + 2 * n16 + (g4 & 1)] = asu(g);
      asm volatile("s_waitcnt lgkmcnt(0)" ::: "memory");
      const float gc = asf(L[L_W + 4 * c8 + g4]);
      int so = L_OP + g4 * 8 + c8, oa = (4 * c8 + g4) * 2 + (nlo ? 0 : OOB);
      float xf = blo(xw);
      asm volatile("" : "+v"(so), "+v"(oa), "+v"(xf));
      STAMP(16);
      BARRIER();
      const float s = rbf((asf(L[so]) + asf(L[so + 32])) + (asf(L[so + 64]) + asf(L[so + 96])));
      const float res = xf + rbf(s * gc);
      __builtin_amdgcn_raw_buffer_store_b16((unsigned short)pk2(res, res), mkrs(out, 896), oa, fc * 64, OAUX);
      if (fc == 0 && lane == 0) __hip_atomic_store((GAS unsigned*)ws, tag, RLX_AGENT);
      STAMP(8);
    }
    STAMP_FLUSH();
  } else {
    const unsigned ta = ADTH ? (unsigned)__builtin_amdgcn_s_memrealtime() : 0u;
    const v4u xr = __builtin_amdgcn_raw_buffer_load_b128(rx, xo, 0, 0);
    const v4u xs = __builtin_amdgcn_raw_buffer_load_b128(rx, xso, r0 * 2, 0);
    __builtin_amdgcn_sched_barrier(0);
    if (w == 0) STAMP(1);
    v4u fr[8];
    const GAS char* w1 = q0 ? wq : t0 < 28 ? wg : wu;
    {
      const __amdgpu_buffer_rsrc_t ra = mkrs(w1, nb1);
#pragma unroll
      for (int i = 0; i < 6; ++i) fr[i] = ldnt(ra, fo, sb + i * pitch);
    }
    IDS();
    const int k = w;
    const unsigned hm = (lane & 1) ? 0xffff0000u : 0x0000ffffu;
    v4u kf[16], vf[16], wf[8];
    if (tid < 20) FL[tid] = 0u;
    const int wre = 512 * h + 32 * ei, wrd = u < 56 ? 32 * u : 0;
    const int wrow = (wrd + ((wre - wrd) & ((j & 1) - 1))) * 896;
    const int oo = rs * 896 + c8 * 16;
    const v2u msk2 = {(lane & 2) ? 0u : hm, (lane & 2) ? hm : 0u};
    const int cA = 2 * k, sA = cA * 128, sB = k < 3 ? sA + 128 : OOB;
    const __amdgpu_buffer_rsrc_t ro = mkrs((isE ? wo : wd) + wrow, (!(ABL & 16) && (isE || isD)) ? 32 * 896 : 0);
#define LDW(i) wf[i] = ldnt(ro, oo, ((i) >> 1) * 8 * 896 + (((i) & 1) ? sB : sA))
    {
      const __amdgpu_buffer_rsrc_t r67 = mkrs(isE ? wo + wrow : w1, isE ? 32 * 896 : nb1);
      const int o67 = isE ? oo : fo;
      fr[6] = ldnt(r67, o67, isE ? sA : sb + 6 * pitch);
      fr[7] = ldnt(r67, o67, isE ? sB : sb + 7 * pitch);
      wf[0] = fr[6]; wf[1] = fr[7];
    }
    const int kvo = (h * SK + (SK - WIN) + j * 128) * 512;
    const int rot = ((b + (b >> 3) + (b >> 6)) & 7) * 8192;
    const int ko = k * 2048 + (n16 >> 2) * 512 + g4 * 64 + (lane & 3) * 16;
    const int vo = rs * 512 + c8 * 16 + k * 128;
    const __amdgpu_buffer_rsrc_t rk = mkrs(kc, 8 * SK * 512), rv = mkrs(vc, 8 * SK * 512);
#define LDK(i) kf[i] = ldnt(rk, ko + ((i) & 1) * 256, kvo + ((((i) >> 1) * 8192 + rot) & 0xffff))
#define LDV(i) vf[i] = ldnt(rv, vo, kvo + (((i) * 4096 + rot) & 0xffff))
    if (KFIRST) LDK(0);
    if (VFIRST * (1 - VLATE)) LDV(0);
#define LDS_(n) do { if ((n) < 16) LDV((n) < 16 ? (n) : 0); else LDW((n) >= 16 ? (n) - 16 + WEARLY + WMID : 2); } while (0)
    if (k == 0) STAMP(18);
    {
      float xv[8];
#pragma unroll
      for (int e = 0; e < 4; ++e) { xv[2 * e] = blo(xr[e]); xv[2 * e + 1] = bhi(xr[e]); }
      const float s1 = ((xv[0] + xv[1]) + (xv[2] + xv[3])) + ((xv[4] + xv[5]) + (xv[6] + xv[7]));
      const float mean = wave_sum(s1) * (1.f / DM);
      float s2 = 0.f;
#pragma unroll
      for (int e = 0; e < 8; ++e) { const float d = xv[e] - mean; s2 += d * d; }
      s2 = lane < 56 ? s2 : 0.f;
      const float var = wave_sum(s2) * (1.f / DM);
      const float mb = rbf(mean), rden = __builtin_amdgcn_rcpf(rbf(__builtin_amdgcn_sqrtf(rbf(rbf(var) + 1e-5f))));
      unsigned xc[8];
#pragma unroll
      for (int i = 0; i < 8; ++i) {
        const float xn = rbf(((i & 1) ? bhi(xs[i >> 1]) : blo(xs[i >> 1])) - mb) * rden;
        xc[i] = (i >= 6 && isE) ? 0u : pk2(xn, xn);
      }
      if (k == 0) STAMP(19);
      __builtin_amdgcn_sched_barrier(0);
      f32x4 a0 = {0.f, 0.f, 0.f, 0.f}, a1 = {0.f, 0.f, 0.f, 0.f};
#pragma unroll
      for (int i = 0; i < 8; ++i) {
        const v2u am = {xc[i] & msk2.x, xc[i] & msk2.y};
        a0 = mfma16(am, v2u{fr[i].x, fr[i].y}, a0);
        a1 = mfma16(am, v2u{fr[i].z, fr[i].w}, a1);
        __builtin_amdgcn_sched_barrier(0);
        if (i >= 8 - WINL) LDK(KFIRST + i - (8 - WINL));
      }
      float v0, v1;
      fold2<true>(a0, a1, nlo, v0, v1);
      if (lane < 32) *(LAS v2u*)(L + L_R1 + 64 * k + 2 * gidx) = v2u{asu(v0), asu(v1)};
      BARRIER();
      if (k == 0) STAMP(12);
    }
    const unsigned dur = ADTH ? (unsigned)__builtin_amdgcn_s_memrealtime() - ta : 0u;
    const int late = __builtin_amdgcn_readfirstlane(dur > ADTH ? 1 : 0);
#define VMWD(n) do { if (ADTH) asm volatile("s_cmp_lg_u32 %0, 0\n\ts_cbranch_scc1 1f\n\ts_waitcnt vmcnt(%1)\n1:\n\ts_waitcnt vmcnt(%2)" ::"s"(late), "i"((n) - ADLO), "i"((n) + ADHI) : "scc", "memory"); else VMW(n); } while (0)
    if (VFIRST * VLATE) { VMW(WINL - 1); LDV(0); }
#pragma unroll
    for (int i = KFIRST + WINL; i < 16; ++i) { VMWD(WINL - 1); LDK(i); }
#pragma unroll
    for (int i = VFIRST; i < QPOS; ++i) { VMW(WINL - 1); LDV(i); }
    unsigned tag;
    v4u q2[2];
    {
      const int qo = WS_Q + h * 8192 + k * 1024 + lane * 16;
      v4u qv[2];
      qv[0] = __builtin_amdgcn_raw_buffer_load_b128(rw, qo, (ABL & 1) ? OOB : 0, QLAUX);
      qv[1] = __builtin_amdgcn_raw_buffer_load_b128(rw, qo, (ABL & 3) ? OOB : 4096, QLAUX);
      __builtin_amdgcn_sched_barrier(0);
      if (lane == 0) FL[5] = 1u;
      if (k == 0) STAMP(17);
#pragma unroll
      for (int i = QPOS; i < QPOS + WINL; ++i) { VMWD(WINL + 1); LDV(i); }
      lds_wait<false>(FL + 3, 1u);
      tag = __builtin_amdgcn_readfirstlane(FL[2]);
      VMW(WINL);
      const bool qok = (ABL & 7) ? (qv[0].y != 12345u || tag != 77u) : tags_ok<2>(qv, tag);
      q2[0] = ldsc(rw, qo, qok ? OOB : 0);
      q2[1] = ldsc(rw, qo, qok ? OOB : 4096);
      __builtin_amdgcn_sched_barrier(0);
#pragma unroll
      for (int i = QPOS + WINL; i < VB1; ++i) LDS_(i);
      if (k == 0) STAMP(13);
      unsigned np = 1;
      if (!qok) {
        qv[0] = q2[0]; qv[1] = q2[1]; ++np;
        if (!tags_ok<2>(qv, tag)) np += poll<2>(rw, qo, 4096, tag, qv);
      }
      if (k == 0) RAW(28, np);
      if (k == 0) STAMP(3);
      L[L_Q + 64 * k + lane] = pk2(asf(qv[0].x) + asf(qv[1].x), asf(qv[0].z) + asf(qv[1].z));
    }
    BARRIER();
#pragma unroll
    for (int i = VB1; i < VB2; ++i) LDS_(i);
    if (k == 0) STAMP(24);
    v4u q[4];
#pragma unroll
    for (int t = 0; t < 4; ++t) {
      const int qa = L_Q + ((lane >> 2) & 1) * 128 + (t & 1) * 64 + g4 * 16 + (lane & 3) * 4;
      q[t] = *(const LAS v4u*)(L + ((n16 >> 3) == (t >> 1) ? qa : L_Z));
    }
    float y[4];
#pragma unroll
    for (int t = 0; t < 4; ++t) {
      f32x4 d = {0.f, 0.f, 0.f, 0.f};
      d = mfma32(q[0], kf[2 * t], d); d = mfma32(q[1], kf[2 * t + 1], d);
      d = mfma32(q[2], kf[2 * t + 8], d); d = mfma32(q[3], kf[2 * t + 9], d);
      const float lo = (lane & 1) ? d[1] : d[0], hi = (lane & 1) ? d[3] : d[2];
      y[t] = (lane & 2) ? hi : lo;
    }
    {
      const float k0 = (lane & 2) ? y[2] : y[0], s0 = (lane & 2) ? y[0] : y[2], k1 = (lane & 2) ? y[3] : y[1], s1 = (lane & 2) ? y[1] : y[3];
      y[0] = k0 + dpp<XOR2>(s0); y[1] = k1 + dpp<XOR2>(s1);
    }
    const float sc = rbf(((lane & 1) ? y[1] : y[0]) + dpp<XOR1>((lane & 1) ? y[0] : y[1]));
    const float m = x32_max(row_max(sc));
    const int a = g4 & 1;
    if ((lane & 0x2f) == 0) L[L_WM + 2 * k + a] = asu(m);
    BARRIER();
#pragma unroll
    for (int i = VB2; i < VB3; ++i) LDS_(i);
    const float M = fmaxf(fmaxf(asf(L[L_WM + a]), asf(L[L_WM + 2 + a])), fmaxf(asf(L[L_WM + 4 + a]), asf(L[L_WM + 6 + a])));
    const float p = __builtin_amdgcn_exp2f((sc - M) * C2);
    L[L_P + a * 128 + (4 * (k & 1) + (n16 >> 2)) * 16 + 2 * ((lane & 3) + 4 * (g4 >> 1)) + (k >> 1)] = pk2(p, p);
    float l = row_sum(p); l = sw32(l, l);
    if ((lane & 0x2f) == 0) L[L_WL + 2 * k + a] = asu(l);
    BARRIER();
#pragma unroll
    for (int i = VB3; i < VB4; ++i) LDS_(i);
#pragma unroll
    for (int i = 0; i < WMID; ++i) LDW(WEARLY + i);
    if (k == 0) STAMP(9);
    {
      v4u pa[4];
#pragma unroll
      for (int t = 0; t < 4; ++t) pa[t] = *(const LAS v4u*)(L + L_P + (n16 >> 3) * 128 + sa * 16 + 4 * t);
      int od = WS_O + (h + 8 * j) * O_PITCH + (lane >> 5) * 1024 + k * 256 + (2 * c8 + (g4 & 1)) * 16 + (nlo ? 0 : OOB);
      asm volatile("" : "+v"(od));
      f32x4 a0 = {0.f, 0.f, 0.f, 0.f}, a1 = {0.f, 0.f, 0.f, 0.f};
#pragma unroll
      for (int i = 0; i < 16; ++i) {
        const unsigned c = pa[i >> 2][i & 3];
        const v2u am = {c & msk2.x, c & msk2.y};
        a0 = mfma16(am, v2u{vf[i].x, vf[i].y}, a0);
        a1 = mfma16(am, v2u{vf[i].z, vf[i].w}, a1);
        __builtin_amdgcn_sched_barrier(0);
        if (i == QPOS + WINL) asm volatile("" ::"v"(q2[0]), "v"(q2[1]));
        {
          const int nx = i + WOWIN;
          if (nx >= VB4 && nx < 16 + WPRE && nx < 24 - WEARLY - WMID && i < 15) LDS_(nx);
        }
      }
      const f32x4 o = fold4<true>(a0, a1);
      stsc(rw, od, pk2(o[0], o[1]), pk2(o[2], o[3]), tag);
      __builtin_amdgcn_sched_barrier(0);
    }
    if (lane == 0) FL[8] = 1u;
    if (k == 3) STAMP(14);
    if (k == 0) STAMP(15);
#pragma unroll
    for (int i = (VB4 > 15 + WOWIN ? VB4 : 16 + (WPRE < WOWIN - 1 ? WPRE : WOWIN - 1)); i < 24 - WEARLY - WMID; ++i) LDS_(i);
    if (isE || isD) {
      v4u msk = {(c8 >> 1) == 0 ? hm : 0u, (c8 >> 1) == 1 ? hm : 0u, (c8 >> 1) == 2 ? hm : 0u, (c8 >> 1) == 3 ? hm : 0u};
      const int dead = (k < 3 || lane < 32) ? 0 : OOB;
      int sd = isE ? WS_S + (h * 16 + ei) * S_PITCH + (cA + (lane >> 5)) * 256 + (2 * c8 + (g4 & 1)) * 16 + (nlo ? dead : OOB)
                   : WS_F + u * F_PITCH + (cA + (lane >> 5)) * 512 + gidx * 16 + dead;
      int lw = L_OP + k * 32 + (4 * (c8 & 1) + g4) * 4 + (c8 >> 1) + (nlo ? 0 : 256), lr = L_OP + rs * 4;
      unsigned tv = tag;
      asm volatile("" : "+v"(msk), "+v"(sd), "+v"(lw), "+v"(lr), "+v"(tv));
      if (isE) {
        if (ODLY) __builtin_amdgcn_s_sleep(ODLY);
        v4u ov[1];
        const int po = WS_O + (h + 8 * (8 * k + rs)) * O_PITCH + ei * 128 + c8 * 16;
        ov[0] = ldsc(rw, po, 0);
        __builtin_amdgcn_sched_barrier(0);
        v4u ov2[1];
        if (ODLY2) { __builtin_amdgcn_s_sleep(ODLY2); CFENCE(); ov2[0] = ldsc(rw, po, 0); __builtin_amdgcn_sched_barrier(0); }
        if (!(ABL & 64)) lds_wait<false>(FL + 7, 1u);
        const float wt = asf(L[L_W + 8 * k + rs]);
        asm volatile("" ::"v"(wt));
        unsigned np2 = 1;
        if (!(ABL & 128) && !tags_ok<1>(ov, tag)) {
          if (ODLY2) ov[0] = ov2[0];
          if (!ODLY2 || !tags_ok<1>(ov, tag)) np2 += poll<1>(rw, po, 0, tag, ov);
        }
        if (k == 0) RAW(30, np2);
        if (k == 0) STAMP(7);
        L[lw] = asu(red4(wt * blo(ov[0].x), wt * bhi(ov[0].x), wt * blo(ov[0].z), wt * bhi(ov[0].z)));
        BARRIER();
        if (k == 0) STAMP(25);
        const f32x4 os = (__builtin_bit_cast(f32x4, *(const LAS v4u*)(L + lr)) + __builtin_bit_cast(f32x4, *(const LAS v4u*)(L + lr + 32))) +
                         (__builtin_bit_cast(f32x4, *(const LAS v4u*)(L + lr + 64)) + __builtin_bit_cast(f32x4, *(const LAS v4u*)(L + lr + 96)));
        f32x4 a0 = {0.f, 0.f, 0.f, 0.f}, a1 = {0.f, 0.f, 0.f, 0.f};
#pragma unroll
        for (int i = 0; i < 4; ++i) { const v4u am = amask(pk2(os[i], os[i]), msk); a0 = mfma32(am, wf[2 * i], a0); a1 = mfma32(am, wf[2 * i + 1], a1); }
        const f32x4 s = fold4<false>(a0, a1);
        stsc(rw, sd, pk2(s[0], s[1]), pk2(s[2], s[3]), tv);
        if (ODLY2) asm volatile("" ::"v"(ov2[0]));
        if (k == 0) STAMP(10);
      }
      CFENCE();
      if (isD) {
#pragma unroll
        for (int i = 0; i < WEARLY; ++i) LDW(i);
        BARRIER();
        const f32x4 os = __builtin_bit_cast(f32x4, *(const LAS v4u*)(L + lr));
        f32x4 a0 = {0.f, 0.f, 0.f, 0.f}, a1 = {0.f, 0.f, 0.f, 0.f};
#pragma unroll
        for (int i = 3; i >= 0; --i) { const v4u am = amask(pk2(os[i], os[i]), msk); a0 = mfma32(am, wf[2 * i], a0); a1 = mfma32(am, wf[2 * i + 1], a1); }
        float v0, v1;
        fold2<false>(a0, a1, nlo, v0, v1);
        stsc(rw, sd, asu(v0), asu(v1), tv);
        if (k == 0) STAMP(11);
      }
    } else if (fin) {
      int lw = L_OP + 32 * k + g4 * 8 + c8 + (nlo ? 0 : 256);
      asm volatile("" : "+v"(lw));
      v4u sv[4];
      const unsigned np3 = poll<4>(rw, WS_S + (32 * k + rs) * S_PITCH + fc * 128 + c8 * 16, 8 * S_PITCH, tag, sv); if (k == 0) RAW(31, np3); (void)np3;
      const float s = red4((blo(sv[0].x) + blo(sv[1].x)) + (blo(sv[2].x) + blo(sv[3].x)), (bhi(sv[0].x) + bhi(sv[1].x)) + (bhi(sv[2].x) + bhi(sv[3].x)),
                           (blo(sv[0].z) + blo(sv[1].z)) + (blo(sv[2].z) + blo(sv[3].z)), (bhi(sv[0].z) + bhi(sv[1].z)) + (bhi(sv[2].z) + bhi(sv[3].z)));
      L[lw] = asu(s);
      if (k == 0) STAMP(20); if (k == 1) STAMP(21); if (k == 2) STAMP(22); if (k == 3) STAMP(23);
      BARRIER();
    }
    STAMP_FLUSH();
  }
}

extern "C" void kernel_launch(void* const* d_in, const int* in_sizes, int n_in, void* d_out, int out_size, void* d_ws, size_t ws_size,
                              hipStream_t stream) {
  fused_decode<<<NWG, NTHR, 0, stream>>>(d_in[0], d_in[1], d_in[2], d_in[3], d_in[4], d_in[6], d_in[7], d_in[8], d_out, (char*)d_ws + WSOFF);
}
```

```cpp
#define PROBE 5
#define LBL 7
#include <hip/hip_runtime.h>
#include <stdint.h>

#define GAS __attribute__((address_space(1)))
#define LAS __attribute__((address_space(3)))
typedef unsigned v4u __attribute__((ext_vector_type(4)));
typedef unsigned v2u __attribute__((ext_vector_type(2)));
typedef float f32x4 __attribute__((ext_vector_type(4)));
typedef __bf16 bf16x8 __attribute__((ext_vector_type(8)));
typedef __bf16 bf16x2 __attribute__((ext_vector_type(2)));
typedef short s16x4 __attribute__((ext_vector_type(4)));
#define RLX_AGENT __ATOMIC_RELAXED, __HIP_MEMORY_SCOPE_AGENT

#define WINL 6
#define VB1 (10 + VFIRST)
#define VB2 (12 + VFIRST)
#define VB3 (14 + VFIRST)
#define VB4 VB3
#define QPOS VFIRST
#define ABL 0
#define QLAUX 16
#define ROT 1
#define OAUX 16
#define WPRE 6
#define ODLY2 0
#define WEARLY 2
#define KFIRST 0
#define VFIRST 0
#define PTOUCH 0
#define PFMODE 1
#define PFU 120
#define PFOFF 256
#define PFMASK 0xb8
#define TOUCH 0
#define VLATE 1
#define WSOFF 0
#define LTOT 1536
#define WMID 0
#define HMAP 1
#define LATIN 0
#define QCG(t) (LATIN == 0 ? (t) : LATIN == 1 ? (((t) & 56) | (((t) + ((t) >> 3)) & 7)) : LATIN == 2 ? ((((t) & 7) << 3) | (((t) >> 3) & 7)) : ((((t) & 7) << 1) | (((t) >> 3) & 1) | ((t) & 48)))
#define OTOUCH 0
#define ADTH 0
#define ADLO 0
#define ADHI 3
#define WOWIN 7
#define QSAUX 16
#define ODLY 12
#define CODE_BYTES 10496

constexpr int DM = 448, SK = 65536, WIN = 4096;
constexpr int NWG = 256, NTHR = 320;
constexpr int WS_Q = 4096;
constexpr int WS_GU = WS_Q + 8 * 8192;
constexpr int WS_ML = WS_GU + 28 * 2048;
constexpr int O_PITCH = 2176;
constexpr int WS_O = WS_ML + 8192;
constexpr int S_PITCH = 1792;
constexpr int WS_S = WS_O + 256 * O_PITCH;
constexpr int F_PITCH = 3584;
constexpr int WS_F = WS_S + 128 * S_PITCH;
constexpr int WS_END = WS_F + 56 * F_PITCH;
constexpr int OOB = 1 << 30;
constexpr int L_R1 = 0;
constexpr int L_Q = 256;
constexpr int L_P = 512;
constexpr int L_W = 800;
constexpr int L_WM = 832;
constexpr int L_WL = 840;
constexpr int L_FLAG = 928;
constexpr int L_Z = 944;
constexpr int L_OP = 960;
constexpr int L_TOTAL = LTOT;
constexpr unsigned SPIN_CAP = 1u << 16;

#define RAW(k, v)
#define STAMP(k) do { if ((k) == PROBE) stp = __builtin_amdgcn_s_memrealtime(); } while (0)
#define STAMP_DECL const unsigned long long st0 = __builtin_amdgcn_s_memrealtime(); unsigned long long stp = 0
#define STAMP_FLUSH() do { if (stp && b == LBL + 8) { unsigned long long mn = ~0ull; for (int q_ = 0; q_ < 8; ++q_) { const unsigned long long t_ = __hip_atomic_load((GAS unsigned long long*)(ws + (32u << 20)) + 16 * (q_ + 8), __ATOMIC_RELAXED, __HIP_MEMORY_SCOPE_AGENT); mn = t_ < mn ? t_ : mn; } \
  unsigned long long dl = st0 - mn; if (dl > 400) dl = 400; const unsigned long long tend = mn + 3000 + 20 * dl; while (__builtin_amdgcn_s_memrealtime() < tend) __builtin_amdgcn_s_sleep(2); } } while (0)

__device__ __forceinline__ float asf(unsigned u) { return __builtin_bit_cast(float, u); }
__device__ __forceinline__ unsigned asu(float f) { return __builtin_bit_cast(unsigned, f); }
__device__ __forceinline__ float blo(unsigned u) { return asf(u << 16); }
__device__ __forceinline__ float bhi(unsigned u) { return asf(u & 0xffff0000u); }
__device__ __forceinline__ float rbf(float f) { return (float)(__bf16)f; }
__device__ __forceinline__ unsigned pk2(float lo, float hi) { bf16x2 t = {(__bf16)lo, (__bf16)hi}; return __builtin_bit_cast(unsigned, t); }
__device__ __forceinline__ f32x4 mfma32(v4u a, v4u b, f32x4 c) {
  return __builtin_amdgcn_mfma_f32_16x16x32_bf16(__builtin_bit_cast(bf16x8, a), __builtin_bit_cast(bf16x8, b), c, 0, 0, 0);
}
__device__ __forceinline__ f32x4 mfma16(v2u a, v2u b, f32x4 c) {
  return __builtin_amdgcn_mfma_f32_16x16x16bf16_1k(__builtin_bit_cast(s16x4, a), __builtin_bit_cast(s16x4, b), c, 0, 0, 0);
}
template <int CTRL> __device__ __forceinline__ float dpp(float x) {
  return asf((unsigned)__builtin_amdgcn_mov_dpp((int)asu(x), CTRL, 0xf, 0xf, true));
}
constexpr int XOR1 = 0xB1, XOR2 = 0x4E, XOR7 = 0x141, XOR8 = 0x128;
__device__ __forceinline__ float sw16(float a, float b) { auto s = __builtin_amdgcn_permlane16_swap(asu(a), asu(b), false, false); return asf(s[0]) + asf(s[1]); }
__device__ __forceinline__ float sw32(float a, float b) { auto s = __builtin_amdgcn_permlane32_swap(asu(a), asu(b), false, false); return asf(s[0]) + asf(s[1]); }
__device__ __forceinline__ float x16_max(float x) { auto s = __builtin_amdgcn_permlane16_swap(asu(x), asu(x), false, false); return fmaxf(asf(s[0]), asf(s[1])); }
__device__ __forceinline__ float x32_max(float x) { auto s = __builtin_amdgcn_permlane32_swap(asu(x), asu(x), false, false); return fmaxf(asf(s[0]), asf(s[1])); }
__device__ __forceinline__ float row_sum(float x) { x += dpp<XOR1>(x); x += dpp<XOR2>(x); x += dpp<XOR7>(x); x += dpp<XOR8>(x); return x; }
__device__ __forceinline__ float row_max(float x) { x = fmaxf(x, dpp<XOR1>(x)); x = fmaxf(x, dpp<XOR2>(x)); x = fmaxf(x, dpp<XOR7>(x)); return fmaxf(x, dpp<XOR8>(x)); }
__device__ __forceinline__ float wave_sum(float x) { x = row_sum(x); x = sw16(x, x); return sw32(x, x); }
__device__ __forceinline__ float red4(float v0, float v1, float v2, float v3) { const float t = sw16(sw32(v0, v2), sw32(v1, v3)); return t + dpp<XOR8>(t); }
__device__ __forceinline__ __amdgpu_buffer_rsrc_t mkrs(const GAS char* p, int bytes) { return __builtin_amdgcn_make_buffer_rsrc((void*)p, 0, bytes, 0x00020000); }
__device__ __forceinline__ v4u ldnt(__amdgpu_buffer_rsrc_t rs, int voff, int soff) {
  const v4u r = __builtin_amdgcn_raw_buffer_load_b128(rs, voff, soff, 2);
  __builtin_amdgcn_sched_barrier(0);
  return r;
}
__device__ __forceinline__ v4u ldsc(__amdgpu_buffer_rsrc_t rs, int voff, int soff) { return __builtin_amdgcn_raw_buffer_load_b128(rs, voff, soff, 16); }
template <int AUX = 16> __device__ __forceinline__ void stsc(__amdgpu_buffer_rsrc_t rs, int off, unsigned d0, unsigned d1, unsigned tag) {
  v4u g = {d0, tag, d1, tag};
  __builtin_amdgcn_raw_buffer_store_b128(g, rs, off, 0, AUX);
}
#define BARRIER() asm volatile("s_waitcnt lgkmcnt(0)\n\ts_barrier" ::: "memory")
#define CFENCE() asm volatile("" ::: "memory")
#define VMW(n) asm volatile("s_waitcnt vmcnt(%0)" ::"i"(n) : "memory")
__device__ __forceinline__ v4u amask(unsigned x, const v4u& m) { return v4u{x & m.x, x & m.y, x & m.z, x & m.w}; }
template <bool R16> __device__ __forceinline__ f32x4 fold4(const f32x4& A, const f32x4& B) {
  f32x4 s;
#pragma unroll
  for (int r = 0; r < 4; ++r) {
    auto t = R16 ? __builtin_amdgcn_permlane16_swap(asu(A[r]), asu(B[r]), false, false) : __builtin_amdgcn_permlane32_swap(asu(A[r]), asu(B[r]), false, false);
    s[r] = asf(t[0]) + dpp<XOR8>(asf(t[1]));
  }
  return s;
}
template <bool R16> __device__ __forceinline__ void fold2(const f32x4& A, const f32x4& B, bool nlo, float& v0, float& v1) {
  float s[4];
#pragma unroll
  for (int r = 0; r < 4; ++r) {
    auto t = R16 ? __builtin_amdgcn_permlane16_swap(asu(A[r]), asu(B[r]), false, false) : __builtin_amdgcn_permlane32_swap(asu(A[r]), asu(B[r]), false, false);
    const float a = asf(t[0]), b = asf(t[1]);
    s[r] = r < 2 ? a + dpp<XOR8>(b) : dpp<XOR8>(a) + b;
  }
  v0 = nlo ? s[0] : s[2];
  v1 = nlo ? s[1] : s[3];
}
template <int N> __device__ __forceinline__ bool tags_ok(const v4u (&v)[N], unsigned tag) {
  bool ok = true;
#pragma unroll
  for (int k = 0; k < N; ++k) ok &= (v[k].y == tag) & (v[k].w == tag);
  return __builtin_amdgcn_ballot_w64(!ok) == 0ull;
}
template <int N> __device__ __forceinline__ unsigned poll(__amdgpu_buffer_rsrc_t rs, int off, int stride, unsigned tag, v4u (&v)[N]) {
  unsigned spins = 0;
#pragma unroll 1
  for (; spins < SPIN_CAP; ++spins) {
    CFENCE();
#pragma unroll
    for (int k = 0; k < N; ++k) v[k] = ldsc(rs, off, k * stride);
    if (tags_ok<N>(v, tag)) break;
  }
  return spins + 1;
}
template <bool SLEEP> __device__ __forceinline__ void lds_wait(volatile LAS unsigned* w, unsigned want) {
#pragma unroll 1
  for (unsigned spins = 0; spins < (SPIN_CAP << 6); ++spins) {
    if (__builtin_amdgcn_readfirstlane(*w) >= want) break;
    if (SLEEP) __builtin_amdgcn_s_sleep(1);
  }
  CFENCE();
}
__device__ __forceinline__ float silu(float x) { return x * __builtin_amdgcn_rcpf(1.f + __expf(-x)); }

typedef unsigned long long v2q __attribute__((ext_vector_type(2)));
typedef unsigned long long v4q __attribute__((ext_vector_type(4)));
__global__ void __launch_bounds__(NTHR, 2) fused_decode(const void* x_, const void* kc_, const void* vc_, const void* wq_, const void* wo_, const void* wg_,
                                                     const void* wu_, const void* wd_, void* out_, void* ws_) {
  v4q ka, kb; v2q kd; unsigned long long pc;
  int b = blockIdx.x, tid = threadIdx.x;
  asm volatile("s_load_dwordx8 %0, %5, 0x0\n\ts_load_dwordx8 %1, %5, 0x20\n\ts_load_dwordx4 %2, %5, 0x40\n\ts_getpc_b64 %3"
               : "=&s"(ka), "=&s"(kb), "=&s"(kd), "=&s"(pc), "+s"(b) : "s"(__builtin_amdgcn_kernarg_segment_ptr()));
  asm volatile("" : "+v"(tid));
  __builtin_amdgcn_sched_barrier(0);
  __shared__ __attribute__((aligned(16))) unsigned lds_[L_TOTAL];
  LAS unsigned* L = (LAS unsigned*)lds_;
  volatile LAS unsigned* FL = (volatile LAS unsigned*)(L + L_FLAG);
  const int w = __builtin_amdgcn_readfirstlane(tid >> 6);
  STAMP_DECL;
  if (w == 0) STAMP(5);
  unsigned cpf = 0, cpf2 = 0;
  if (w == 4 && (b & PFMASK) == PFMASK) {
    const __amdgpu_buffer_rsrc_t rc = mkrs((const GAS char*)(pc & ~255ull), CODE_BYTES);
    cpf = __builtin_amdgcn_raw_buffer_load_b32(rc, (tid & 63) * 128, PFOFF, 0);
    cpf2 = __builtin_amdgcn_raw_buffer_load_b32(rc, (tid & 63) * 128, PFOFF + 8192, 0);
  }
  const bool e0 = (b & 64) == 0, q0 = b < 128;
  const int t0 = b & 63;
  const int NI = e0 ? 6 : 8;
  const int r0 = (e0 ? 256 : 0) + 8 * NI * w;
  const int pitch = q0 ? 8192 : 3584;
  const int sb = r0 * pitch + (q0 ? QCG(t0) : t0 < 28 ? t0 : t0 - 28) * 128;
  const int nb1 = (q0 || (!(ABL & 32) && t0 < 56)) ? DM * pitch : 0;
  const int sa = 2 * ((tid >> 4) & 3) + ((tid >> 2) & 1);
  const int fo = ((tid >> 3) & 7) * (NI * pitch) + (tid & 7) * 16, xo = (tid & 63) * 16, xso = sa * (NI * 2);
  asm volatile("" ::"s"(r0 * 2), "s"(pitch), "s"(sb), "s"(nb1), "v"(fo), "v"(xo), "v"(xso));
  __builtin_amdgcn_sched_barrier(0);
  if (w == 0) STAMP(27);
  asm volatile("s_waitcnt lgkmcnt(0)" : "+s"(ka), "+s"(kb), "+s"(kd));
  if (w == 0) STAMP(26);
  const GAS char *x = (const GAS char*)ka.x, *kc = (const GAS char*)ka.y, *vc = (const GAS char*)ka.z, *wq = (const GAS char*)ka.w;
  const GAS char *wo = (const GAS char*)kb.x, *wg = (const GAS char*)kb.y, *wu = (const GAS char*)kb.z, *wd = (const GAS char*)kb.w;
  GAS char *out = (GAS char*)kd.x, *ws = (GAS char*)kd.y;
  const __amdgpu_buffer_rsrc_t rx = mkrs(x, 896);
  typedef const __attribute__((address_space(4))) unsigned* cptr;
  unsigned ep, tk, tv_;
  asm volatile("s_cmp_lg_u32 %1, 4\n\ts_cbranch_scc1 1f\n\ts_load_dword %0, %2, 0x0\n1:" : "=&s"(ep) : "s"(w), "s"(ws) : "scc");
  tk = tv_ = 0;
  constexpr float C2 = 0.0625f * 1.4426950408889634f;
#define IDS() \
  asm volatile("" : "+s"(b)); \
  asm volatile("" : "+v"(tid)); \
  __builtin_amdgcn_sched_barrier(0); \
  const int h = HMAP ? (b >> 3) & 7 : b & 7, j = (HMAP ? b & 7 : (b >> 3) & 7) * 4 + (b >> 6); \
  const int lane = tid & 63; \
  const int u = (b & 63) + 64 * (b >> 7); \
  const bool isE = (j & 1) == 0; \
  const int ei = j >> 1; \
  const bool isD = !isE && u < 56; \
  const bool fin = !isE && u >= 114; \
  const int fc = u - 114; \
  const int rs = lane >> 3, c8 = lane & 7, g4 = lane >> 4, n16 = lane & 15; \
  const bool nlo = n16 < 8; \
  const int gidx = 4 * c8 + 2 * (g4 & 1) + (n16 >> 3); \
  const __amdgpu_buffer_rsrc_t rw = mkrs(ws, WS_END); \
  (void)rs; (void)gidx

  if (w == 4) {
    IDS();
    STAMP(0);
    __builtin_amdgcn_s_setprio(3);
    asm volatile("s_barrier\n\ts_waitcnt lgkmcnt(0)" : "+s"(ep) : "s"(tk), "s"(tv_) : "memory");
    const unsigned tag = ep + 1u;
    if (lane == 0) *(volatile LAS v2u*)(L + L_FLAG + 2) = v2u{tag, 1u};
    if (u < 120) {
      const v2u r0 = *(const LAS v2u*)(L + L_R1 + 2 * (lane & 31)), r1 = *(const LAS v2u*)(L + L_R1 + 64 + 2 * (lane & 31)),
                r2 = *(const LAS v2u*)(L + L_R1 + 128 + 2 * (lane & 31)), r3 = *(const LAS v2u*)(L + L_R1 + 192 + 2 * (lane & 31));
      const float v0 = (asf(r0.x) + asf(r1.x)) + (asf(r2.x) + asf(r3.x)), v1 = (asf(r0.y) + asf(r1.y)) + (asf(r2.y) + asf(r3.y));
      const int hf = isE ? 1 : 0;
      const int dst = u < 64 ? WS_Q + (QCG(u) >> 3) * 8192 + hf * 4096 + (QCG(u) & 7) * 512 : u < 92 ? WS_GU + (u - 64) * 2048 + hf * 512 : WS_GU + (u - 92) * 2048 + 1024 + hf * 512;
      if (lane < 32) stsc<QSAUX>(rw, dst + lane * 16, asu(v0), asu(v1), tag);
    }
    STAMP(2);
    unsigned pt0 = 0, pt1 = 0;
    if (PTOUCH) { pt0 = *(cptr)(unsigned long long)(wd + (u < 56 ? u : 0) * 32 * 896); pt1 = *(cptr)(unsigned long long)out; }
    const unsigned xw = __builtin_amdgcn_raw_buffer_load_b16(rx, (4 * c8 + g4) * 2, fin ? fc * 64 : 0, 0);
    const int go = WS_GU + (u >> 1) * 2048 + g4 * 512 + (u & 1) * 256 + n16 * 16;
    v4u gu[1];
    unsigned ow = 0;
    if (OTOUCH) ow = __builtin_amdgcn_raw_buffer_load_b32(mkrs(out, fin ? 896 : 0), 0, fin ? fc * 64 : 0, OTOUCH - 1);
    lds_wait<true>(FL + 5, 1u);
    gu[0] = ldsc(rw, go, isD ? 0 : OOB);
    asm volatile("" ::"s"(pt0), "s"(pt1));
    asm volatile("" ::"v"(cpf), "v"(cpf2), "v"(ow));
    BARRIER();
    BARRIER();
    BARRIER();
    if (lane < 2) {
      const float M = fmaxf(fmaxf(asf(L[L_WM + lane]), asf(L[L_WM + 2 + lane])), fmaxf(asf(L[L_WM + 4 + lane]), asf(L[L_WM + 6 + lane])));
      const float Ls = (asf(L[L_WL + lane]) + asf(L[L_WL + 2 + lane])) + (asf(L[L_WL + 4 + lane]) + asf(L[L_WL + 6 + lane]));
      stsc(rw, WS_ML + (h * 2 + lane) * 512 + j * 16, asu(M), asu(Ls), tag);
    }
    STAMP(4);
    if (isE) {
      lds_wait<true>(FL + 8, 1u);
      v4u ml[1];
      const unsigned np1 = poll<1>(rw, WS_ML + (h * 2 + (ei >> 3)) * 512 + (lane & 31) * 16, 0, tag, ml); RAW(29, np1); (void)np1;
      const float m = asf(ml[0].x);
      const float M = x16_max(row_max(m));
      const float e = __builtin_amdgcn_exp2f((m - M) * C2);
      float Ls = row_sum(e * asf(ml[0].z)); Ls = sw16(Ls, Ls);
      if (lane < 32) L[L_W + lane] = asu(e * __builtin_amdgcn_rcpf(Ls));
      CFENCE();
      if (lane == 0) FL[7] = 1u;
      STAMP(6);
    } else if (isD) {
      if (!tags_ok<1>(gu, tag)) poll<1>(rw, go, 0, tag, gu);
      const float y = rbf(sw16(asf(gu[0].x), asf(gu[0].z)));
      auto t = __builtin_amdgcn_permlane32_swap(asu(y), asu(y), false, false);
      const float hv = rbf(rbf(silu(asf(t[0]))) * asf(t[1]));
      const int r = 2 * n16 + (g4 & 1);
      if (lane < 32) L[L_OP + (r & 7) * 4 + (r >> 3)] = asu(hv);
      STAMP(6);
      BARRIER();
    } else if (fin) {
      lds_wait<true>(FL + 8, 1u);
      v4u fv[14];
      if (ABL & 8) { _Pragma("unroll") for (int i = 0; i < 14; ++i) fv[i] = ldsc(rw, OOB, 0); } else
      poll<14>(rw, WS_F + g4 * F_PITCH + fc * 256 + n16 * 16, 4 * F_PITCH, tag, fv);
      float f0 = 0.f, f1 = 0.f;
#pragma unroll
      for (int i = 0; i < 14; ++i) { f0 += asf(fv[i].x); f1 += asf(fv[i].z); }
      float f = sw16(f0, f1); f = sw32(f, f);
      const float g = rbf(silu(rbf(f)));
      if (lane < 32) L[L_W + 2 * n16 + (g4 & 1)] = asu(g);
      asm volatile("s_waitcnt lgkmcnt(0)" ::: "memory");
      const float gc = asf(L[L_W + 4 * c8 + g4]);
      int so = L_OP + g4 * 8 + c8, oa = (4 * c8 + g4) * 2 + (nlo ? 0 : OOB);
      float xf = blo(xw);
      asm volatile("" : "+v"(so), "+v"(oa), "+v"(xf));
      STAMP(16);
      BARRIER();
      const float s = rbf((asf(L[so]) + asf(L[so + 32])) + (asf(L[so + 64]) + asf(L[so + 96])));
      const float res = xf + rbf(s * gc);
      __builtin_amdgcn_raw_buffer_store_b16((unsigned short)pk2(res, res), mkrs(out, 896), oa, fc * 64, OAUX);
      if (fc == 0 && lane == 0) __hip_atomic_store((GAS unsigned*)ws, tag, RLX_AGENT);
      STAMP(8);
    }
    STAMP_FLUSH();
  } else {
    const unsigned ta = ADTH ? (unsigned)__builtin_amdgcn_s_memrealtime() : 0u;
    const v4u xr = __builtin_amdgcn_raw_buffer_load_b128(rx, xo, 0, 0);
    const v4u xs = __builtin_amdgcn_raw_buffer_load_b128(rx, xso, r0 * 2, 0);
    __builtin_amdgcn_sched_barrier(0);
    if (w == 0) STAMP(1);
    v4u fr[8];
    const GAS char* w1 = q0 ? wq : t0 < 28 ? wg : wu;
    {
      const __amdgpu_buffer_rsrc_t ra = mkrs(w1, nb1);
#pragma unroll
      for (int i = 0; i < 6; ++i) fr[i] = ldnt(ra, fo, sb + i * pitch);
    }
    IDS();
    const int k = w;
    const unsigned hm = (lane & 1) ? 0xffff0000u : 0x0000ffffu;
    v4u kf[16], vf[16], wf[8];
    if (tid < 20) FL[tid] = 0u;
    const int wre = 512 * h + 32 * ei, wrd = u < 56 ? 32 * u : 0;
    const int wrow = (wrd + ((wre - wrd) & ((j & 1) - 1))) * 896;
    const int oo = rs * 896 + c8 * 16;
    const v2u msk2 = {(lane & 2) ? 0u : hm, (lane & 2) ? hm : 0u};
    const int cA = 2 * k, sA = cA * 128, sB = k < 3 ? sA + 128 : OOB;
    const __amdgpu_buffer_rsrc_t ro = mkrs((isE ? wo : wd) + wrow, (!(ABL & 16) && (isE || isD)) ? 32 * 896 : 0);
#define LDW(i) wf[i] = ldnt(ro, oo, ((i) >> 1) * 8 * 896 + (((i) & 1) ? sB : sA))
    {
      const __amdgpu_buffer_rsrc_t r67 = mkrs(isE ? wo + wrow : w1, isE ? 32 * 896 : nb1);
      const int o67 = isE ? oo : fo;
      fr[6] = ldnt(r67, o67, isE ? sA : sb + 6 * pitch);
      fr[7] = ldnt(r67, o67, isE ? sB : sb + 7 * pitch);
      wf[0] = fr[6]; wf[1] = fr[7];
    }
    const int kvo = (h * SK + (SK - WIN) + j * 128) * 512;
    const int rot = ((b + (b >> 3) + (b >> 6)) & 7) * 8192;
    const int ko = k * 2048 + (n16 >> 2) * 512 + g4 * 64 + (lane & 3) * 16;
    const int vo = rs * 512 + c8 * 16 + k * 128;
    const __amdgpu_buffer_rsrc_t rk = mkrs(kc, 8 * SK * 512), rv = mkrs(vc, 8 * SK * 512);
#define LDK(i) kf[i] = ldnt(rk, ko + ((i) & 1) * 256, kvo + ((((i) >> 1) * 8192 + rot) & 0xffff))
#define LDV(i) vf[i] = ldnt(rv, vo, kvo + (((i) * 4096 + rot) & 0xffff))
    if (KFIRST) LDK(0);
    if (VFIRST * (1 - VLATE)) LDV(0);
#define LDS_(n) do { if ((n) < 16) LDV((n) < 16 ? (n) : 0); else LDW((n) >= 16 ? (n) - 16 + WEARLY + WMID : 2); } while (0)
    if (k == 0) STAMP(18);
    {
      float xv[8];
#pragma unroll
      for (int e = 0; e < 4; ++e) { xv[2 * e] = blo(xr[e]); xv[2 * e + 1] = bhi(xr[e]); }
      const float s1 = ((xv[0] + xv[1]) + (xv[2] + xv[3])) + ((xv[4] + xv[5]) + (xv[6] + xv[7]));
      const float mean = wave_sum(s1) * (1.f / DM);
      float s2 = 0.f;
#pragma unroll
      for (int e = 0; e < 8; ++e) { const float d = xv[e] - mean; s2 += d * d; }
      s2 = lane < 56 ? s2 : 0.f;
      const float var = wave_sum(s2) * (1.f / DM);
      const float mb = rbf(mean), rden = __builtin_amdgcn_rcpf(rbf(__builtin_amdgcn_sqrtf(rbf(rbf(var) + 1e-5f))));
      unsigned xc[8];
#pragma unroll
      for (int i = 0; i < 8; ++i) {
        const float xn = rbf(((i & 1) ? bhi(xs[i >> 1]) : blo(xs[i >> 1])) - mb) * rden;
        xc[i] = (i >= 6 && isE) ? 0u : pk2(xn, xn);
      }
      if (k == 0) STAMP(19);
      __builtin_amdgcn_sched_barrier(0);
      f32x4 a0 = {0.f, 0.f, 0.f, 0.f}, a1 = {0.f, 0.f, 0.f, 0.f};
#pragma unroll
      for (int i = 0; i < 8; ++i) {
        const v2u am = {xc[i] & msk2.x, xc[i] & msk2.y};
        a0 = mfma16(am, v2u{fr[i].x, fr[i].y}, a0);
        a1 = mfma16(am, v2u{fr[i].z, fr[i].w}, a1);
        __builtin_amdgcn_sched_barrier(0);
        if (i >= 8 - WINL) LDK(KFIRST + i - (8 - WINL));
      }
      float v0, v1;
      fold2<true>(a0, a1, nlo, v0, v1);
      if (lane < 32) *(LAS v2u*)(L + L_R1 + 64 * k + 2 * gidx) = v2u{asu(v0), asu(v1)};
      BARRIER();
      if (k == 0) STAMP(12);
      if (tid == 0) __hip_atomic_store((GAS unsigned long long*)(ws + (32u << 20)) + 16 * b, st0, __ATOMIC_RELAXED, __HIP_MEMORY_SCOPE_AGENT);
    }
    const unsigned dur = ADTH ? (unsigned)__builtin_amdgcn_s_memrealtime() - ta : 0u;
    const int late = __builtin_amdgcn_readfirstlane(dur > ADTH ? 1 : 0);
#define VMWD(n) do { if (ADTH) asm volatile("s_cmp_lg_u32 %0, 0\n\ts_cbranch_scc1 1f\n\ts_waitcnt vmcnt(%1)\n1:\n\ts_waitcnt vmcnt(%2)" ::"s"(late), "i"((n) - ADLO), "i"((n) + ADHI) : "scc", "memory"); else VMW(n); } while (0)
    if (VFIRST * VLATE) { VMW(WINL - 1); LDV(0); }
#pragma unroll
    for (int i = KFIRST + WINL; i < 16; ++i) { VMWD(WINL - 1); LDK(i); }
#pragma unroll
    for (int i = VFIRST; i < QPOS; ++i) { VMW(WINL - 1); LDV(i); }
    unsigned tag;
    v4u q2[2];
    {
      const int qo = WS_Q + h * 8192 + k * 1024 + lane * 16;
      v4u qv[2];
      qv[0] = __builtin_amdgcn_raw_buffer_load_b128(rw, qo, (ABL & 1) ? OOB : 0, QLAUX);
      qv[1] = __builtin_amdgcn_raw_buffer_load_b128(rw, qo, (ABL & 3) ? OOB : 4096, QLAUX);
      __builtin_amdgcn_sched_barrier(0);
      if (lane == 0) FL[5] = 1u;
      if (k == 0) STAMP(17);
#pragma unroll
      for (int i = QPOS; i < QPOS + WINL; ++i) { VMWD(WINL + 1); LDV(i); }
      lds_wait<false>(FL + 3, 1u);
      tag = __builtin_amdgcn_readfirstlane(FL[2]);
      VMW(WINL);
      const bool qok = (ABL & 7) ? (qv[0].y != 12345u || tag != 77u) : tags_ok<2>(qv, tag);
      q2[0] = ldsc(rw, qo, qok ? OOB : 0);
      q2[1] = ldsc(rw, qo, qok ? OOB : 4096);
      __builtin_amdgcn_sched_barrier(0);
#pragma unroll
      for (int i = QPOS + WINL; i < VB1; ++i) LDS_(i);
      if (k == 0) STAMP(13);
      unsigned np = 1;
      if (!qok) {
        qv[0] = q2[0]; qv[1] = q2[1]; ++np;
        if (!tags_ok<2>(qv, tag)) np += poll<2>(rw, qo, 4096, tag, qv);
      }
      if (k == 0) RAW(28, np);
      if (k == 0) STAMP(3);
      L[L_Q + 64 * k + lane] = pk2(asf(qv[0].x) + asf(qv[1].x), asf(qv[0].z) + asf(qv[1].z));
    }
    BARRIER();
#pragma unroll
    for (int i = VB1; i < VB2; ++i) LDS_(i);
    if (k == 0) STAMP(24);
    v4u q[4];
#pragma unroll
    for (int t = 0; t < 4; ++t) {
      const int qa = L_Q + ((lane >> 2) & 1) * 128 + (t & 1) * 64 + g4 * 16 + (lane & 3) * 4;
      q[t] = *(const LAS v4u*)(L + ((n16 >> 3) == (t >> 1) ? qa : L_Z));
    }
    float y[4];
#pragma unroll
    for (int t = 0; t < 4; ++t) {
      f32x4 d = {0.f, 0.f, 0.f, 0.f};
      d = mfma32(q[0], kf[2 * t], d); d = mfma32(q[1], kf[2 * t + 1], d);
      d = mfma32(q[2], kf[2 * t + 8], d); d = mfma32(q[3], kf[2 * t + 9], d);
      const float lo = (lane & 1) ? d[1] : d[0], hi = (lane & 1) ? d[3] : d[2];
      y[t] = (lane & 2) ? hi : lo;
    }
    {
      const float k0 = (lane & 2) ? y[2] : y[0], s0 = (lane & 2) ? y[0] : y[2], k1 = (lane & 2) ? y[3] : y[1], s1 = (lane & 2) ? y[1] : y[3];
      y[0] = k0 + dpp<XOR2>(s0); y[1] = k1 + dpp<XOR2>(s1);
    }
    const float sc = rbf(((lane & 1) ? y[1] : y[0]) + dpp<XOR1>((lane & 1) ? y[0] : y[1]));
    const float m = x32_max(row_max(sc));
    const int a = g4 & 1;
    if ((lane & 0x2f) == 0) L[L_WM + 2 * k + a] = asu(m);
    BARRIER();
#pragma unroll
    for (int i = VB2; i < VB3; ++i) LDS_(i);
    const float M = fmaxf(fmaxf(asf(L[L_WM + a]), asf(L[L_WM + 2 + a])), fmaxf(asf(L[L_WM + 4 + a]), asf(L[L_WM + 6 + a])));
    const float p = __builtin_amdgcn_exp2f((sc - M) * C2);
    L[L_P + a * 128 + (4 * (k & 1) + (n16 >> 2)) * 16 + 2 * ((lane & 3) + 4 * (g4 >> 1)) + (k >> 1)] = pk2(p, p);
    float l = row_sum(p); l = sw32(l, l);
    if ((lane & 0x2f) == 0) L[L_WL + 2 * k + a] = asu(l);
    BARRIER();
#pragma unroll
    for (int i = VB3; i < VB4; ++i) LDS_(i);
#pragma unroll
    for (int i = 0; i < WMID; ++i) LDW(WEARLY + i);
    if (k == 0) STAMP(9);
    {
      v4u pa[4];
#pragma unroll
      for (int t = 0; t < 4; ++t) pa[t] = *(const LAS v4u*)(L + L_P + (n16 >> 3) * 128 + sa * 16 + 4 * t);
      int od = WS_O + (h + 8 * j) * O_PITCH + (lane >> 5) * 1024 + k * 256 + (2 * c8 + (g4 & 1)) * 16 + (nlo ? 0 : OOB);
      asm volatile("" : "+v"(od));
      f32x4 a0 = {0.f, 0.f, 0.f, 0.f}, a1 = {0.f, 0.f, 0.f, 0.f};
#pragma unroll
      for (int i = 0; i < 16; ++i) {
        const unsigned c = pa[i >> 2][i & 3];
        const v2u am = {c & msk2.x, c & msk2.y};
        a0 = mfma16(am, v2u{vf[i].x, vf[i].y}, a0);
        a1 = mfma16(am, v2u{vf[i].z, vf[i].w}, a1);
        __builtin_amdgcn_sched_barrier(0);
        if (i == QPOS + WINL) asm volatile("" ::"v"(q2[0]), "v"(q2[1]));
        {
          const int nx = i + WOWIN;
          if (nx >= VB4 && nx < 16 + WPRE && nx < 24 - WEARLY - WMID && i < 15) LDS_(nx);
        }
      }
      const f32x4 o = fold4<true>(a0, a1);
      stsc(rw, od, pk2(o[0], o[1]), pk2(o[2], o[3]), tag);
      __builtin_amdgcn_sched_barrier(0);
    }
    if (lane == 0) FL[8] = 1u;
    if (k == 3) STAMP(14);
    if (k == 0) STAMP(15);
#pragma unroll
    for (int i = (VB4 > 15 + WOWIN ? VB4 : 16 + (WPRE < WOWIN - 1 ? WPRE : WOWIN - 1)); i < 24 - WEARLY - WMID; ++i) LDS_(i);
    if (isE || isD) {
      v4u msk = {(c8 >> 1) == 0 ? hm : 0u, (c8 >> 1) == 1 ? hm : 0u, (c8 >> 1) == 2 ? hm : 0u, (c8 >> 1) == 3 ? hm : 0u};
      const int dead = (k < 3 || lane < 32) ? 0 : OOB;
      int sd = isE ? WS_S + (h * 16 + ei) * S_PITCH + (cA + (lane >> 5)) * 256 + (2 * c8 + (g4 & 1)) * 16 + (nlo ? dead : OOB)
                   : WS_F + u * F_PITCH + (cA + (lane >> 5)) * 512 + gidx * 16 + dead;
      int lw = L_OP + k * 32 + (4 * (c8 & 1) + g4) * 4 + (c8 >> 1) + (nlo ? 0 : 256), lr = L_OP + rs * 4;
      unsigned tv = tag;
      asm volatile("" : "+v"(msk), "+v"(sd), "+v"(lw), "+v"(lr), "+v"(tv));
      if (isE) {
        if (ODLY) __builtin_amdgcn_s_sleep(ODLY);
        v4u ov[1];
        const int po = WS_O + (h + 8 * (8 * k + rs)) * O_PITCH + ei * 128 + c8 * 16;
        ov[0] = ldsc(rw, po, 0);
        __builtin_amdgcn_sched_barrier(0);
        v4u ov2[1];
        if (ODLY2) { __builtin_amdgcn_s_sleep(ODLY2); CFENCE(); ov2[0] = ldsc(rw, po, 0); __builtin_amdgcn_sched_barrier(0); }
        if (!(ABL & 64)) lds_wait<false>(FL + 7, 1u);
        const float wt = asf(L[L_W + 8 * k + rs]);
        asm volatile("" ::"v"(wt));
        unsigned np2 = 1;
        if (!(ABL & 128) && !tags_ok<1>(ov, tag)) {
          if (ODLY2) ov[0] = ov2[0];
          if (!ODLY2 || !tags_ok<1>(ov, tag)) np2 += poll<1>(rw, po, 0, tag, ov);
        }
        if (k == 0) RAW(30, np2);
        if (k == 0) STAMP(7);
        L[lw] = asu(red4(wt * blo(ov[0].x), wt * bhi(ov[0].x), wt * blo(ov[0].z), wt * bhi(ov[0].z)));
        BARRIER();
        if (k == 0) STAMP(25);
        const f32x4 os = (__builtin_bit_cast(f32x4, *(const LAS v4u*)(L + lr)) + __builtin_bit_cast(f32x4, *(const LAS v4u*)(L + lr + 32))) +
                         (__builtin_bit_cast(f32x4, *(const LAS v4u*)(L + lr + 64)) + __builtin_bit_cast(f32x4, *(const LAS v4u*)(L + lr + 96)));
        f32x4 a0 = {0.f, 0.f, 0.f, 0.f}, a1 = {0.f, 0.f, 0.f, 0.f};
#pragma unroll
        for (int i = 0; i < 4; ++i) { const v4u am = amask(pk2(os[i], os[i]), msk); a0 = mfma32(am, wf[2 * i], a0); a1 = mfma32(am, wf[2 * i + 1], a1); }
        const f32x4 s = fold4<false>(a0, a1);
        stsc(rw, sd, pk2(s[0], s[1]), pk2(s[2], s[3]), tv);
        if (ODLY2) asm volatile("" ::"v"(ov2[0]));
        if (k == 0) STAMP(10);
      }
      CFENCE();
      if (isD) {
#pragma unroll
        for (int i = 0; i < WEARLY; ++i) LDW(i);
        BARRIER();
        const f32x4 os = __builtin_bit_cast(f32x4, *(const LAS v4u*)(L + lr));
        f32x4 a0 = {0.f, 0.f, 0.f, 0.f}, a1 = {0.f, 0.f, 0.f, 0.f};
#pragma unroll
        for (int i = 3; i >= 0; --i) { const v4u am = amask(pk2(os[i], os[i]), msk); a0 = mfma32(am, wf[2 * i], a0); a1 = mfma32(am, wf[2 * i + 1], a1); }
        float v0, v1;
        fold2<false>(a0, a1, nlo, v0, v1);
        stsc(rw, sd, asu(v0), asu(v1), tv);
        if (k == 0) STAMP(11);
      }
    } else if (fin) {
      int lw = L_OP + 32 * k + g4 * 8 + c8 + (nlo ? 0 : 256);
      asm volatile("" : "+v"(lw));
      v4u sv[4];
      const unsigned np3 = poll<4>(rw, WS_S + (32 * k + rs) * S_PITCH + fc * 128 + c8 * 16, 8 * S_PITCH, tag, sv); if (k == 0) RAW(31, np3); (void)np3;
      const float s = red4((blo(sv[0].x) + blo(sv[1].x)) + (blo(sv[2].x) + blo(sv[3].x)), (bhi(sv[0].x) + bhi(sv[1].x)) + (bhi(sv[2].x) + bhi(sv[3].x)),
                           (blo(sv[0].z) + blo(sv[1].z)) + (blo(sv[2].z) + blo(sv[3].z)), (bhi(sv[0].z) + bhi(sv[1].z)) + (bhi(sv[2].z) + bhi(sv[3].z)));
      L[lw] = asu(s);
      if (k == 0) STAMP(20); if (k == 1) STAMP(21); if (k == 2) STAMP(22); if (k == 3) STAMP(23);
      BARRIER();
    }
    STAMP_FLUSH();
  }
}

extern "C" void kernel_launch(void* const* d_in, const int* in_sizes, int n_in, void* d_out, int out_size, void* d_ws, size_t ws_size,
                              hipStream_t stream) {
  fused_decode<<<NWG, NTHR, 0, stream>>>(d_in[0], d_in[1], d_in[2], d_in[3], d_in[4], d_in[6], d_in[7], d_in[8], d_out, (char*)d_ws + WSOFF);
}
```

```cpp
#include <hip/hip_runtime.h>
#include <stdint.h>

#define GAS __attribute__((address_space(1)))
#define LAS __attribute__((address_space(3)))
typedef unsigned v4u __attribute__((ext_vector_type(4)));
typedef unsigned v2u __attribute__((ext_vector_type(2)));
typedef float f32x4 __attribute__((ext_vector_type(4)));
typedef __bf16 bf16x8 __attribute__((ext_vector_type(8)));
typedef __bf16 bf16x2 __attribute__((ext_vector_type(2)));
typedef short s16x4 __attribute__((ext_vector_type(4)));
typedef unsigned long long v2q __attribute__((ext_vector_type(2)));
typedef unsigned long long v4q __attribute__((ext_vector_type(4)));

constexpr int DM = 448, SK = 65536, WIN = 4096;
constexpr int NWG = 256, NTHR = 320;
constexpr int WINL = 6;
constexpr int VB1 = 10, VB2 = 12, VB3 = 14;
constexpr int WOWIN = 7;
constexpr int CODE_BYTES = 10496;
constexpr int WS_Q = 4096;
constexpr int WS_GU = WS_Q + 8 * 8192;
constexpr int WS_ML = WS_GU + 28 * 2048;
constexpr int O_PITCH = 2176;
constexpr int WS_O = WS_ML + 8192;
constexpr int S_PITCH = 1792;
constexpr int WS_S = WS_O + 256 * O_PITCH;
constexpr int F_PITCH = 3584;
constexpr int WS_F = WS_S + 128 * S_PITCH;
constexpr int WS_END = WS_F + 56 * F_PITCH;
constexpr int OOB = 1 << 30;
constexpr int L_R1 = 0;
constexpr int L_Q = 256;
constexpr int L_P = 512;
constexpr int L_W = 800;
constexpr int L_WM = 832;
constexpr int L_WL = 840;
constexpr int L_FLAG = 928;
constexpr int L_Z = 944;
constexpr int L_OP = 960;
constexpr int L_TOTAL = 1536;
constexpr unsigned SPIN_CAP = 1u << 18;

__device__ __forceinline__ float asf(unsigned u) { return __builtin_bit_cast(float, u); }
__device__ __forceinline__ unsigned asu(float f) { return __builtin_bit_cast(unsigned, f); }
__device__ __forceinline__ float blo(unsigned u) { return asf(u << 16); }
__device__ __forceinline__ float bhi(unsigned u) { return asf(u & 0xffff0000u); }
__device__ __forceinline__ float rbf(float f) { return (float)(__bf16)f; }
__device__ __forceinline__ unsigned pk2(float lo, float hi) { bf16x2 t = {(__bf16)lo, (__bf16)hi}; return __builtin_bit_cast(unsigned, t); }
__device__ __forceinline__ f32x4 mfma32(v4u a, v4u b, f32x4 c) {
  return __builtin_amdgcn_mfma_f32_16x16x32_bf16(__builtin_bit_cast(bf16x8, a), __builtin_bit_cast(bf16x8, b), c, 0, 0, 0);
}
__device__ __forceinline__ f32x4 mfma16(v2u a, v2u b, f32x4 c) {
  return __builtin_amdgcn_mfma_f32_16x16x16bf16_1k(__builtin_bit_cast(s16x4, a), __builtin_bit_cast(s16x4, b), c, 0, 0, 0);
}
template <int CTRL> __device__ __forceinline__ float dpp(float x) {
  return asf((unsigned)__builtin_amdgcn_mov_dpp((int)asu(x), CTRL, 0xf, 0xf, true));
}
constexpr int XOR1 = 0xB1, XOR2 = 0x4E, XOR7 = 0x141, XOR8 = 0x128;
__device__ __forceinline__ float sw16(float a, float b) { auto s = __builtin_amdgcn_permlane16_swap(asu(a), asu(b), false, false); return asf(s[0]) + asf(s[1]); }
__device__ __forceinline__ float sw32(float a, float b) { auto s = __builtin_amdgcn_permlane32_swap(asu(a), asu(b), false, false); return asf(s[0]) + asf(s[1]); }
__device__ __forceinline__ float x16_max(float x) { auto s = __builtin_amdgcn_permlane16_swap(asu(x), asu(x), false, false); return fmaxf(asf(s[0]), asf(s[1])); }
__device__ __forceinline__ float x32_max(float x) { auto s = __builtin_amdgcn_permlane32_swap(asu(x), asu(x), false, false); return fmaxf(asf(s[0]), asf(s[1])); }
__device__ __forceinline__ float row_sum(float x) { x += dpp<XOR1>(x); x += dpp<XOR2>(x); x += dpp<XOR7>(x); x += dpp<XOR8>(x); return x; }
__device__ __forceinline__ float row_max(float x) { x = fmaxf(x, dpp<XOR1>(x)); x = fmaxf(x, dpp<XOR2>(x)); x = fmaxf(x, dpp<XOR7>(x)); return fmaxf(x, dpp<XOR8>(x)); }
__device__ __forceinline__ float wave_sum(float x) { x = row_sum(x); x = sw16(x, x); return sw32(x, x); }
__device__ __forceinline__ float red4(float v0, float v1, float v2, float v3) { const float t = sw16(sw32(v0, v2), sw32(v1, v3)); return t + dpp<XOR8>(t); }
__device__ __forceinline__ __amdgpu_buffer_rsrc_t mkrs(const GAS char* p, int bytes) { return __builtin_amdgcn_make_buffer_rsrc((void*)p, 0, bytes, 0x00020000); }
__device__ __forceinline__ v4u ldnt(__amdgpu_buffer_rsrc_t rs, int voff, int soff) {
  const v4u r = __builtin_amdgcn_raw_buffer_load_b128(rs, voff, soff, 2);
  __builtin_amdgcn_sched_barrier(0);
  return r;
}
__device__ __forceinline__ v4u ldsc(__amdgpu_buffer_rsrc_t rs, int voff, int soff) { return __builtin_amdgcn_raw_buffer_load_b128(rs, voff, soff, 16); }
__device__ __forceinline__ void stsc(__amdgpu_buffer_rsrc_t rs, int off, unsigned d0, unsigned d1, unsigned tag) {
  v4u g = {d0, tag, d1, tag};
  __builtin_amdgcn_raw_buffer_store_b128(g, rs, off, 0, 16);
}
#define BARRIER() asm volatile("s_waitcnt lgkmcnt(0)\n\ts_barrier" ::: "memory")
#define CFENCE() asm volatile("" ::: "memory")
#define VMW(n) asm volatile("s_waitcnt vmcnt(%0)" ::"i"(n) : "memory")
__device__ __forceinline__ v4u amask(unsigned x, const v4u& m) { return v4u{x & m.x, x & m.y, x & m.z, x & m.w}; }
template <bool R16> __device__ __forceinline__ f32x4 fold4(const f32x4& A, const f32x4& B) {
  f32x4 s;
#pragma unroll
  for (int r = 0; r < 4; ++r) {
    auto t = R16 ? __builtin_amdgcn_permlane16_swap(asu(A[r]), asu(B[r]), false, false) : __builtin_amdgcn_permlane32_swap(asu(A[r]), asu(B[r]), false, false);
    float z = asf(t[0]) + dpp<XOR8>(asf(t[1]));
    asm volatile("" : "+v"(z));
    s[r] = z;
  }
  return s;
}
template <bool R16> __device__ __forceinline__ void fold2(const f32x4& A, const f32x4& B, bool nlo, float& v0, float& v1) {
  float s[4];
#pragma unroll
  for (int r = 0; r < 4; ++r) {
    auto t = R16 ? __builtin_amdgcn_permlane16_swap(asu(A[r]), asu(B[r]), false, false) : __builtin_amdgcn_permlane32_swap(asu(A[r]), asu(B[r]), false, false);
    const float a = asf(t[0]), b = asf(t[1]);
    s[r] = r < 2 ? a + dpp<XOR8>(b) : dpp<XOR8>(a) + b;
  }
  v0 = nlo ? s[0] : s[2];
  v1 = nlo ? s[1] : s[3];
}
template <int N> __device__ __forceinline__ unsigned long long tags_bad(const v4u (&v)[N], unsigned tag) {
  unsigned long long bad = 0ull;
#pragma unroll
  for (int k = 0; k < N; ++k) bad |= __builtin_amdgcn_ballot_w64(v[k].y != tag) | __builtin_amdgcn_ballot_w64(v[k].w != tag);
  return bad;
}
template <int N> __device__ __forceinline__ bool tags_ok(const v4u (&v)[N], unsigned tag) { return tags_bad<N>(v, tag) == 0ull; }
template <int N> __device__ __forceinline__ void poll(__amdgpu_buffer_rsrc_t rs, int off, int stride, unsigned tag, v4u (&v)[N]) {
  unsigned spins = 0;
  unsigned long long again;
#pragma unroll 1
  do {
    CFENCE();
#pragma unroll
    for (int k = 0; k < N; ++k) v[k] = ldsc(rs, off, k * stride);
    unsigned long long live;
    asm volatile("s_add_u32 %1, %1, 1\n\ts_cmp_lt_u32 %1, %2\n\ts_cselect_b64 %0, -1, 0" : "=s"(live), "+s"(spins) : "i"(SPIN_CAP) : "scc");
    again = tags_bad<N>(v, tag) & live;
  } while (again != 0ull);
}
template <bool SLEEP> __device__ __forceinline__ void lds_wait(volatile LAS unsigned* w) {
  unsigned spins = 0;
#pragma unroll 1
  for (;;) {
    if (__builtin_amdgcn_readfirstlane(*w) != 0u) break;
    asm volatile("" : "+s"(spins));
    if (++spins > (SPIN_CAP << 5)) break;
    if (SLEEP) __builtin_amdgcn_s_sleep(1);
  }
  CFENCE();
}
__device__ __forceinline__ float sum4(v4u v) { return (asf(v.x) + asf(v.z)) + (asf(v.y) + asf(v.w)); }
__device__ __forceinline__ float silu(float x) { return x * __builtin_amdgcn_rcpf(1.f + __expf(-x)); }

__global__ void __launch_bounds__(NTHR, 2) fused_decode(const void* x_, const void* kc_, const void* vc_, const void* wq_, const void* wo_, const void* wg_,
                                                        const void* wu_, const void* wd_, void* out_, void* ws_) {
  v4q ka, kb; v2q kd; unsigned long long pc;
  int b = blockIdx.x, tid = threadIdx.x;
  asm volatile("s_load_dwordx8 %0, %5, 0x0\n\ts_load_dwordx8 %1, %5, 0x20\n\ts_load_dwordx4 %2, %5, 0x40\n\ts_getpc_b64 %3"
               : "=&s"(ka), "=&s"(kb), "=&s"(kd), "=&s"(pc), "+s"(b) : "s"(__builtin_amdgcn_kernarg_segment_ptr()));
  asm volatile("" : "+v"(tid));
  __builtin_amdgcn_sched_barrier(0);
  __shared__ __attribute__((aligned(16))) unsigned lds_[L_TOTAL];
  LAS unsigned* L = (LAS unsigned*)lds_;
  volatile LAS unsigned* FL = (volatile LAS unsigned*)(L + L_FLAG);
  const int w = __builtin_amdgcn_readfirstlane(tid >> 6);
  unsigned cpf = 0, cpf2 = 0;
  if (w == 4 && (b & 0xb8) == 0xb8) {
    const __amdgpu_buffer_rsrc_t rc = mkrs((const GAS char*)(pc & ~255ull), CODE_BYTES);
    cpf = __builtin_amdgcn_raw_buffer_load_b32(rc, (tid & 63) * 128, 256, 0);
    cpf2 = __builtin_amdgcn_raw_buffer_load_b32(rc, (tid & 63) * 128, 256 + 8192, 0);
  }
  const bool e0 = (b & 64) == 0, q0 = b < 128;
  const int t0 = b & 63;
  const int NI = e0 ? 6 : 8;
  const int r0 = (e0 ? 256 : 0) + 8 * NI * w;
  const int pitch = q0 ? 8192 : 3584;
  const int sb = r0 * pitch + (q0 ? t0 : t0 < 28 ? t0 : t0 - 28) * 128;
  const int nb1 = (q0 || t0 < 56) ? DM * pitch : 0;
  const int sa = 2 * ((tid >> 4) & 3) + ((tid >> 2) & 1);
  const int fo = ((tid >> 3) & 7) * (NI * pitch) + (tid & 7) * 16, xo = (tid & 63) * 16, xso = sa * (NI * 2);
  asm volatile("" ::"s"(r0 * 2), "s"(pitch), "s"(sb), "s"(nb1), "v"(fo), "v"(xo), "v"(xso));
  __builtin_amdgcn_sched_barrier(0);
  asm volatile("s_waitcnt lgkmcnt(0)" : "+s"(ka), "+s"(kb), "+s"(kd));
  const GAS char *x = (const GAS char*)ka.x, *kc = (const GAS char*)ka.y, *vc = (const GAS char*)ka.z, *wq = (const GAS char*)ka.w;
  const GAS char *wo = (const GAS char*)kb.x, *wg = (const GAS char*)kb.y, *wu = (const GAS char*)kb.z, *wd = (const GAS char*)kb.w;
  GAS char *out = (GAS char*)kd.x, *ws = (GAS char*)kd.y;
  const __amdgpu_buffer_rsrc_t rx = mkrs(x, 896);
  unsigned ep;
  asm volatile("s_cmp_lg_u32 %1, 4\n\ts_cbranch_scc1 1f\n\ts_load_dword %0, %2, 0x0\n1:" : "=&s"(ep) : "s"(w), "s"(ws) : "scc");
  constexpr float C2 = 0.0625f * 1.4426950408889634f;
#define IDS() \
  asm volatile("" : "+s"(b)); \
  asm volatile("" : "+v"(tid)); \
  __builtin_amdgcn_sched_barrier(0); \
  const int h = (b >> 3) & 7, j = (b & 7) * 4 + (b >> 6); \
  const int lane = tid & 63; \
  const int u = (b & 63) + 64 * (b >> 7); \
  const bool isE = (j & 1) == 0; \
  const int ei = j >> 1; \
  const bool isD = !isE && u < 56; \
  const bool fin = !isE && u >= 114; \
  const int fc = u - 114; \
  const int c8 = lane & 7, g4 = lane >> 4, n16 = lane & 15; \
  const bool nlo = n16 < 8; \
  const __amdgpu_buffer_rsrc_t rw = mkrs(ws, WS_END)

  if (w == 4) {
    IDS();
    __builtin_amdgcn_s_setprio(3);
    asm volatile("s_barrier\n\ts_waitcnt lgkmcnt(0)" : "+s"(ep)::"memory");
    const unsigned tag = ep + 1u;
    if (lane == 0) *(volatile LAS v2u*)(L + L_FLAG + 2) = v2u{tag, 1u};
    if (u < 120) {
      const v2u p0 = *(const LAS v2u*)(L + L_R1 + 2 * (lane & 31)), p1 = *(const LAS v2u*)(L + L_R1 + 64 + 2 * (lane & 31)),
                p2 = *(const LAS v2u*)(L + L_R1 + 128 + 2 * (lane & 31)), p3 = *(const LAS v2u*)(L + L_R1 + 192 + 2 * (lane & 31));
      const float v0 = (asf(p0.x) + asf(p1.x)) + (asf(p2.x) + asf(p3.x)), v1 = (asf(p0.y) + asf(p1.y)) + (asf(p2.y) + asf(p3.y));
      const int hf = isE ? 1 : 0;
      const int dst = u < 64 ? WS_Q + (u >> 3) * 8192 + hf * 4096 + (u & 7) * 512 : u < 92 ? WS_GU + (u - 64) * 2048 + hf * 512 : WS_GU + (u - 92) * 2048 + 1024 + hf * 512;
      if (lane < 32) stsc(rw, dst + lane * 16, asu(v0), asu(v1), tag);
    }
    const unsigned xw = __builtin_amdgcn_raw_buffer_load_b16(rx, (4 * c8 + g4) * 2, fin ? fc * 64 : 0, 0);
    const int go = WS_GU + (u >> 1) * 2048 + g4 * 512 + (u & 1) * 256 + n16 * 16;
    v4u gu[1];
    lds_wait<true>(FL + 5);
    gu[0] = ldsc(rw, go, isD ? 0 : OOB);
    asm volatile("" ::"v"(cpf), "v"(cpf2));
    BARRIER();
    BARRIER();
    BARRIER();
    if (lane < 2) {
      const float M = fmaxf(fmaxf(asf(L[L_WM + lane]), asf(L[L_WM + 2 + lane])), fmaxf(asf(L[L_WM + 4 + lane]), asf(L[L_WM + 6 + lane])));
      const float Ls = (asf(L[L_WL + lane]) + asf(L[L_WL + 2 + lane])) + (asf(L[L_WL + 4 + lane]) + asf(L[L_WL + 6 + lane]));
      stsc(rw, WS_ML + (h * 2 + lane) * 512 + j * 16, asu(M), asu(Ls), tag);
    }
    if (isE) {
      lds_wait<true>(FL + 8);
      v4u ml[1];
      poll<1>(rw, WS_ML + (h * 2 + (ei >> 3)) * 512 + (lane & 31) * 16, 0, tag, ml);
      const float m = asf(ml[0].x);
      const float M = x16_max(row_max(m));
      const float e = __builtin_amdgcn_exp2f((m - M) * C2);
      float Ls = row_sum(e * asf(ml[0].z)); Ls = sw16(Ls, Ls);
      if (lane < 32) L[L_W + lane] = asu(e * __builtin_amdgcn_rcpf(Ls));
      CFENCE();
      if (lane == 0) FL[7] = 1u;
    } else if (isD) {
      if (!tags_ok<1>(gu, tag)) poll<1>(rw, go, 0, tag, gu);
      const float y = rbf(sw16(asf(gu[0].x), asf(gu[0].z)));
      auto t = __builtin_amdgcn_permlane32_swap(asu(y), asu(y), false, false);
      const float hv = rbf(rbf(silu(asf(t[0]))) * asf(t[1]));
      const int r = 2 * n16 + (g4 & 1);
      if (lane < 32) L[L_OP + (r & 7) * 4 + (r >> 3)] = asu(hv);
      BARRIER();
    } else if (fin) {
      lds_wait<true>(FL + 8);
      v4u fv[14];
      poll<14>(rw, WS_F + g4 * F_PITCH + fc * 256 + n16 * 16, 4 * F_PITCH, tag, fv);
      float f0 = 0.f, f1 = 0.f;
#pragma unroll
      for (int i = 0; i < 14; ++i) { f0 += asf(fv[i].x); f1 += asf(fv[i].z); }
      float f = sw16(f0, f1); f = sw32(f, f);
      const float g = rbf(silu(rbf(f)));
      if (lane < 32) L[L_W + 2 * n16 + (g4 & 1)] = asu(g);
      asm volatile("s_waitcnt lgkmcnt(0)" ::: "memory");
      const float gc = asf(L[L_W + 4 * c8 + g4]);
      const LAS unsigned* so = L + L_OP + (4 * c8 + g4) * 4;
      int oa = (4 * c8 + g4) * 2 + (nlo ? 0 : OOB);
      float xf = blo(xw);
      asm volatile("" : "+v"(so), "+v"(oa), "+v"(xf));
      BARRIER();
      const float s = rbf(sum4(*(const LAS v4u*)so));
      const float res = xf + rbf(s * gc);
      __builtin_amdgcn_raw_buffer_store_b16((unsigned short)pk2(res, res), mkrs(out, 896), oa, fc * 64, 16);
      if (fc == 0 && lane == 0) __hip_atomic_store((GAS unsigned*)ws, tag, __ATOMIC_RELAXED, __HIP_MEMORY_SCOPE_AGENT);
    }
  } else {
    const v4u xr = __builtin_amdgcn_raw_buffer_load_b128(rx, xo, 0, 0);
    const v4u xs = __builtin_amdgcn_raw_buffer_load_b128(rx, xso, r0 * 2, 0);
    __builtin_amdgcn_sched_barrier(0);
    v4u fr[8];
    const GAS char* w1 = q0 ? wq : t0 < 28 ? wg : wu;
    {
      const __amdgpu_buffer_rsrc_t ra = mkrs(w1, nb1);
#pragma unroll
      for (int i = 0; i < 6; ++i) fr[i] = ldnt(ra, fo, sb + i * pitch);
    }
    IDS();
    const int k = w, rs = lane >> 3;
    const int gidx = 4 * c8 + 2 * (g4 & 1) + (n16 >> 3);
    const unsigned hm = (lane & 1) ? 0xffff0000u : 0x0000ffffu;
    v4u kf[16], vf[16], wf[8];
    if (tid < 20) FL[tid] = 0u;
    const int wre = 512 * h + 32 * ei, wrd = u < 56 ? 32 * u : 0;
    const int wrow = (wrd + ((wre - wrd) & ((j & 1) - 1))) * 896;
    const int oo = rs * 896 + c8 * 16;
    const v2u msk2 = {(lane & 2) ? 0u : hm, (lane & 2) ? hm : 0u};
    const int cA = 2 * k, sA = cA * 128, sB = k < 3 ? sA + 128 : OOB;
    const __amdgpu_buffer_rsrc_t ro = mkrs((isE ? wo : wd) + wrow, (isE || isD) ? 32 * 896 : 0);
#define LDW(i) wf[i] = ldnt(ro, oo, ((i) >> 1) * 8 * 896 + (((i) & 1) ? sB : sA))
    {
      const __amdgpu_buffer_rsrc_t r67 = mkrs(isE ? wo + wrow : w1, isE ? 32 * 896 : nb1);
      const int o67 = isE ? oo : fo;
      fr[6] = ldnt(r67, o67, isE ? sA : sb + 6 * pitch);
      fr[7] = ldnt(r67, o67, isE ? sB : sb + 7 * pitch);
      wf[0] = fr[6]; wf[1] = fr[7];
    }
    const int kvo = (h * SK + (SK - WIN) + j * 128) * 512;
    const int rot = ((b + (b >> 3) + (b >> 6)) & 7) * 8192;
    const int ko = k * 2048 + (n16 >> 2) * 512 + g4 * 64 + (lane & 3) * 16;
    const int vo = rs * 512 + c8 * 16 + k * 128;
    const __amdgpu_buffer_rsrc_t rk = mkrs(kc, 8 * SK * 512), rv = mkrs(vc, 8 * SK * 512);
#define LDK(i) kf[i] = ldnt(rk, ko + ((i) & 1) * 256, kvo + ((((i) >> 1) * 8192 + rot) & 0xffff))
#define LDV(i) vf[i] = ldnt(rv, vo, kvo + (((i) * 4096 + rot) & 0xffff))
    {
      float xv[8];
#pragma unroll
      for (int e = 0; e < 4; ++e) { xv[2 * e] = blo(xr[e]); xv[2 * e + 1] = bhi(xr[e]); }
      const float s1 = ((xv[0] + xv[1]) + (xv[2] + xv[3])) + ((xv[4] + xv[5]) + (xv[6] + xv[7]));
      const float mean = wave_sum(s1) * (1.f / DM);
      float s2 = 0.f;
#pragma unroll
      for (int e = 0; e < 8; ++e) { const float d = xv[e] - mean; s2 += d * d; }
      s2 = lane < 56 ? s2 : 0.f;
      const float var = wave_sum(s2) * (1.f / DM);
      const float mb = rbf(mean), rden = __builtin_amdgcn_rcpf(rbf(__builtin_amdgcn_sqrtf(rbf(rbf(var) + 1e-5f))));
      unsigned xc[8];
#pragma unroll
      for (int i = 0; i < 8; ++i) {
        const float xn = rbf(((i & 1) ? bhi(xs[i >> 1]) : blo(xs[i >> 1])) - mb) * rden;
        xc[i] = (i >= 6 && isE) ? 0u : pk2(xn, xn);
      }
      __builtin_amdgcn_sched_barrier(0);
      f32x4 a0 = {0.f, 0.f, 0.f, 0.f}, a1 = {0.f, 0.f, 0.f, 0.f};
#pragma unroll
      for (int i = 0; i < 8; ++i) {
        const v2u am = {xc[i] & msk2.x, xc[i] & msk2.y};
        a0 = mfma16(am, v2u{fr[i].x, fr[i].y}, a0);
        a1 = mfma16(am, v2u{fr[i].z, fr[i].w}, a1);
        __builtin_amdgcn_sched_barrier(0);
        if (i >= 8 - WINL) LDK(i - (8 - WINL));
      }
      float v0, v1;
      fold2<true>(a0, a1, nlo, v0, v1);
      if (lane < 32) *(LAS v2u*)(L + L_R1 + 64 * k + 2 * gidx) = v2u{asu(v0), asu(v1)};
      BARRIER();
    }
#pragma unroll
    for (int i = WINL; i < 16; ++i) { VMW(WINL - 1); LDK(i); }
    unsigned tag;
    v4u q2[2];
    {
      const int qo = WS_Q + h * 8192 + k * 1024 + lane * 16;
      v4u qv[2];
      qv[0] = ldsc(rw, qo, 0);
      qv[1] = ldsc(rw, qo, 4096);
      __builtin_amdgcn_sched_barrier(0);
      if (lane == 0) FL[5] = 1u;
#pragma unroll
      for (int i = 0; i < WINL; ++i) { VMW(WINL + 1); LDV(i); }
      lds_wait<false>(FL + 3);
      tag = __builtin_amdgcn_readfirstlane(FL[2]);
      VMW(WINL);
      const bool qok = tags_ok<2>(qv, tag);
      q2[0] = ldsc(rw, qo, qok ? OOB : 0);
      q2[1] = ldsc(rw, qo, qok ? OOB : 4096);
      __builtin_amdgcn_sched_barrier(0);
#pragma unroll
      for (int i = WINL; i < VB1; ++i) LDV(i);
      if (!qok) {
        qv[0] = q2[0]; qv[1] = q2[1];
        if (!tags_ok<2>(qv, tag)) poll<2>(rw, qo, 4096, tag, qv);
      }
      L[L_Q + 64 * k + lane] = pk2(asf(qv[0].x) + asf(qv[1].x), asf(qv[0].z) + asf(qv[1].z));
    }
    BARRIER();
#pragma unroll
    for (int i = VB1; i < VB2; ++i) LDV(i);
    v4u q[4];
#pragma unroll
    for (int t = 0; t < 4; ++t) {
      const int qa = L_Q + ((lane >> 2) & 1) * 128 + (t & 1) * 64 + g4 * 16 + (lane & 3) * 4;
      q[t] = *(const LAS v4u*)(L + ((n16 >> 3) == (t >> 1) ? qa : L_Z));
    }
    float y[4];
#pragma unroll
    for (int t = 0; t < 4; ++t) {
      f32x4 d = {0.f, 0.f, 0.f, 0.f};
      d = mfma32(q[0], kf[2 * t], d); d = mfma32(q[1], kf[2 * t + 1], d);
      d = mfma32(q[2], kf[2 * t + 8], d); d = mfma32(q[3], kf[2 * t + 9], d);
      const float lo = (lane & 1) ? d[1] : d[0], hi = (lane & 1) ? d[3] : d[2];
      y[t] = (lane & 2) ? hi : lo;
    }
    {
      const float k0 = (lane & 2) ? y[2] : y[0], s0 = (lane & 2) ? y[0] : y[2], k1 = (lane & 2) ? y[3] : y[1], s1 = (lane & 2) ? y[1] : y[3];
      y[0] = k0 + dpp<XOR2>(s0); y[1] = k1 + dpp<XOR2>(s1);
    }
    const float sc = rbf(((lane & 1) ? y[1] : y[0]) + dpp<XOR1>((lane & 1) ? y[0] : y[1]));
    const float m = x32_max(row_max(sc));
    const int a = g4 & 1;
    if ((lane & 0x2f) == 0) L[L_WM + 2 * k + a] = asu(m);
    BARRIER();
#pragma unroll
    for (int i = VB2; i < VB3; ++i) LDV(i);
    const float M = fmaxf(fmaxf(asf(L[L_WM + a]), asf(L[L_WM + 2 + a])), fmaxf(asf(L[L_WM + 4 + a]), asf(L[L_WM + 6 + a])));
    const float p = __builtin_amdgcn_exp2f((sc - M) * C2);
    L[L_P + a * 128 + (4 * (k & 1) + (n16 >> 2)) * 16 + 2 * ((lane & 3) + 4 * (g4 >> 1)) + (k >> 1)] = pk2(p, p);
    float l = row_sum(p); l = sw32(l, l);
    if ((lane & 0x2f) == 0) L[L_WL + 2 * k + a] = asu(l);
    BARRIER();
    {
      v4u pa[4];
#pragma unroll
      for (int t = 0; t < 4; ++t) pa[t] = *(const LAS v4u*)(L + L_P + (n16 >> 3) * 128 + sa * 16 + 4 * t);
      int od = WS_O + (h + 8 * j) * O_PITCH + (lane >> 5) * 1024 + k * 256 + (2 * c8 + (g4 & 1)) * 16 + (nlo ? 0 : OOB);
      asm volatile("" : "+v"(od));
      f32x4 a0 = {0.f, 0.f, 0.f, 0.f}, a1 = {0.f, 0.f, 0.f, 0.f};
#pragma unroll
      for (int i = 0; i < 16; ++i) {
        const unsigned c = pa[i >> 2][i & 3];
        const v2u am = {c & msk2.x, c & msk2.y};
        a0 = mfma16(am, v2u{vf[i].x, vf[i].y}, a0);
        a1 = mfma16(am, v2u{vf[i].z, vf[i].w}, a1);
        __builtin_amdgcn_sched_barrier(0);
        if (i == WINL) asm volatile("" ::"v"(q2[0]), "v"(q2[1]));
        const int nx = i + WOWIN;
        if (nx >= VB3 && nx < 16) LDV(nx < 16 ? nx : 0);
        if (nx >= 16 && nx < 22) LDW(nx >= 16 && nx < 22 ? nx - 14 : 2);
      }
      const f32x4 o = fold4<true>(a0, a1);
      stsc(rw, od, pk2(o[0], o[1]), pk2(o[2], o[3]), tag);
      __builtin_amdgcn_sched_barrier(0);
    }
    if (lane == 0) FL[8] = 1u;
    if (isE || isD) {
      v4u msk = {(c8 >> 1) == 0 ? hm : 0u, (c8 >> 1) == 1 ? hm : 0u, (c8 >> 1) == 2 ? hm : 0u, (c8 >> 1) == 3 ? hm : 0u};
      const int dead = (k < 3 || lane < 32) ? 0 : OOB;
      int sd = isE ? WS_S + (h * 16 + ei) * S_PITCH + (cA + (lane >> 5)) * 256 + (2 * c8 + (g4 & 1)) * 16 + (nlo ? dead : OOB)
                   : WS_F + u * F_PITCH + (cA + (lane >> 5)) * 512 + gidx * 16 + dead;
      LAS unsigned* lw = L + L_OP + ((4 * (c8 & 1) + g4) * 4 + (c8 >> 1)) * 4 + k + (nlo ? 0 : 256);
      const LAS unsigned* lr = L + L_OP + rs * (isE ? 16 : 4);
      unsigned tv = tag;
      asm volatile("" : "+v"(msk), "+v"(sd), "+v"(lw), "+v"(lr), "+v"(tv));
      if (isE) {
        __builtin_amdgcn_s_sleep(12);
        v4u ov[1];
        const int po = WS_O + (h + 8 * (8 * k + rs)) * O_PITCH + ei * 128 + c8 * 16;
        ov[0] = ldsc(rw, po, 0);
        __builtin_amdgcn_sched_barrier(0);
        lds_wait<false>(FL + 7);
        const float wt = asf(L[L_W + 8 * k + rs]);
        asm volatile("" ::"v"(wt));
        if (!tags_ok<1>(ov, tag)) poll<1>(rw, po, 0, tag, ov);
        *lw = asu(red4(wt * blo(ov[0].x), wt * bhi(ov[0].x), wt * blo(ov[0].z), wt * bhi(ov[0].z)));
        BARRIER();
        v4u op[4];
#pragma unroll
        for (int i = 0; i < 4; ++i) op[i] = *(const LAS v4u*)(lr + 4 * i);
        __builtin_amdgcn_sched_barrier(0);
        f32x4 a0 = {0.f, 0.f, 0.f, 0.f}, a1 = {0.f, 0.f, 0.f, 0.f};
#pragma unroll
        for (int i = 0; i < 4; ++i) { const float o = sum4(op[i]); const v4u am = amask(pk2(o, o), msk); a0 = mfma32(am, wf[2 * i], a0); a1 = mfma32(am, wf[2 * i + 1], a1); }
        const f32x4 s = fold4<false>(a0, a1);
        stsc(rw, sd, pk2(s[0], s[1]), pk2(s[2], s[3]), tv);
      }
      CFENCE();
      if (isD) {
        LDW(0); LDW(1);
        BARRIER();
        const f32x4 os = __builtin_bit_cast(f32x4, *(const LAS v4u*)lr);
        f32x4 a0 = {0.f, 0.f, 0.f, 0.f}, a1 = {0.f, 0.f, 0.f, 0.f};
#pragma unroll
        for (int i = 3; i >= 0; --i) { const v4u am = amask(pk2(os[i], os[i]), msk); a0 = mfma32(am, wf[2 * i], a0); a1 = mfma32(am, wf[2 * i + 1], a1); }
        float v0, v1;
        fold2<false>(a0, a1, nlo, v0, v1);
        stsc(rw, sd, asu(v0), asu(v1), tv);
      }
    } else if (fin) {
      LAS unsigned* lw = L + L_OP + (4 * c8 + g4) * 4 + k + (nlo ? 0 : 256);
      v2u ones = {0x3f803f80u & msk2.x, 0x3f803f80u & msk2.y};
      asm volatile("" : "+v"(lw), "+v"(ones));
      v4u sv[4];
      poll<4>(rw, WS_S + (32 * k + rs) * S_PITCH + fc * 128 + c8 * 16, 8 * S_PITCH, tag, sv);
      f32x4 d = {0.f, 0.f, 0.f, 0.f};
#pragma unroll
      for (int i = 0; i < 4; ++i) d = mfma16(ones, v2u{sv[i].x, sv[i].z}, d);
      const float lo = (g4 & 1) ? d[1] : d[0], hi = (g4 & 1) ? d[3] : d[2], t = (g4 & 2) ? hi : lo;
      *lw = asu(t + dpp<XOR8>(t));
      BARRIER();
    }
  }
}

extern "C" void kernel_launch(void* const* d_in, const int* in_sizes, int n_in, void* d_out, int out_size, void* d_ws, size_t ws_size,
                              hipStream_t stream) {
  fused_decode<<<NWG, NTHR, 0, stream>>>(d_in[0], d_in[1], d_in[2], d_in[3], d_in[4], d_in[6], d_in[7], d_in[8], d_out, d_ws);
}
```

```cpp
#include <hip/hip_runtime.h>
#include <stdint.h>

#define GAS __attribute__((address_space(1)))
#define LAS __attribute__((address_space(3)))
typedef unsigned v4u __attribute__((ext_vector_type(4)));
typedef unsigned v2u __attribute__((ext_vector_type(2)));
typedef float f32x4 __attribute__((ext_vector_type(4)));
typedef __bf16 bf16x8 __attribute__((ext_vector_type(8)));
typedef __bf16 bf16x2 __attribute__((ext_vector_type(2)));
typedef short s16x4 __attribute__((ext_vector_type(4)));
typedef unsigned long long v2q __attribute__((ext_vector_type(2)));
typedef unsigned long long v4q __attribute__((ext_vector_type(4)));

constexpr int DM = 448, SK = 65536, WIN = 4096;
constexpr int NWG = 256, NTHR = 320;
constexpr int WINL = 6;
constexpr int VB1 = 10, VB2 = 12, VB3 = 14;
constexpr int WOWIN = 7;
constexpr int CODE_BYTES = 10624;
constexpr int WS_Q = 4096;
constexpr int WS_GU = WS_Q + 8 * 8192;
constexpr int WS_ML = WS_GU + 28 * 2048;
constexpr int O_PITCH = 2176;
constexpr int WS_O = WS_ML + 8192;
constexpr int S_PITCH = 1792;
constexpr int WS_S = WS_O + 256 * O_PITCH;
constexpr int F_PITCH = 3584;
constexpr int WS_F = WS_S + 128 * S_PITCH;
constexpr int WS_END = WS_F + 56 * F_PITCH;
constexpr int OOB = 1 << 30;
constexpr int L_R1 = 0;
constexpr int L_Q = 256;
constexpr int L_P = 512;
constexpr int L_W = 800;
constexpr int L_WM = 832;
constexpr int L_WL = 840;
constexpr int L_FLAG = 928;
constexpr int L_Z = 944;
constexpr int L_OP = 960;
constexpr int L_TOTAL = 1536;
constexpr unsigned SPIN_CAP = 1u << 18;

__device__ __forceinline__ float asf(unsigned u) { return __builtin_bit_cast(float, u); }
__device__ __forceinline__ unsigned asu(float f) { return __builtin_bit_cast(unsigned, f); }
__device__ __forceinline__ float blo(unsigned u) { return asf(u << 16); }
__device__ __forceinline__ float bhi(unsigned u) { return asf(u & 0xffff0000u); }
__device__ __forceinline__ float rbf(float f) { return (float)(__bf16)f; }
__device__ __forceinline__ unsigned pk2(float lo, float hi) { bf16x2 t = {(__bf16)lo, (__bf16)hi}; return __builtin_bit_cast(unsigned, t); }
__device__ __forceinline__ f32x4 mfma32(v4u a, v4u b, f32x4 c) {
  return __builtin_amdgcn_mfma_f32_16x16x32_bf16(__builtin_bit_cast(bf16x8, a), __builtin_bit_cast(bf16x8, b), c, 0, 0, 0);
}
__device__ __forceinline__ f32x4 mfma16(v2u a, v2u b, f32x4 c) {
  return __builtin_amdgcn_mfma_f32_16x16x16bf16_1k(__builtin_bit_cast(s16x4, a), __builtin_bit_cast(s16x4, b), c, 0, 0, 0);
}
template <int CTRL> __device__ __forceinline__ float dpp(float x) {
  return asf((unsigned)__builtin_amdgcn_mov_dpp((int)asu(x), CTRL, 0xf, 0xf, true));
}
constexpr int XOR1 = 0xB1, XOR2 = 0x4E, XOR7 = 0x141, XOR8 = 0x128;
__device__ __forceinline__ float sw16(float a, float b) { auto s = __builtin_amdgcn_permlane16_swap(asu(a), asu(b), false, false); return asf(s[0]) + asf(s[1]); }
__device__ __forceinline__ float sw32(float a, float b) { auto s = __builtin_amdgcn_permlane32_swap(asu(a), asu(b), false, false); return asf(s[0]) + asf(s[1]); }
__device__ __forceinline__ float x16_max(float x) { auto s = __builtin_amdgcn_permlane16_swap(asu(x), asu(x), false, false); return fmaxf(asf(s[0]), asf(s[1])); }
__device__ __forceinline__ float x32_max(float x) { auto s = __builtin_amdgcn_permlane32_swap(asu(x), asu(x), false, false); return fmaxf(asf(s[0]), asf(s[1])); }
__device__ __forceinline__ float row_sum(float x) { x += dpp<XOR1>(x); x += dpp<XOR2>(x); x += dpp<XOR7>(x); x += dpp<XOR8>(x); return x; }
__device__ __forceinline__ float row_max(float x) { x = fmaxf(x, dpp<XOR1>(x)); x = fmaxf(x, dpp<XOR2>(x)); x = fmaxf(x, dpp<XOR7>(x)); return fmaxf(x, dpp<XOR8>(x)); }
__device__ __forceinline__ float wave_sum(float x) { x = row_sum(x); x = sw16(x, x); return sw32(x, x); }
__device__ __forceinline__ float red4(float v0, float v1, float v2, float v3) { const float t = sw16(sw32(v0, v2), sw32(v1, v3)); return t + dpp<XOR8>(t); }
__device__ __forceinline__ __amdgpu_buffer_rsrc_t mkrs(const GAS char* p, int bytes) { return __builtin_amdgcn_make_buffer_rsrc((void*)p, 0, bytes, 0x00020000); }
__device__ __forceinline__ v4u ldnt(__amdgpu_buffer_rsrc_t rs, int voff, int soff) {
  const v4u r = __builtin_amdgcn_raw_buffer_load_b128(rs, voff, soff, 2);
  __builtin_amdgcn_sched_barrier(0);
  return r;
}
__device__ __forceinline__ v4u ldsc(__amdgpu_buffer_rsrc_t rs, int voff, int soff) { return __builtin_amdgcn_raw_buffer_load_b128(rs, voff, soff, 16); }
__device__ __forceinline__ void stsc(__amdgpu_buffer_rsrc_t rs, int off, unsigned d0, unsigned d1, unsigned tag) {
  v4u g = {d0, tag, d1, tag};
  __builtin_amdgcn_raw_buffer_store_b128(g, rs, off, 0, 16);
}
#define BARRIER() asm volatile("s_waitcnt lgkmcnt(0)\n\ts_barrier" ::: "memory")
#define CFENCE() asm volatile("" ::: "memory")
#define VMW(n) asm volatile("s_waitcnt vmcnt(%0)" ::"i"(n) : "memory")
__device__ __forceinline__ v4u amask(unsigned x, const v4u& m) { return v4u{x & m.x, x & m.y, x & m.z, x & m.w}; }
template <bool R16> __device__ __forceinline__ f32x4 fold4(const f32x4& A, const f32x4& B) {
  f32x4 s;
#pragma unroll
  for (int r = 0; r < 4; ++r) {
    auto t = R16 ? __builtin_amdgcn_permlane16_swap(asu(A[r]), asu(B[r]), false, false) : __builtin_amdgcn_permlane32_swap(asu(A[r]), asu(B[r]), false, false);
    float z = asf(t[0]) + dpp<XOR8>(asf(t[1]));
    asm volatile("" : "+v"(z));
    s[r] = z;
  }
  return s;
}
template <bool R16> __device__ __forceinline__ void fold2(const f32x4& A, const f32x4& B, bool nlo, float& v0, float& v1) {
  float s[4];
#pragma unroll
  for (int r = 0; r < 4; ++r) {
    auto t = R16 ? __builtin_amdgcn_permlane16_swap(asu(A[r]), asu(B[r]), false, false) : __builtin_amdgcn_permlane32_swap(asu(A[r]), asu(B[r]), false, false);
    const float a = asf(t[0]), b = asf(t[1]);
    s[r] = r < 2 ? a + dpp<XOR8>(b) : dpp<XOR8>(a) + b;
  }
  v0 = nlo ? s[0] : s[2];
  v1 = nlo ? s[1] : s[3];
}
template <int N> __device__ __forceinline__ unsigned long long tags_bad(const v4u (&v)[N], unsigned tag) {
  unsigned long long bad = 0ull;
#pragma unroll
  for (int k = 0; k < N; ++k) bad |= __builtin_amdgcn_ballot_w64(v[k].y != tag) | __builtin_amdgcn_ballot_w64(v[k].w != tag);
  return bad;
}
template <int N> __device__ __forceinline__ bool tags_ok(const v4u (&v)[N], unsigned tag) { return tags_bad<N>(v, tag) == 0ull; }
template <int N> __device__ __forceinline__ void poll(__amdgpu_buffer_rsrc_t rs, int off, int stride, unsigned tag, v4u (&v)[N]) {
  unsigned spins = 0;
  unsigned long long again;
#pragma unroll 1
  do {
    CFENCE();
#pragma unroll
    for (int k = 0; k < N; ++k) v[k] = ldsc(rs, off, k * stride);
    unsigned long long live;
    asm volatile("s_add_u32 %1, %1, 1\n\ts_cmp_lt_u32 %1, %2\n\ts_cselect_b64 %0, -1, 0" : "=s"(live), "+s"(spins) : "i"(SPIN_CAP) : "scc");
    again = tags_bad<N>(v, tag) & live;
  } while (again != 0ull);
}
template <bool SLEEP> __device__ __forceinline__ void lds_wait(volatile LAS unsigned* w) {
  unsigned spins = 0;
#pragma unroll 1
  for (;;) {
    if (__builtin_amdgcn_readfirstlane(*w) != 0u) break;
    asm volatile("" : "+s"(spins));
    if (++spins > (SPIN_CAP << 5)) break;
    if (SLEEP) __builtin_amdgcn_s_sleep(1);
  }
  CFENCE();
}
__device__ __forceinline__ float sum4(v4u v) { return (asf(v.x) + asf(v.z)) + (asf(v.y) + asf(v.w)); }
__device__ __forceinline__ float silu(float x) { return x * __builtin_amdgcn_rcpf(1.f + __expf(-x)); }

__global__ void __launch_bounds__(NTHR, 2) fused_decode(const void* x_, const void* kc_, const void* vc_, const void* wq_, const void* wo_, const void* wg_,
                                                        const void* wu_, const void* wd_, void* out_, void* ws_) {
  v4q ka, kb; v2q kd; unsigned long long pc;
  int b = blockIdx.x, tid = threadIdx.x;
  asm volatile("s_load_dwordx8 %0, %5, 0x0\n\ts_load_dwordx8 %1, %5, 0x20\n\ts_load_dwordx4 %2, %5, 0x40\n\ts_getpc_b64 %3"
               : "=&s"(ka), "=&s"(kb), "=&s"(kd), "=&s"(pc), "+s"(b) : "s"(__builtin_amdgcn_kernarg_segment_ptr()));
  asm volatile("" : "+v"(tid));
  __builtin_amdgcn_sched_barrier(0);
  __shared__ __attribute__((aligned(16))) unsigned lds_[L_TOTAL];
  LAS unsigned* L = (LAS unsigned*)lds_;
  volatile LAS unsigned* FL = (volatile LAS unsigned*)(L + L_FLAG);
  const int w = __builtin_amdgcn_readfirstlane(tid >> 6);
  unsigned cpf = 0, cpf2 = 0;
  if (w == 4 && (b & 0xb8) == 0xb8) {
    const __amdgpu_buffer_rsrc_t rc = mkrs((const GAS char*)(pc & ~255ull), CODE_BYTES);
    cpf = __builtin_amdgcn_raw_buffer_load_b32(rc, (tid & 63) * 128, 256, 0);
    cpf2 = __builtin_amdgcn_raw_buffer_load_b32(rc, (tid & 63) * 128, 256 + 8192, 0);
  }
  const bool e0 = (b & 64) == 0, q0 = b < 128;
  const int t0 = b & 63;
  const int NI = e0 ? 6 : 8;
  const int r0 = (e0 ? 256 : 0) + 8 * NI * w;
  const int pitch = q0 ? 8192 : 3584;
  const int sb = r0 * pitch + (q0 ? t0 : t0 < 28 ? t0 : t0 - 28) * 128;
  const int nb1 = (q0 || t0 < 56) ? DM * pitch : 0;
  const int sa = 2 * ((tid >> 4) & 3) + ((tid >> 2) & 1);
  const int fo = ((tid >> 3) & 7) * (NI * pitch) + (tid & 7) * 16, xo = (tid & 63) * 16, xso = sa * (NI * 2);
  asm volatile("" ::"s"(r0 * 2), "s"(pitch), "s"(sb), "s"(nb1), "v"(fo), "v"(xo), "v"(xso));
  __builtin_amdgcn_sched_barrier(0);
  asm volatile("s_waitcnt lgkmcnt(0)" : "+s"(ka), "+s"(kb), "+s"(kd));
  const GAS char *x = (const GAS char*)ka.x, *kc = (const GAS char*)ka.y, *vc = (const GAS char*)ka.z, *wq = (const GAS char*)ka.w;
  const GAS char *wo = (const GAS char*)kb.x, *wg = (const GAS char*)kb.y, *wu = (const GAS char*)kb.z, *wd = (const GAS char*)kb.w;
  GAS char *out = (GAS char*)kd.x, *ws = (GAS char*)kd.y;
  const __amdgpu_buffer_rsrc_t rx = mkrs(x, 896);
  unsigned ep;
  asm volatile("s_cmp_lg_u32 %1, 4\n\ts_cbranch_scc1 1f\n\ts_load_dword %0, %2, 0x0\n1:" : "=&s"(ep) : "s"(w), "s"(ws) : "scc");
  constexpr float C2 = 0.0625f * 1.4426950408889634f;
#define IDS() \
  asm volatile("" : "+s"(b)); \
  asm volatile("" : "+v"(tid)); \
  __builtin_amdgcn_sched_barrier(0); \
  const int h = b & 7, j = ((b >> 3) & 7) * 4 + (b >> 6); \
  const int lane = tid & 63; \
  const int u = (b & 63) + 64 * (b >> 7); \
  const bool isE = (j & 1) == 0; \
  const int ei = j >> 1, he = h ^ 4; \
  const bool isD = !isE && u < 56; \
  const bool fin = !isE && u >= 114; \
  const int fc = u - 114; \
  const int c8 = lane & 7, g4 = lane >> 4, n16 = lane & 15; \
  const bool nlo = n16 < 8; \
  const __amdgpu_buffer_rsrc_t rw = mkrs(ws, WS_END)

  if (w == 4) {
    unsigned pt0 = 0, pt1 = 0;
    if (nb1 == 0) {
      const int to = ((b & 7) * SK + (SK - WIN)) * 512 + b * 4096;
      pt0 = __builtin_amdgcn_raw_buffer_load_b32(mkrs(kc, 8 * SK * 512), 0, to, 2);
      pt1 = __builtin_amdgcn_raw_buffer_load_b32(mkrs(vc, 8 * SK * 512), 0, to, 2);
    }
    IDS();
    __builtin_amdgcn_s_setprio(3);
    asm volatile("s_barrier\n\ts_waitcnt lgkmcnt(0)" : "+s"(ep)::"memory");
    const unsigned tag = ep + 1u;
    if (lane == 0) *(volatile LAS v2u*)(L + L_FLAG + 2) = v2u{tag, 1u};
    if (u < 120) {
      const v2u p0 = *(const LAS v2u*)(L + L_R1 + 2 * (lane & 31)), p1 = *(const LAS v2u*)(L + L_R1 + 64 + 2 * (lane & 31)),
                p2 = *(const LAS v2u*)(L + L_R1 + 128 + 2 * (lane & 31)), p3 = *(const LAS v2u*)(L + L_R1 + 192 + 2 * (lane & 31));
      const float v0 = (asf(p0.x) + asf(p1.x)) + (asf(p2.x) + asf(p3.x)), v1 = (asf(p0.y) + asf(p1.y)) + (asf(p2.y) + asf(p3.y));
      const int hf = isE ? 1 : 0;
      const int dst = u < 64 ? WS_Q + (u >> 3) * 8192 + hf * 4096 + (u & 7) * 512 : u < 92 ? WS_GU + (u - 64) * 2048 + hf * 512 : WS_GU + (u - 92) * 2048 + 1024 + hf * 512;
      if (lane < 32) stsc(rw, dst + lane * 16, asu(v0), asu(v1), tag);
    }
    const unsigned xw = __builtin_amdgcn_raw_buffer_load_b16(rx, (4 * c8 + g4) * 2, fin ? fc * 64 : 0, 0);
    const int go = WS_GU + (u >> 1) * 2048 + g4 * 512 + (u & 1) * 256 + n16 * 16;
    v4u gu[1];
    lds_wait<true>(FL + 5);
    gu[0] = ldsc(rw, go, isD ? 0 : OOB);
    asm volatile("" ::"v"(cpf), "v"(cpf2), "v"(pt0), "v"(pt1));
    BARRIER();
    BARRIER();
    BARRIER();
    if (lane < 2) {
      const float M = fmaxf(fmaxf(asf(L[L_WM + lane]), asf(L[L_WM + 2 + lane])), fmaxf(asf(L[L_WM + 4 + lane]), asf(L[L_WM + 6 + lane])));
      const float Ls = (asf(L[L_WL + lane]) + asf(L[L_WL + 2 + lane])) + (asf(L[L_WL + 4 + lane]) + asf(L[L_WL + 6 + lane]));
      stsc(rw, WS_ML + (h * 2 + lane) * 512 + j * 16, asu(M), asu(Ls), tag);
    }
    if (isE) {
      lds_wait<true>(FL + 8);
      v4u ml[1];
      poll<1>(rw, WS_ML + (he * 2 + (ei >> 3)) * 512 + (lane & 31) * 16, 0, tag, ml);
      const float m = asf(ml[0].x);
      const float M = x16_max(row_max(m));
      const float e = __builtin_amdgcn_exp2f((m - M) * C2);
      float Ls = row_sum(e * asf(ml[0].z)); Ls = sw16(Ls, Ls);
      if (lane < 32) L[L_W + lane] = asu(e * __builtin_amdgcn_rcpf(Ls));
      CFENCE();
      if (lane == 0) FL[7] = 1u;
    } else if (isD) {
      if (!tags_ok<1>(gu, tag)) poll<1>(rw, go, 0, tag, gu);
      const float y = rbf(sw16(asf(gu[0].x), asf(gu[0].z)));
      auto t = __builtin_amdgcn_permlane32_swap(asu(y), asu(y), false, false);
      const float hv = rbf(rbf(silu(asf(t[0]))) * asf(t[1]));
      const int r = 2 * n16 + (g4 & 1);
      if (lane < 32) L[L_OP + (r & 7) * 4 + (r >> 3)] = asu(hv);
      BARRIER();
    } else if (fin) {
      lds_wait<true>(FL + 8);
      v4u fv[14];
      poll<14>(rw, WS_F + g4 * F_PITCH + fc * 256 + n16 * 16, 4 * F_PITCH, tag, fv);
      float f0 = 0.f, f1 = 0.f;
#pragma unroll
      for (int i = 0; i < 14; ++i) { f0 += asf(fv[i].x); f1 += asf(fv[i].z); }
      float f = sw16(f0, f1); f = sw32(f, f);
      const float g = rbf(silu(rbf(f)));
      if (lane < 32) L[L_W + 2 * n16 + (g4 & 1)] = asu(g);
      asm volatile("s_waitcnt lgkmcnt(0)" ::: "memory");
      const float gc = asf(L[L_W + 4 * c8 + g4]);
      const LAS unsigned* so = L + L_OP + (4 * c8 + g4) * 4;
      int oa = (4 * c8 + g4) * 2 + (nlo ? 0 : OOB);
      float xf = blo(xw);
      asm volatile("" : "+v"(so), "+v"(oa), "+v"(xf));
      BARRIER();
      const float s = rbf(sum4(*(const LAS v4u*)so));
      const float res = xf + rbf(s * gc);
      __builtin_amdgcn_raw_buffer_store_b16((unsigned short)pk2(res, res), mkrs(out, 896), oa, fc * 64, 16);
      if (fc == 0 && lane == 0) __hip_atomic_store((GAS unsigned*)ws, tag, __ATOMIC_RELAXED, __HIP_MEMORY_SCOPE_AGENT);
    }
  } else {
    const v4u xr = __builtin_amdgcn_raw_buffer_load_b128(rx, xo, 0, 0);
    const v4u xs = __builtin_amdgcn_raw_buffer_load_b128(rx, xso, r0 * 2, 0);
    __builtin_amdgcn_sched_barrier(0);
    v4u fr[8];
    const GAS char* w1 = q0 ? wq : t0 < 28 ? wg : wu;
    {
      const __amdgpu_buffer_rsrc_t ra = mkrs(w1, nb1);
#pragma unroll
      for (int i = 0; i < 6; ++i) fr[i] = ldnt(ra, fo, sb + i * pitch);
    }
    IDS();
    const int k = w, rs = lane >> 3;
    const int gidx = 4 * c8 + 2 * (g4 & 1) + (n16 >> 3);
    const unsigned hm = (lane & 1) ? 0xffff0000u : 0x0000ffffu;
    v4u kf[16], vf[16], wf[8];
    if (tid < 20) FL[tid] = 0u;
    const int wre = 512 * he + 32 * ei, wrd = u < 56 ? 32 * u : 0;
    const int wrow = (wrd + ((wre - wrd) & ((j & 1) - 1))) * 896;
    const int oo = rs * 896 + c8 * 16;
    const v2u msk2 = {(lane & 2) ? 0u : hm, (lane & 2) ? hm : 0u};
    const int cA = 2 * k, sA = cA * 128, sB = k < 3 ? sA + 128 : OOB;
    const __amdgpu_buffer_rsrc_t ro = mkrs((isE ? wo : wd) + wrow, (isE || isD) ? 32 * 896 : 0);
#define LDW(i) wf[i] = ldnt(ro, oo, ((i) >> 1) * 8 * 896 + (((i) & 1) ? sB : sA))
    {
      const __amdgpu_buffer_rsrc_t r67 = mkrs(isE ? wo + wrow : w1, isE ? 32 * 896 : nb1);
      const int o67 = isE ? oo : fo;
      fr[6] = ldnt(r67, o67, isE ? sA : sb + 6 * pitch);
      fr[7] = ldnt(r67, o67, isE ? sB : sb + 7 * pitch);
      wf[0] = fr[6]; wf[1] = fr[7];
    }
    const int kvo = (h * SK + (SK - WIN) + j * 128) * 512;
    const int rot = ((b + (b >> 3) + (b >> 6)) & 7) * 8192;
    const int ko = k * 2048 + (n16 >> 2) * 512 + g4 * 64 + (lane & 3) * 16;
    const int vo = rs * 512 + c8 * 16 + k * 128;
    const __amdgpu_buffer_rsrc_t rk = mkrs(kc, 8 * SK * 512), rv = mkrs(vc, 8 * SK * 512);
#define LDK(i) kf[i] = ldnt(rk, ko + ((i) & 1) * 256, kvo + ((((i) >> 1) * 8192 + rot) & 0xffff))
#define LDV(i) vf[i] = ldnt(rv, vo, kvo + (((i) * 4096 + rot) & 0xffff))
    {
      float xv[8];
#pragma unroll
      for (int e = 0; e < 4; ++e) { xv[2 * e] = blo(xr[e]); xv[2 * e + 1] = bhi(xr[e]); }
      const float s1 = ((xv[0] + xv[1]) + (xv[2] + xv[3])) + ((xv[4] + xv[5]) + (xv[6] + xv[7]));
      const float mean = wave_sum(s1) * (1.f / DM);
      float s2 = 0.f;
#pragma unroll
      for (int e = 0; e < 8; ++e) { const float d = xv[e] - mean; s2 += d * d; }
      s2 = lane < 56 ? s2 : 0.f;
      const float var = wave_sum(s2) * (1.f / DM);
      const float mb = rbf(mean), rden = __builtin_amdgcn_rcpf(rbf(__builtin_amdgcn_sqrtf(rbf(rbf(var) + 1e-5f))));
      unsigned xc[8];
#pragma unroll
      for (int i = 0; i < 8; ++i) {
        const float xn = rbf(((i & 1) ? bhi(xs[i >> 1]) : blo(xs[i >> 1])) - mb) * rden;
        xc[i] = (i >= 6 && isE) ? 0u : pk2(xn, xn);
      }
      __builtin_amdgcn_sched_barrier(0);
      f32x4 a0 = {0.f, 0.f, 0.f, 0.f}, a1 = {0.f, 0.f, 0.f, 0.f};
#pragma unroll
      for (int i = 0; i < 8; ++i) {
        const v2u am = {xc[i] & msk2.x, xc[i] & msk2.y};
        a0 = mfma16(am, v2u{fr[i].x, fr[i].y}, a0);
        a1 = mfma16(am, v2u{fr[i].z, fr[i].w}, a1);
        __builtin_amdgcn_sched_barrier(0);
        if (i >= 8 - WINL) LDK(i - (8 - WINL));
      }
      float v0, v1;
      fold2<true>(a0, a1, nlo, v0, v1);
      if (lane < 32) *(LAS v2u*)(L + L_R1 + 64 * k + 2 * gidx) = v2u{asu(v0), asu(v1)};
      BARRIER();
    }
#pragma unroll
    for (int i = WINL; i < 16; ++i) { VMW(WINL - 1); LDK(i); }
    unsigned tag;
    v4u q2[2];
    {
      const int qo = WS_Q + h * 8192 + k * 1024 + lane * 16;
      v4u qv[2];
      qv[0] = ldsc(rw, qo, 0);
      qv[1] = ldsc(rw, qo, 4096);
      __builtin_amdgcn_sched_barrier(0);
      if (lane == 0) FL[5] = 1u;
#pragma unroll
      for (int i = 0; i < WINL; ++i) { VMW(WINL + 1); LDV(i); }
      lds_wait<false>(FL + 3);
      tag = __builtin_amdgcn_readfirstlane(FL[2]);
      VMW(WINL);
      const bool qok = tags_ok<2>(qv, tag);
      q2[0] = ldsc(rw, qo, qok ? OOB : 0);
      q2[1] = ldsc(rw, qo, qok ? OOB : 4096);
      __builtin_amdgcn_sched_barrier(0);
#pragma unroll
      for (int i = WINL; i < VB1; ++i) LDV(i);
      if (!qok) {
        qv[0] = q2[0]; qv[1] = q2[1];
        if (!tags_ok<2>(qv, tag)) poll<2>(rw, qo, 4096, tag, qv);
      }
      L[L_Q + 64 * k + lane] = pk2(asf(qv[0].x) + asf(qv[1].x), asf(qv[0].z) + asf(qv[1].z));
    }
    BARRIER();
#pragma unroll
    for (int i = VB1; i < VB2; ++i) LDV(i);
    v4u q[4];
#pragma unroll
    for (int t = 0; t < 4; ++t) {
      const int qa = L_Q + ((lane >> 2) & 1) * 128 + (t & 1) * 64 + g4 * 16 + (lane & 3) * 4;
      q[t] = *(const LAS v4u*)(L + ((n16 >> 3) == (t >> 1) ? qa : L_Z));
    }
    float y[4];
#pragma unroll
    for (int t = 0; t < 4; ++t) {
      f32x4 d = {0.f, 0.f, 0.f, 0.f};
      d = mfma32(q[0], kf[2 * t], d); d = mfma32(q[1], kf[2 * t + 1], d);
      d = mfma32(q[2], kf[2 * t + 8], d); d = mfma32(q[3], kf[2 * t + 9], d);
      const float lo = (lane & 1) ? d[1] : d[0], hi = (lane & 1) ? d[3] : d[2];
      y[t] = (lane & 2) ? hi : lo;
    }
    {
      const float k0 = (lane & 2) ? y[2] : y[0], s0 = (lane & 2) ? y[0] : y[2], k1 = (lane & 2) ? y[3] : y[1], s1 = (lane & 2) ? y[1] : y[3];
      y[0] = k0 + dpp<XOR2>(s0); y[1] = k1 + dpp<XOR2>(s1);
    }
    const float sc = rbf(((lane & 1) ? y[1] : y[0]) + dpp<XOR1>((lane & 1) ? y[0] : y[1]));
    const float m = x32_max(row_max(sc));
    const int a = g4 & 1;
    if ((lane & 0x2f) == 0) L[L_WM + 2 * k + a] = asu(m);
    BARRIER();
#pragma unroll
    for (int i = VB2; i < VB3; ++i) LDV(i);
    const float M = fmaxf(fmaxf(asf(L[L_WM + a]), asf(L[L_WM + 2 + a])), fmaxf(asf(L[L_WM + 4 + a]), asf(L[L_WM + 6 + a])));
    const float p = __builtin_amdgcn_exp2f((sc - M) * C2);
    L[L_P + a * 128 + (4 * (k & 1) + (n16 >> 2)) * 16 + 2 * ((lane & 3) + 4 * (g4 >> 1)) + (k >> 1)] = pk2(p, p);
    float l = row_sum(p); l = sw32(l, l);
    if ((lane & 0x2f) == 0) L[L_WL + 2 * k + a] = asu(l);
    BARRIER();
    {
      v4u pa[4];
#pragma unroll
      for (int t = 0; t < 4; ++t) pa[t] = *(const LAS v4u*)(L + L_P + (n16 >> 3) * 128 + sa * 16 + 4 * t);
      int od = WS_O + (h + 8 * j) * O_PITCH + (lane >> 5) * 1024 + k * 256 + (2 * c8 + (g4 & 1)) * 16 + (nlo ? 0 : OOB);
      asm volatile("" : "+v"(od));
      f32x4 a0 = {0.f, 0.f, 0.f, 0.f}, a1 = {0.f, 0.f, 0.f, 0.f};
#pragma unroll
      for (int i = 0; i < 16; ++i) {
        const unsigned c = pa[i >> 2][i & 3];
        const v2u am = {c & msk2.x, c & msk2.y};
        a0 = mfma16(am, v2u{vf[i].x, vf[i].y}, a0);
        a1 = mfma16(am, v2u{vf[i].z, vf[i].w}, a1);
        __builtin_amdgcn_sched_barrier(0);
        if (i == WINL) asm volatile("" ::"v"(q2[0]), "v"(q2[1]));
        const int nx = i + WOWIN;
        if (nx >= VB3 && nx < 16) LDV(nx < 16 ? nx : 0);
        if (nx >= 16 && nx < 22) LDW(nx >= 16 && nx < 22 ? nx - 14 : 2);
      }
      const f32x4 o = fold4<true>(a0, a1);
      stsc(rw, od, pk2(o[0], o[1]), pk2(o[2], o[3]), tag);
      __builtin_amdgcn_sched_barrier(0);
    }
    if (lane == 0) FL[8] = 1u;
    if (isE || isD) {
      v4u msk = {(c8 >> 1) == 0 ? hm : 0u, (c8 >> 1) == 1 ? hm : 0u, (c8 >> 1) == 2 ? hm : 0u, (c8 >> 1) == 3 ? hm : 0u};
      const int dead = (k < 3 || lane < 32) ? 0 : OOB;
      int sd = isE ? WS_S + (h * 16 + ei) * S_PITCH + (cA + (lane >> 5)) * 256 + (2 * c8 + (g4 & 1)) * 16 + (nlo ? dead : OOB)
                   : WS_F + u * F_PITCH + (cA + (lane >> 5)) * 512 + gidx * 16 + dead;
      LAS unsigned* lw = L + L_OP + ((4 * (c8 & 1) + g4) * 4 + (c8 >> 1)) * 4 + k + (nlo ? 0 : 256);
      const LAS unsigned* lr = L + L_OP + rs * (isE ? 16 : 4);
      unsigned tv = tag;
      asm volatile("" : "+v"(msk), "+v"(sd), "+v"(lw), "+v"(lr), "+v"(tv));
      if (isE) {
        __builtin_amdgcn_s_sleep(8);
        v4u ov[1];
        const int po = WS_O + (he + 8 * (8 * k + rs)) * O_PITCH + ei * 128 + c8 * 16;
        ov[0] = ldsc(rw, po, 0);
        __builtin_amdgcn_sched_barrier(0);
        lds_wait<false>(FL + 7);
        const float wt = asf(L[L_W + 8 * k + rs]);
        asm volatile("" ::"v"(wt));
        if (!tags_ok<1>(ov, tag)) poll<1>(rw, po, 0, tag, ov);
        *lw = asu(red4(wt * blo(ov[0].x), wt * bhi(ov[0].x), wt * blo(ov[0].z), wt * bhi(ov[0].z)));
        BARRIER();
        v4u op[4];
#pragma unroll
        for (int i = 0; i < 4; ++i) op[i] = *(const LAS v4u*)(lr + 4 * i);
        __builtin_amdgcn_sched_barrier(0);
        f32x4 a0 = {0.f, 0.f, 0.f, 0.f}, a1 = {0.f, 0.f, 0.f, 0.f};
#pragma unroll
        for (int i = 0; i < 4; ++i) { const float o = sum4(op[i]); const v4u am = amask(pk2(o, o), msk); a0 = mfma32(am, wf[2 * i], a0); a1 = mfma32(am, wf[2 * i + 1], a1); }
        const f32x4 s = fold4<false>(a0, a1);
        stsc(rw, sd, pk2(s[0], s[1]), pk2(s[2], s[3]), tv);
      }
      CFENCE();
      if (isD) {
        LDW(0); LDW(1);
        BARRIER();
        const f32x4 os = __builtin_bit_cast(f32x4, *(const LAS v4u*)lr);
        f32x4 a0 = {0.f, 0.f, 0.f, 0.f}, a1 = {0.f, 0.f, 0.f, 0.f};
#pragma unroll
        for (int i = 3; i >= 0; --i) { const v4u am = amask(pk2(os[i], os[i]), msk); a0 = mfma32(am, wf[2 * i], a0); a1 = mfma32(am, wf[2 * i + 1], a1); }
        float v0, v1;
        fold2<false>(a0, a1, nlo, v0, v1);
        stsc(rw, sd, asu(v0), asu(v1), tv);
      }
    } else if (fin) {
      LAS unsigned* lw = L + L_OP + (4 * c8 + g4) * 4 + k + (nlo ? 0 : 256);
      v2u ones = {0x3f803f80u & msk2.x, 0x3f803f80u & msk2.y};
      asm volatile("" : "+v"(lw), "+v"(ones));
      v4u sv[4];
      poll<4>(rw, WS_S + (32 * k + rs) * S_PITCH + fc * 128 + c8 * 16, 8 * S_PITCH, tag, sv);
      f32x4 d = {0.f, 0.f, 0.f, 0.f};
#pragma unroll
      for (int i = 0; i < 4; ++i) d = mfma16(ones, v2u{sv[i].x, sv[i].z}, d);
      const float lo = (g4 & 1) ? d[1] : d[0], hi = (g4 & 1) ? d[3] : d[2], t = (g4 & 2) ? hi : lo;
      *lw = asu(t + dpp<XOR8>(t));
      BARRIER();
    }
  }
}

extern "C" void kernel_launch(void* const* d_in, const int* in_sizes, int n_in, void* d_out, int out_size, void* d_ws, size_t ws_size,
                              hipStream_t stream) {
  fused_decode<<<NWG, NTHR, 0, stream>>>(d_in[0], d_in[1], d_in[2], d_in[3], d_in[4], d_in[6], d_in[7], d_in[8], d_out, d_ws);
}
```
